```python
import torch

def _softmax(x, axis=-1):
    return torch.softmax(x, dim=axis)

def _gelu(x):
    return torch.nn.functional.gelu(x, approximate='tanh')

def _silu(x):
    return torch.nn.functional.silu(x)

def _sigmoid(x):
    return torch.sigmoid(x)

def _layernorm(x, axis=-1, eps=1e-05):
    m = x.mean(dim=axis, keepdim=True)
    v = x.var(dim=axis, keepdim=True, unbiased=False)
    return (x - m) / torch.sqrt(v + eps)

def _scatter_add(tgt, idx, src, axis=0):
    return tgt.clone().index_add_(axis, idx, src)

def _segment_sum(src, seg, n):
    out = torch.zeros((n,) + src.shape[1:], dtype=src.dtype)
    return out.index_add_(0, seg, src)

def _pad(x, pw):
    flat = tuple((v for pair in reversed(pw) for v in pair))
    return torch.nn.functional.pad(x, flat)
kernel_name = 'gated_mix__memory_aug_recurrent_mem_tr__moe_arch_mod__prompt'

def setup_inputs():

    _w = lambda a: (lambda t: t.to(torch.bfloat16) if t.is_floating_point() else t)(torch.as_tensor(a))

    def _A_setup():
        B, S, Dm = (1, 2048, 1024)
        H, Dh = (32, 128)
        M, seg = (8, 32)
        seg = min(seg, S)
        S = S // seg * seg
        return {'x': _w((lambda *s: torch.randn(*s, dtype=torch.bfloat16) * 0.05)(B, S, Dm)), 'mem0': _w((lambda *s: torch.randn(*s, dtype=torch.bfloat16) * 0.8)(B, M, Dm)), 'Wq': _w((lambda *s: torch.randn(*s, dtype=torch.bfloat16) * 3.2)(Dm, H, Dh) * Dm ** (-0.5)), 'Wk': _w((lambda *s: torch.randn(*s, dtype=torch.bfloat16) * 3.2)(Dm, H, Dh) * Dm ** (-0.5)), 'Wv': _w((lambda *s: torch.randn(*s, dtype=torch.bfloat16) * 0.8)(Dm, H, Dh) * Dm ** (-0.5)), 'Wo': _w((lambda *s: torch.randn(*s, dtype=torch.bfloat16) * 0.8)(H, Dh, Dm) * (H * Dh) ** (-0.5)), 'seg_len': seg}
    _A_in = _A_setup()

    def _B_setup():
        B, S, D, F, C = (1, 2048, 1024, 3584, 2)
        return {'x': _w((lambda *s: torch.randn(*s, dtype=torch.bfloat16) * 0.05)(B, S, D)), 'w_r': _w((lambda *s: torch.randn(*s, dtype=torch.bfloat16) * 1.6)(D)), 'w_gate': _w((lambda *s: torch.randn(*s, dtype=torch.bfloat16) * 0.2)(D, F)), 'w_up': _w((lambda *s: torch.randn(*s, dtype=torch.bfloat16) * 0.2)(D, F)), 'w_down': _w((lambda *s: torch.randn(*s, dtype=torch.bfloat16) * 0.2)(F, D))}
    _B_in = _B_setup()
    return {'A_x': _A_in['x'], 'A_mem0': _A_in['mem0'], 'A_Wq': _A_in['Wq'], 'A_Wk': _A_in['Wk'], 'A_Wv': _A_in['Wv'], 'A_Wo': _A_in['Wo'], 'A_seg_len': _A_in['seg_len'], 'B_x': _B_in['x'], 'B_w_r': _B_in['w_r'], 'B_w_gate': _B_in['w_gate'], 'B_w_up': _B_in['w_up'], 'B_w_down': _B_in['w_down'], 'gate_w': _w((lambda *s: torch.randn(*s, dtype=torch.bfloat16) * 0.05)(1024) * 16.0)}

def reference(A_x, A_mem0, A_Wq, A_Wk, A_Wv, A_Wo, A_seg_len, B_x, B_w_r, B_w_gate, B_w_up, B_w_down, gate_w):

    def _A_ref(x, mem0, Wq, Wk, Wv, Wo, seg_len):
        B, S, Dm = x.shape
        H, Dh = (Wq.shape[1], Wq.shape[2])
        M = mem0.shape[1]
        scale = Dh ** (-0.5)
        n_seg = S // seg_len
        L = M + seg_len
        idx = torch.arange(L)
        mask = (idx[:, None] < M) | (idx[:, None] >= idx[None, :])
        mem = mem0
        outs = []
        for s in range(n_seg):
            xs = x[:, s * seg_len:(s + 1) * seg_len, :].clone()
            h = torch.concatenate([mem, xs], axis=1)
            q = torch.einsum('btd,dhe->bhte', h, Wq)
            k = torch.einsum('btd,dhe->bhte', h, Wk)
            v = torch.einsum('btd,dhe->bhte', h, Wv)
            scores = torch.einsum('bhqe,bhke->bhqk', q, k) * scale
            scores = torch.where(mask[None, None], scores, -1000000000.0)
            p = _softmax(scores, axis=-1)
            o = torch.einsum('bhqk,bhke->bhqe', p, v)
            o = torch.einsum('bhte,hed->btd', o, Wo)
            mem = o[:, :M, :].clone()
            outs.append(o[:, M:, :])
        return torch.concatenate(outs, axis=1)

    def _B_ref(x, w_r, w_gate, w_up, w_down):
        B, S, D = x.shape
        C = 2
        r = x @ w_r
        idx = torch.argsort(-r, axis=-1)[:, :C]
        idx_e = (lambda x, n, axis: torch.repeat_interleave(x, n, dim=axis))(idx.reshape(B, C, 1), D, axis=2)
        x_sel = (lambda x, i, axis: torch.take_along_dim(x, i, dim=axis))(x, idx_e, axis=1)
        g = _silu(x_sel @ w_gate)
        u = x_sel @ w_up
        y = g * u @ w_down
        r_sel = _sigmoid((lambda x, i, axis: torch.take_along_dim(x, i, dim=axis))(r, idx, axis=1))
        y = y * r_sel.reshape(B, C, 1)
        b_off = (torch.arange(B) * S).reshape(B, 1)
        flat_idx = (idx + b_off).reshape(B * C)
        zero = (x * 0.0).reshape(B * S, D)
        upd = _scatter_add(zero, flat_idx, y.reshape(B * C, D), axis=0)
        return x + upd.reshape(B, S, D)
    x = A_x
    g = _sigmoid((x * gate_w).sum(-1, keepdims=True))
    a = _A_ref(x, A_mem0, A_Wq, A_Wk, A_Wv, A_Wo, A_seg_len)
    b = _B_ref(x, B_w_r, B_w_gate, B_w_up, B_w_down)
    return g * a + (1 - g) * b
```

```cpp
#include <hip/hip_runtime.h>
#include <hip/hip_bf16.h>
#include <stdint.h>

#define GAS __attribute__((address_space(1)))
#define LAS __attribute__((address_space(3)))
typedef unsigned v4u __attribute__((ext_vector_type(4)));
typedef unsigned v2u __attribute__((ext_vector_type(2)));
typedef unsigned v8u __attribute__((ext_vector_type(8)));
typedef float f32x4 __attribute__((ext_vector_type(4)));
typedef __bf16 bfx8 __attribute__((ext_vector_type(8)));
typedef __bf16 bfx16 __attribute__((ext_vector_type(16)));
typedef short s16x4 __attribute__((ext_vector_type(4)));
typedef GAS unsigned gu32;
typedef unsigned short u16;
typedef unsigned char u8;
#define RLX_AGENT __ATOMIC_RELAXED, __HIP_MEMORY_SCOPE_AGENT

namespace {

constexpr int SEQ = 2048, DM = 1024, NH = 32, HD = 4096, NSEG = 64, FF = 3584;
constexpr int NCHAIN = 128, NWORK = 128, NWG = 256, NTHR = 512;
constexpr unsigned SENT = 0xFFFFFFFFu;
constexpr int AUX_SC1 = 16;

constexpr unsigned CTRL_BYTES = 65536;
constexpr int W_TMO = 0, W_ARM = 64  , W_RG = 640, W_M1 = 704, W_M2 = 768, W_TOP = 832  ,
              W_T2 = 1024  , W_PROG = 2048  , W_FKV = 4096  , W_FQ = 6144  ;
constexpr unsigned MEM_SZ = 16384, QKV_SZ = 6144, PART_SZ = 128 * 8192;
constexpr unsigned OFF_MEM = CTRL_BYTES;
constexpr unsigned OFF_QKV = OFF_MEM + 65 * MEM_SZ;
constexpr unsigned OFF_PART = OFF_QKV + NSEG * NH * QKV_SZ;
constexpr unsigned OFF_ARM_END = OFF_PART + 2 * PART_SZ;
constexpr unsigned OFF_KX = OFF_ARM_END;
constexpr unsigned OFF_VX = OFF_KX + NSEG * NH * 8192;
constexpr unsigned OFF_QX = OFF_VX + NSEG * NH * 8192;
constexpr unsigned OFF_AX = OFF_QX + NSEG * NH * 8192;
constexpr unsigned OFF_R = OFF_AX + SEQ * HD * 2;
constexpr unsigned OFF_G = OFF_R + SEQ * 4;
constexpr unsigned OFF_HID = OFF_G + SEQ * 4;
constexpr unsigned OFF_YP = OFF_HID + 2 * FF * 4;
constexpr unsigned WS_END = OFF_YP + 14 * 2 * DM * 4;
constexpr int LDS_BYTES = 160 * 1024;
constexpr unsigned SPIN_CAP = 1u << 18;

#define W_A 500
#define W_B 250
#define W_C 350

struct Params {
  const u16 *x, *mem0, *Wq, *Wk, *Wv, *Wo, *wr, *wg, *wu, *wd, *gw;
  u16* out;
  u8* ws;
};

__device__ __forceinline__ float u2f(unsigned u) { return __builtin_bit_cast(float, u); }
__device__ __forceinline__ unsigned f2u(float f) { return __builtin_bit_cast(unsigned, f); }
__device__ __forceinline__ float bf2f(unsigned b) { return u2f(b << 16); }
__device__ __forceinline__ float bflo(unsigned d) { return u2f(d << 16); }
__device__ __forceinline__ float bfhi(unsigned d) { return u2f(d & 0xFFFF0000u); }
__device__ __forceinline__ unsigned pk2(float lo, float hi) {
  typedef __bf16 bfx2 __attribute__((ext_vector_type(2)));
  bfx2 v; v.x = (__bf16)lo; v.y = (__bf16)hi;
  return __builtin_bit_cast(unsigned, v);
}
__device__ __forceinline__ float bfr(float f) { return (float)(__bf16)f; }
__device__ __forceinline__ unsigned canon(unsigned d) { return d == SENT ? 0x7FC07FC0u : d; }
__device__ __forceinline__ unsigned canonf(float f) { unsigned d = f2u(f); return d == SENT ? 0x7FC00000u : d; }
__device__ __forceinline__ f32x4 mfma32(v4u a, v4u b, f32x4 c) {
  return __builtin_amdgcn_mfma_f32_16x16x32_bf16(__builtin_bit_cast(bfx8, a), __builtin_bit_cast(bfx8, b), c, 0, 0, 0);
}
__device__ __forceinline__ f32x4 mfma16(v2u a, v2u b, f32x4 c) { return mfma32(v4u{a.x, a.y, 0u, 0u}, v4u{b.x, b.y, 0u, 0u}, c); }
__device__ __forceinline__ f32x4 smf64(v4u a, v8u b, f32x4 c, int idx) {
  return __builtin_amdgcn_smfmac_f32_16x16x64_bf16(__builtin_bit_cast(bfx8, a), __builtin_bit_cast(bfx16, b), c, idx, 0, 0);
}
__device__ __forceinline__ v2u trread(unsigned lds_addr) {
  return __builtin_bit_cast(v2u, __builtin_amdgcn_ds_read_tr16_b64_v4i16((LAS s16x4*)(size_t)lds_addr));
}
__device__ __forceinline__ float swap32_add(float a, float b) {
  auto r = __builtin_amdgcn_permlane32_swap(f2u(a), f2u(b), false, false);
  return u2f(r[0]) + u2f(r[1]);
}
__device__ __forceinline__ float xmax16(float a) { auto r = __builtin_amdgcn_permlane16_swap(f2u(a), f2u(a), false, false); return fmaxf(u2f(r[0]), u2f(r[1])); }
__device__ __forceinline__ float xmax32(float a) { auto r = __builtin_amdgcn_permlane32_swap(f2u(a), f2u(a), false, false); return fmaxf(u2f(r[0]), u2f(r[1])); }
__device__ __forceinline__ float xadd16(float a) { auto r = __builtin_amdgcn_permlane16_swap(f2u(a), f2u(a), false, false); return u2f(r[0]) + u2f(r[1]); }
__device__ __forceinline__ float xadd32(float a) { auto r = __builtin_amdgcn_permlane32_swap(f2u(a), f2u(a), false, false); return u2f(r[0]) + u2f(r[1]); }

#define CFENCE() asm volatile("" ::: "memory")
#define DRAIN_VM() asm volatile("s_waitcnt vmcnt(0)" ::: "memory")
#define WAIT_LGKM() asm volatile("s_waitcnt lgkmcnt(0)" ::: "memory")
__device__ __forceinline__ void wgbar() { WAIT_LGKM(); __builtin_amdgcn_s_barrier(); CFENCE(); }

typedef __amdgpu_buffer_rsrc_t rsrc_t;
__device__ __forceinline__ v4u ld16(rsrc_t rs, unsigned off) { return __builtin_amdgcn_raw_buffer_load_b128(rs, off, 0, AUX_SC1); }
__device__ __forceinline__ v2u ld8(rsrc_t rs, unsigned off) { return __builtin_amdgcn_raw_buffer_load_b64(rs, off, 0, AUX_SC1); }
__device__ __forceinline__ unsigned ld4(rsrc_t rs, unsigned off) { return __builtin_amdgcn_raw_buffer_load_b32(rs, off, 0, AUX_SC1); }
__device__ __forceinline__ void st16(rsrc_t rs, unsigned off, v4u v) { __builtin_amdgcn_raw_buffer_store_b128(v, rs, off, 0, AUX_SC1); }
__device__ __forceinline__ void st8(rsrc_t rs, unsigned off, v2u v) { __builtin_amdgcn_raw_buffer_store_b64(v, rs, off, 0, AUX_SC1); }
__device__ __forceinline__ void st4(rsrc_t rs, unsigned off, unsigned v) { __builtin_amdgcn_raw_buffer_store_b32(v, rs, off, 0, AUX_SC1); }
__device__ __forceinline__ unsigned umax4(v4u v) { return max(max(v.x, v.y), max(v.z, v.w)); }

template <int N> __device__ __forceinline__ void delay_cyc() {
  if (N >= 64) __builtin_amdgcn_s_sleep(N / 64);
#pragma unroll
  for (int i = 0; i < (N % 64) / 16; ++i) asm volatile("s_nop 15");
}

__device__ __forceinline__ bool spin_fail(unsigned& spins, gu32* ctrl, unsigned code) {
  ++spins;
  if (spins > SPIN_CAP) { __hip_atomic_store(ctrl + W_TMO, 1u, RLX_AGENT); __hip_atomic_fetch_max(ctrl + W_TMO + 1, code, RLX_AGENT); return true; }
  return (spins & 255u) == 0u && __builtin_amdgcn_readfirstlane(__hip_atomic_load(ctrl + W_TMO, RLX_AGENT)) != 0u;
}
__device__ __forceinline__ void wait_ge(gu32* ctrl, int word, unsigned want, unsigned code, bool& dead) {
  if (dead) return;
  for (unsigned sp = 0;;) {
    if ((unsigned)__builtin_amdgcn_readfirstlane(__hip_atomic_load(ctrl + word, RLX_AGENT)) >= want) return;
    __builtin_amdgcn_s_sleep(4);
    if (spin_fail(sp, ctrl, code)) { dead = true; return; }
  }
}
__device__ __forceinline__ void wait_flags2(gu32* ctrl, int w0, int w1, unsigned code, bool& dead) {
  if (dead) return;
  for (unsigned sp = 0;;) {
    const unsigned long long a = __hip_atomic_load((GAS unsigned long long*)(ctrl + w0), RLX_AGENT);
    const unsigned long long b = __hip_atomic_load((GAS unsigned long long*)(ctrl + w1), RLX_AGENT);
    const unsigned long long m = a & b;
    if (__builtin_amdgcn_readfirstlane((unsigned)m & (unsigned)(m >> 32)) != 0u) return;
    __builtin_amdgcn_s_sleep(4);
    if (spin_fail(sp, ctrl, code)) { dead = true; return; }
  }
}
__device__ __forceinline__ void wait_armed(gu32* ctrl, int lane, bool& dead) {
  if (dead) return;
  for (unsigned sp = 0;;) {
    const unsigned v = __hip_atomic_load(ctrl + W_ARM + 64 * (lane & 7), RLX_AGENT);
    if (__all(v >= 32u)) return;
    __builtin_amdgcn_s_sleep(4);
    if (spin_fail(sp, ctrl, 0x10u)) { dead = true; return; }
  }
}
__device__ __forceinline__ void arm_share(rsrc_t rs, int b, int tid) {
  constexpr unsigned NCH = (OFF_ARM_END - OFF_MEM) / 16;
  const v4u s = {SENT, SENT, SENT, SENT};
  for (unsigned i = (unsigned)b * NTHR + tid; i < NCH; i += NWG * NTHR) st16(rs, OFF_MEM + i * 16, s);
}
__device__ __forceinline__ void arm_signal(gu32* ctrl, int b, int tid) {
  DRAIN_VM();
  wgbar();
  if (tid == 0) __hip_atomic_fetch_add(ctrl + W_ARM + 64 * (b & 7), 1u, RLX_AGENT);
}

#define GLDS16(src, dst) __builtin_amdgcn_global_load_lds((const GAS unsigned*)(src), (LAS unsigned*)(dst), 16, 0, 0)

__device__ __forceinline__ void slice_dma(const u16* src, int ld, LAS u8* dst, int lane) {
#pragma unroll
  for (int p = 0; p < 8; ++p)
    GLDS16(src + (size_t)(16 * p + (lane >> 2)) * ld + 8 * (lane & 3), dst + p * 1024 + lane * 16);
}
template <bool PERM>
__device__ __forceinline__ void slice_frags(unsigned base, int lane, v8u (&f)[2][2]) {
  const int gB = lane >> 4, q = (lane & 15) >> 2, p = lane & 3;
#pragma unroll
  for (int cc = 0; cc < 2; ++cc)
#pragma unroll
    for (int rr = 0; rr < 4; ++rr) {
      const int kp = 64 * cc + 32 * (rr >> 1) + 16 * (gB >> 1) + 4 * (gB & 1) + 2 * (rr & 1) + (q & 1) + 8 * (q >> 1);
      const int row = PERM ? ((kp & ~31) + 16 * (kp & 1) + ((kp & 31) >> 1)) : kp;
#pragma unroll
      for (int tb = 0; tb < 2; ++tb) {
        const v2u v = trread(base + row * 64 + tb * 32 + p * 8);
        f[tb][cc][2 * rr] = v.x; f[tb][cc][2 * rr + 1] = v.y;
      }
    }
}

__device__ __forceinline__ void chain_role(const Params& p, LAS u8* L, rsrc_t rs, int b) {
  const int tid = threadIdx.x, lane = tid & 63, w = __builtin_amdgcn_readfirstlane(tid >> 6);
  const int c = (b & 7) * 16 + (b >> 3);
  const int h = c >> 2, j = c & 3;
  const int r16 = lane & 15, g = lane >> 4;
  gu32* ctrl = (gu32*)p.ws;
  bool dead = false;
  const unsigned lbase = (unsigned)(size_t)L;

  v8u W1[3][2][2];
  v8u WO[2][2];
  {
    LAS u8* mine = L + w * 16384;
    const size_t wcol = (size_t)h * 128 + 32 * j;
    slice_dma(p.Wq + (size_t)128 * w * HD + wcol, HD, mine, lane);
    slice_dma(p.Wk + (size_t)128 * w * HD + wcol, HD, mine + 8192, lane);
    asm volatile("s_waitcnt vmcnt(8)" ::: "memory");
    slice_frags<true>(lbase + w * 16384, lane, W1[0]);
    WAIT_LGKM(); __builtin_amdgcn_sched_barrier(0);
    slice_dma(p.Wv + (size_t)128 * w * HD + wcol, HD, mine, lane);
    asm volatile("s_waitcnt vmcnt(8)" ::: "memory");
    slice_frags<true>(lbase + w * 16384 + 8192, lane, W1[1]);
    WAIT_LGKM(); __builtin_amdgcn_sched_barrier(0);
    slice_dma(p.Wo + (size_t)128 * h * DM + 256 * j + 32 * w, DM, mine + 8192, lane);
    asm volatile("s_waitcnt vmcnt(8)" ::: "memory");
    slice_frags<true>(lbase + w * 16384, lane, W1[2]);
    WAIT_LGKM(); __builtin_amdgcn_sched_barrier(0);
    arm_share(rs, b, tid);
    DRAIN_VM();
    slice_frags<false>(lbase + w * 16384 + 8192, lane, WO);
    WAIT_LGKM(); __builtin_amdgcn_sched_barrier(0);
  }
  arm_signal(ctrl, b, tid);
  wait_armed(ctrl, lane, dead);

  LAS float* RED = (LAS float*)L;
  LAS u8* AHF = L + 24576;
  const int sidx = r16 < 8 ? 0x4444 : 0xEEEE;
  const int gg = g & 1, hi = g >> 1;
  const float sc2 = 0.08838834764831845f * 1.4426950408889634f;

  v4u KX[2][4], VX[2];
  auto prefetch_kv = [&](int s) {
    if ((s & 3) == 0) { const int blk = s >> 2; wait_flags2(ctrl, W_FKV + blk * 128 + 2 * h, W_FKV + blk * 128 + 64 + 2 * h, 0x20u, dead); }
    CFENCE();
    const unsigned kb = OFF_KX + (unsigned)(s * NH + h) * 8192u + lane * 16, vb = OFF_VX + (unsigned)(s * NH + h) * 8192u + lane * 16;
#pragma unroll
    for (int t = 0; t < 2; ++t)
#pragma unroll
      for (int u = 0; u < 4; ++u) KX[t][u] = ld16(rs, kb + (t * 4 + u) * 1024);
    VX[0] = ld16(rs, vb + (2 * (w & 3)) * 1024); VX[1] = ld16(rs, vb + (2 * (w & 3) + 1) * 1024);
  };
  if (w < 4) prefetch_kv(0);

  for (int s = 0; s < NSEG; ++s) {
    v4u a0, a1;
    if (s == 0) {
      const int n0 = 4 * (2 * gg + (r16 >> 3));
      const u16* m0 = p.mem0 + (r16 & 7) * DM + 128 * w + 32 * hi + n0;
      const v2u l0 = *(const GAS v2u*)m0, h0 = *(const GAS v2u*)(m0 + 16), l1 = *(const GAS v2u*)(m0 + 64), h1 = *(const GAS v2u*)(m0 + 80);
      a0.x = __builtin_amdgcn_perm(h0.x, l0.x, 0x05040100u); a0.y = __builtin_amdgcn_perm(h0.x, l0.x, 0x07060302u);
      a0.z = __builtin_amdgcn_perm(h0.y, l0.y, 0x05040100u); a0.w = __builtin_amdgcn_perm(h0.y, l0.y, 0x07060302u);
      a1.x = __builtin_amdgcn_perm(h1.x, l1.x, 0x05040100u); a1.y = __builtin_amdgcn_perm(h1.x, l1.x, 0x07060302u);
      a1.z = __builtin_amdgcn_perm(h1.y, l1.y, 0x05040100u); a1.w = __builtin_amdgcn_perm(h1.y, l1.y, 0x07060302u);
    } else {
      const unsigned mo = OFF_MEM + (unsigned)s * MEM_SZ + w * 2048 + lane * 16;
      delay_cyc<W_A>();
      for (unsigned sp = 0;;) {
        CFENCE();
        a0 = ld16(rs, mo); a1 = ld16(rs, mo + 1024);
        if (__all(max(umax4(a0), umax4(a1)) != SENT) || dead) break;
        if (spin_fail(sp, ctrl, 0x100u + s)) { dead = true; break; }
      }
    }
    {
      f32x4 acc[6];
#pragma unroll
      for (int t = 0; t < 6; ++t) acc[t] = smf64(a0, W1[t >> 1][t & 1][0], f32x4{0.f, 0.f, 0.f, 0.f}, sidx);
#pragma unroll
      for (int t = 0; t < 6; ++t) acc[t] = smf64(a1, W1[t >> 1][t & 1][1], acc[t], sidx);
      LAS float* rw = RED + w * 768;
#pragma unroll
      for (int t = 0; t < 6; ++t) {
        const float s01 = swap32_add(acc[t][0], acc[t][1]), s23 = swap32_add(acc[t][2], acc[t][3]);
        if (t < 4) { rw[t * 128 + (4 * gg + hi) * 16 + r16] = s01; rw[t * 128 + (4 * gg + 2 + hi) * 16 + r16] = s23; }
        else { typedef float f32x2 __attribute__((ext_vector_type(2))); *(LAS f32x2*)(rw + t * 128 + r16 * 8 + (hi * 2 + gg) * 2) = f32x2{s01, s23}; }
      }
    }
    wgbar();
    const unsigned qo = OFF_QKV + (unsigned)(s * NH + h) * QKV_SZ;
    if (w < 3) {
      int ro; unsigned so;
      if (w < 2) {
        const int tb = lane >> 5, cr = lane & 7, g4 = (lane >> 3) & 3;
        ro = (2 * w + tb) * 128 + cr * 16 + 4 * g4;
        const int c16 = w == 0 ? cr : 8 + (cr >> 1) + 4 * (cr & 1);
        so = qo + j * 1024 + (c16 + 16 * g4) * 16 + tb * 8;
      } else {
        ro = (4 + gg) * 128 + r16 * 8 + 4 * hi;
        so = qo + 4096 + (32 * j + 16 * gg + r16) * 16 + hi * 8;
      }
      f32x4 t0 = *(LAS f32x4*)(RED + ro), t1 = *(LAS f32x4*)(RED + 768 + ro);
#pragma unroll
      for (int k = 2; k < 8; k += 2) { t0 += *(LAS f32x4*)(RED + k * 768 + ro); t1 += *(LAS f32x4*)(RED + (k + 1) * 768 + ro); }
      t0 += t1;
      st8(rs, so, v2u{canon(pk2(t0[0], t0[1])), canon(pk2(t0[2], t0[3]))});
    } else if (w == 7 && s > 0) {
      const v4u sv = {SENT, SENT, SENT, SENT};
      const unsigned po = OFF_PART + (unsigned)((s - 1) & 1) * PART_SZ + c * 8192 + lane * 16;
#pragma unroll
      for (int i = 0; i < 8; ++i) st16(rs, po + i * 1024, sv);
    }
    if (s == NSEG - 1) break;
    if (w < 4) {
      v4u F[4]; v2u VM[2];
      delay_cyc<W_B>();
      for (unsigned sp = 0;;) {
        CFENCE();
#pragma unroll
        for (int u = 0; u < 4; ++u) F[u] = ld16(rs, qo + u * 1024 + lane * 16);
        VM[0] = ld8(rs, qo + 4096 + (32 * w + r16) * 16 + gg * 8); VM[1] = ld8(rs, qo + 4096 + (32 * w + 16 + r16) * 16 + gg * 8);
        const unsigned m = max(max(max(umax4(F[0]), umax4(F[1])), max(umax4(F[2]), umax4(F[3]))), max(max(VM[0].x, VM[0].y), max(VM[1].x, VM[1].y)));
        if (__all(m != SENT) || dead) break;
        if (spin_fail(sp, ctrl, 0x200u + s)) { dead = true; break; }
      }
      f32x4 sA = {0.f, 0.f, 0.f, 0.f}, sB = sA, sM = sA;
#pragma unroll
      for (int u = 0; u < 4; ++u) { sM = mfma32(F[u], F[u], sM); sA = mfma32(KX[0][u], F[u], sA); sB = mfma32(KX[1][u], F[u], sB); }
      if (g < 2) sM = f32x4{-INFINITY, -INFINITY, -INFINITY, -INFINITY};
      float mx = fmaxf(fmaxf(fmaxf(sA[0], sA[1]), fmaxf(sA[2], sA[3])), fmaxf(fmaxf(sB[0], sB[1]), fmaxf(sB[2], sB[3])));
      mx = fmaxf(mx, fmaxf(fmaxf(sM[0], sM[1]), fmaxf(sM[2], sM[3])));
      mx = xmax32(xmax16(mx));
      const float mb = mx * sc2;
      float sum = 0.f;
#pragma unroll
      for (int r = 0; r < 4; ++r) {
        sA[r] = __builtin_amdgcn_exp2f(sA[r] * sc2 - mb); sB[r] = __builtin_amdgcn_exp2f(sB[r] * sc2 - mb); sM[r] = __builtin_amdgcn_exp2f(sM[r] * sc2 - mb);
        sum += sA[r] + sB[r] + sM[r];
      }
      sum = xadd32(xadd16(sum));
      const float inv = __builtin_amdgcn_rcpf(sum);
      const v4u pb = {pk2(sA[0] * inv, sA[1] * inv), pk2(sA[2] * inv, sA[3] * inv), pk2(sB[0] * inv, sB[1] * inv), pk2(sB[2] * inv, sB[3] * inv)};
      const v2u pm = {pk2(sM[0] * inv, sM[1] * inv), pk2(sM[2] * inv, sM[3] * inv)};
#pragma unroll
      for (int e = 0; e < 2; ++e) {
        f32x4 o = mfma32(VX[e], pb, f32x4{0.f, 0.f, 0.f, 0.f});
        o = mfma16(VM[e], pm, o);
        if (r16 < 8) *(LAS v2u*)(AHF + (2 * (2 * w + e) + hi) * 128 + r16 * 16 + gg * 8) = v2u{pk2(o[0], o[1]), pk2(o[2], o[3])};
      }
    }
    wgbar();
    {
      const v4u b0 = *(LAS v4u*)(AHF + lane * 16), b1 = *(LAS v4u*)(AHF + 1024 + lane * 16);
      f32x4 o0 = smf64(b0, WO[0][0], f32x4{0.f, 0.f, 0.f, 0.f}, sidx), o1 = smf64(b0, WO[1][0], f32x4{0.f, 0.f, 0.f, 0.f}, sidx);
      o0 = smf64(b1, WO[0][1], o0, sidx); o1 = smf64(b1, WO[1][1], o1, sidx);
      const v4u pv = {canonf(swap32_add(o0[0], o0[1])), canonf(swap32_add(o1[0], o1[1])), canonf(swap32_add(o0[2], o0[3])), canonf(swap32_add(o1[2], o1[3]))};
      st16(rs, OFF_PART + (unsigned)(s & 1) * PART_SZ + (32 * j + 4 * w + (r16 >> 2)) * 8192 + h * 256 + (gg * 2 + hi) * 64 + (r16 & 3) * 16, pv);
    }
    if (w < 4) prefetch_kv(s + 1);
    if (w == 6 && j == 0 && lane == 0) __hip_atomic_store(ctrl + W_PROG + 32 * h, (unsigned)(s + 1), RLX_AGENT);
    if (w == 7) {
      const unsigned po = OFF_PART + (unsigned)(s & 1) * PART_SZ + c * 8192 + lane * 16;
      v4u pr[8];
      delay_cyc<W_C>();
      for (unsigned sp = 0;;) {
        CFENCE();
#pragma unroll
        for (int i = 0; i < 8; ++i) pr[i] = ld16(rs, po + i * 1024);
        unsigned m = 0;
#pragma unroll
        for (int i = 0; i < 8; ++i) m = max(m, umax4(pr[i]));
        if (__all(m != SENT) || dead) break;
        if (spin_fail(sp, ctrl, 0x300u + s)) { dead = true; break; }
      }
      f32x4 t = __builtin_bit_cast(f32x4, pr[0]);
#pragma unroll
      for (int i = 1; i < 8; ++i) t += __builtin_bit_cast(f32x4, pr[i]);
#pragma unroll
      for (int r = 0; r < 4; ++r) t[r] = xadd32(xadd16(t[r]));
      const int rg = r16 >> 2, row = 4 * (rg >> 1) + (rg & 1) + 2 * gg;
      const unsigned d = canon(gg ? pk2(t[2], t[3]) : pk2(t[0], t[1]));
      DRAIN_VM();
      if (lane < 32) st4(rs, OFF_MEM + (unsigned)(s + 1) * MEM_SZ + c * 128 + row * 16 + (r16 & 3) * 4, d);
    }
    wgbar();
  }
}

enum { M_K = 0, M_V = 1, M_O = 2 };

template <int BN, int MODE>
__device__ __forceinline__ void gemm_tile(LAS u8* L, const u16* A, int lda, const u16* W, int ldw, int nkt, f32x4 (&acc)[4][BN / 64]) {
  constexpr int NT = BN / 64, NCG = BN / 64, STG = 16384 + BN * 128, NB = BN / 64;
  const int tid = threadIdx.x, lane = tid & 63, w = __builtin_amdgcn_readfirstlane(tid >> 6);
  const int wm = w >> 2, wn = w & 3, r16 = lane & 15, g = lane >> 4, q = r16 >> 2, pp = lane & 3;
  const int arow = 16 * w + (lane >> 3);
  const u16* asrc0 = A + (size_t)arow * lda + 8 * ((lane & 7) ^ ((arow >> 1) & 7));
  const u16* asrc1 = A + (size_t)(arow + 8) * lda + 8 * ((lane & 7) ^ (((arow + 8) >> 1) & 7));
  const int kr = 8 * w + (lane >> 3);
  const int krs = MODE == M_O ? ((kr & ~31) + 16 * ((kr >> 2) & 1) + 4 * ((kr >> 3) & 3) + (kr & 3)) : kr;
  const int fsw = ((kr >> 1) & 1) | (((kr >> 3) & 1) << 1);
  const u16* wsrc = W + (size_t)krs * ldw + 16 * (((lane & 7) >> 1) ^ fsw) + 8 * (lane & 1);
  auto stage = [&](int kt, int buf) {
    LAS u8* sb = L + buf * STG;
    GLDS16(asrc0 + kt * 64, sb + (2 * w) * 1024 + lane * 16);
    GLDS16(asrc1 + kt * 64, sb + (2 * w + 1) * 1024 + lane * 16);
#pragma unroll
    for (int i = 0; i < NB; ++i) GLDS16(wsrc + (size_t)kt * 64 * ldw + 64 * i, sb + 16384 + (w * NB + i) * 1024 + lane * 16);
  };
  const unsigned lb = (unsigned)(size_t)L;
  const unsigned aoff = lb + (64 * wm + r16) * 128 + ((g ^ ((r16 >> 1) & 7)) * 16);
  const int fr = ((q >> 1) & 1) | ((g & 1) << 1);
  const int cgw = (wn * NT) >> 2, c0 = (wn * NT) & 3;
  unsigned boff[NT];
#pragma unroll
  for (int n = 0; n < NT; ++n) boff[n] = lb + 16384 + (g * NCG + cgw) * 1024 + q * 128 + (((c0 + n) ^ fr) * 32) + pp * 8;
#pragma unroll
  for (int m = 0; m < 4; ++m)
#pragma unroll
    for (int n = 0; n < NT; ++n) acc[m][n] = f32x4{0.f, 0.f, 0.f, 0.f};

  wgbar();
  stage(0, 0); stage(1, 1);
  int buf = 0;
  for (int kt = 0; kt < nkt; ++kt) {
    if (kt + 1 < nkt) { if (NB == 4) asm volatile("s_waitcnt vmcnt(6)" ::: "memory"); else if (NB == 2) asm volatile("s_waitcnt vmcnt(4)" ::: "memory"); else asm volatile("s_waitcnt vmcnt(3)" ::: "memory"); }
    else DRAIN_VM();
    __builtin_amdgcn_s_barrier(); CFENCE();
    if (kt + 2 < nkt) stage(kt + 2, buf >= 1 ? buf - 1 : 2);
    const unsigned so = buf * STG;
#pragma unroll
    for (int ks = 0; ks < 2; ++ks) {
      v4u af[4], bf[NT];
#pragma unroll
      for (int m = 0; m < 4; ++m) af[m] = *(LAS v4u*)(size_t)(((aoff + so) ^ (ks * 64)) + m * 2048);
#pragma unroll
      for (int n = 0; n < NT; ++n) {
        const v2u x0 = trread(boff[n] + so + ks * 4 * NCG * 1024), x1 = trread(boff[n] + so + ks * 4 * NCG * 1024 + 512);
        bf[n] = v4u{x0.x, x0.y, x1.x, x1.y};
      }
      __builtin_amdgcn_s_setprio(1);
#pragma unroll
      for (int m = 0; m < 4; ++m)
#pragma unroll
        for (int n = 0; n < NT; ++n) acc[m][n] = MODE == M_V ? mfma32(af[m], bf[n], acc[m][n]) : mfma32(bf[n], af[m], acc[m][n]);
      __builtin_amdgcn_s_setprio(0);
    }
    WAIT_LGKM();
    buf = buf == 2 ? 0 : buf + 1;
  }
}

template <int BN, int MODE>
__device__ __forceinline__ void proj_task(const Params& p, LAS u8* L, rsrc_t rs, int blk, int mtx, int col0) {
  constexpr int NT = BN / 64;
  const int tid = threadIdx.x, lane = tid & 63, w = __builtin_amdgcn_readfirstlane(tid >> 6), wm = w >> 2, wn = w & 3;
  f32x4 acc[4][NT];
  const u16* W = (mtx == 0 ? p.Wk : mtx == 1 ? p.Wv : p.Wq) + col0;
  gemm_tile<BN, MODE>(L, p.x + (size_t)blk * 128 * DM, DM, W, HD, DM / 64, acc);
  const int cw = col0 + wn * (BN / 4), h = cw >> 7;
  const unsigned base = (mtx == 0 ? OFF_KX : mtx == 1 ? OFF_VX : OFF_QX) + (unsigned)((blk * 4 + 2 * wm) * NH + h) * 8192u + lane * 16;
  if (MODE == M_K) {
#pragma unroll
    for (int m = 0; m < 4; ++m) {
      const unsigned o = base + (m >> 1) * (NH * 8192u) + ((m & 1) * 4 + ((cw & 127) >> 5)) * 1024;
      if (NT == 1) st8(rs, o + ((cw >> 4) & 1) * 8, v2u{pk2(acc[m][0][0], acc[m][0][1]), pk2(acc[m][0][2], acc[m][0][3])});
      else {
#pragma unroll
        for (int v = 0; v < NT / 2; ++v)
          st16(rs, o + v * 1024, v4u{pk2(acc[m][2 * v][0], acc[m][2 * v][1]), pk2(acc[m][2 * v][2], acc[m][2 * v][3]),
                                     pk2(acc[m][2 * v + 1][0], acc[m][2 * v + 1][1]), pk2(acc[m][2 * v + 1][2], acc[m][2 * v + 1][3])});
      }
    }
  } else {
#pragma unroll
    for (int sg = 0; sg < 2; ++sg)
#pragma unroll
      for (int n = 0; n < NT; ++n)
        st16(rs, base + sg * (NH * 8192u) + (((cw & 127) >> 4) + n) * 1024,
             v4u{pk2(acc[2 * sg][n][0], acc[2 * sg][n][1]), pk2(acc[2 * sg][n][2], acc[2 * sg][n][3]),
                 pk2(acc[2 * sg + 1][n][0], acc[2 * sg + 1][n][1]), pk2(acc[2 * sg + 1][n][2], acc[2 * sg + 1][n][3])});
  }
  DRAIN_VM();
  wgbar();
  gu32* ctrl = (gu32*)p.ws;
  if (tid < NT) {
    const int word = mtx == 2 ? W_FQ + blk * 64 + (col0 >> 6) + tid : W_FKV + blk * 128 + mtx * 64 + (col0 >> 6) + tid;
    __hip_atomic_store(ctrl + word, 1u, RLX_AGENT);
  }
}

__device__ __forceinline__ float wave_sum(float v) {
  v += __shfl_xor(v, 1); v += __shfl_xor(v, 2); v += __shfl_xor(v, 4); v += __shfl_xor(v, 8);
  return xadd32(xadd16(v));
}
__device__ __forceinline__ float sigmoidf_(float v) { return 1.0f / (1.0f + __expf(-v)); }

__device__ __forceinline__ void rg_task(const Params& p, rsrc_t rs, int wk) {
  const int tid = threadIdx.x, lane = tid & 63, w = __builtin_amdgcn_readfirstlane(tid >> 6);
  v4u wr[2], gw[2];
#pragma unroll
  for (int i = 0; i < 2; ++i) { wr[i] = *(const GAS v4u*)(p.wr + i * 512 + lane * 8); gw[i] = *(const GAS v4u*)(p.gw + i * 512 + lane * 8); }
#pragma unroll
  for (int t = 0; t < 2; ++t) {
    const int tok = wk * 16 + 2 * w + t;
    float r = 0.f, gs = 0.f;
#pragma unroll
    for (int i = 0; i < 2; ++i) {
      const v4u xv = *(const GAS v4u*)(p.x + (size_t)tok * DM + i * 512 + lane * 8);
#pragma unroll
      for (int d = 0; d < 4; ++d) {
        r = fmaf(bflo(xv[d]), bflo(wr[i][d]), r); r = fmaf(bfhi(xv[d]), bfhi(wr[i][d]), r);
        gs += bfr(bflo(xv[d]) * bflo(gw[i][d])); gs += bfr(bfhi(xv[d]) * bfhi(gw[i][d]));
      }
    }
    r = bfr(wave_sum(r)); gs = bfr(wave_sum(gs));
    if (lane == 0) { st4(rs, OFF_R + tok * 4, f2u(r)); st4(rs, OFF_G + tok * 4, f2u(bfr(sigmoidf_(gs)))); }
  }
  DRAIN_VM();
  wgbar();
  if (tid == 0) __hip_atomic_fetch_add((gu32*)p.ws + W_RG, 1u, RLX_AGENT);
}

__device__ __forceinline__ unsigned long long top_key(unsigned bits, int idx) {
  const unsigned o = (bits & 0x7FFFFFFFu) > 0x7F800000u ? 0u : ((bits & 0x80000000u) ? ~bits : (bits | 0x80000000u));
  return ((unsigned long long)o << 32) | (unsigned)(0x7FFFFFFF - idx);
}
__device__ __forceinline__ void top_ins(unsigned long long k, unsigned long long& a, unsigned long long& b) {
  const unsigned long long lo = k < a ? k : a;
  a = k < a ? a : k;
  b = lo > b ? lo : b;
}
__device__ __forceinline__ void top2(rsrc_t rs, LAS u8* L, int& i0, int& i1) {
  const int tid = threadIdx.x, lane = tid & 63, w = tid >> 6;
  const v4u rv = ld16(rs, OFF_R + tid * 16);
  unsigned long long a = 0ull, b = 0ull;
#pragma unroll
  for (int d = 0; d < 4; ++d) top_ins(top_key(rv[d], tid * 4 + d), a, b);
#pragma unroll
  for (int o = 1; o < 64; o <<= 1) {
    const unsigned long long c0 = __shfl_xor(a, o), c1 = __shfl_xor(b, o);
    top_ins(c0, a, b); top_ins(c1, a, b);
  }
  LAS unsigned long long* tv = (LAS unsigned long long*)L;
  wgbar();
  if (lane == 0) { tv[2 * w] = a; tv[2 * w + 1] = b; }
  wgbar();
  a = b = 0ull;
#pragma unroll
  for (int k = 0; k < 16; ++k) top_ins(tv[k], a, b);
  wgbar();
  i0 = (0x7FFFFFFF - (int)(unsigned)a) & 2047; i1 = (0x7FFFFFFF - (int)(unsigned)b) & 2047;
}

__device__ __forceinline__ void mlp1_task(const Params& p, LAS u8* L, rsrc_t rs, int t, bool& dead) {
  const int tid = threadIdx.x;
  gu32* ctrl = (gu32*)p.ws;
  wait_ge(ctrl, W_RG, NWORK, 0x40u, dead);
  int i0, i1;
  top2(rs, L, i0, i1);
  if (t == 0 && tid == 0) {
    const float r0 = u2f(ld4(rs, OFF_R + i0 * 4)), r1 = u2f(ld4(rs, OFF_R + i1 * 4));
    st16(rs, W_TOP * 4, v4u{(unsigned)i0, (unsigned)i1, f2u(bfr(sigmoidf_(r0))), f2u(bfr(sigmoidf_(r1)))});
  }
  const int rg = tid >> 3, cq = tid & 7;
  float ag[2][8], au[2][8];
#pragma unroll
  for (int e = 0; e < 8; ++e) { ag[0][e] = ag[1][e] = au[0][e] = au[1][e] = 0.f; }
#pragma unroll 4
  for (int i = 0; i < 16; ++i) {
    const int k = rg + 64 * i;
    const v4u gv = *(const GAS v4u*)(p.wg + (size_t)k * FF + 64 * t + 8 * cq), uv = *(const GAS v4u*)(p.wu + (size_t)k * FF + 64 * t + 8 * cq);
    const float x0 = bf2f(p.x[(size_t)i0 * DM + k]), x1 = bf2f(p.x[(size_t)i1 * DM + k]);
#pragma unroll
    for (int d = 0; d < 4; ++d) {
      ag[0][2 * d] = fmaf(x0, bflo(gv[d]), ag[0][2 * d]); ag[0][2 * d + 1] = fmaf(x0, bfhi(gv[d]), ag[0][2 * d + 1]);
      ag[1][2 * d] = fmaf(x1, bflo(gv[d]), ag[1][2 * d]); ag[1][2 * d + 1] = fmaf(x1, bfhi(gv[d]), ag[1][2 * d + 1]);
      au[0][2 * d] = fmaf(x0, bflo(uv[d]), au[0][2 * d]); au[0][2 * d + 1] = fmaf(x0, bfhi(uv[d]), au[0][2 * d + 1]);
      au[1][2 * d] = fmaf(x1, bflo(uv[d]), au[1][2 * d]); au[1][2 * d + 1] = fmaf(x1, bfhi(uv[d]), au[1][2 * d + 1]);
    }
  }
  LAS float* R = (LAS float*)L;
#pragma unroll
  for (int e = 0; e < 8; ++e) {
    R[(0 * 64 + rg) * 65 + 8 * cq + e] = ag[0][e]; R[(1 * 64 + rg) * 65 + 8 * cq + e] = ag[1][e];
    R[(2 * 64 + rg) * 65 + 8 * cq + e] = au[0][e]; R[(3 * 64 + rg) * 65 + 8 * cq + e] = au[1][e];
  }
  wgbar();
  if (tid < 128) {
    const int tok = tid >> 6, col = tid & 63;
    float gs = 0.f, us = 0.f;
    for (int r = 0; r < 64; ++r) { gs += R[(tok * 64 + r) * 65 + col]; us += R[((2 + tok) * 64 + r) * 65 + col]; }
    gs = bfr(gs); us = bfr(us);
    const float act = bfr(gs * sigmoidf_(gs));
    st4(rs, OFF_HID + (tok * FF + 64 * t + col) * 4, f2u(bfr(act * us)));
  }
  DRAIN_VM();
  wgbar();
  if (tid == 0) __hip_atomic_fetch_add(ctrl + W_M1, 1u, RLX_AGENT);
}

__device__ __forceinline__ void mlp2_task(const Params& p, LAS u8* L, rsrc_t rs, int t, bool& dead) {
  const int tid = threadIdx.x;
  gu32* ctrl = (gu32*)p.ws;
  wait_ge(ctrl, W_M1, 56, 0x41u, dead);
  CFENCE();
  const int kc = t >> 2, cb = t & 3, rg = tid >> 5, cq = tid & 31;
  float a[2][8];
#pragma unroll
  for (int e = 0; e < 8; ++e) a[0][e] = a[1][e] = 0.f;
#pragma unroll 4
  for (int i = 0; i < 16; ++i) {
    const int k = 256 * kc + rg + 16 * i;
    const v4u dv = *(const GAS v4u*)(p.wd + (size_t)k * DM + 256 * cb + 8 * cq);
    const float h0 = u2f(ld4(rs, OFF_HID + k * 4)), h1 = u2f(ld4(rs, OFF_HID + (FF + k) * 4));
#pragma unroll
    for (int d = 0; d < 4; ++d) {
      a[0][2 * d] = fmaf(h0, bflo(dv[d]), a[0][2 * d]); a[0][2 * d + 1] = fmaf(h0, bfhi(dv[d]), a[0][2 * d + 1]);
      a[1][2 * d] = fmaf(h1, bflo(dv[d]), a[1][2 * d]); a[1][2 * d + 1] = fmaf(h1, bfhi(dv[d]), a[1][2 * d + 1]);
    }
  }
  LAS float* R = (LAS float*)L;
  wgbar();
#pragma unroll
  for (int e = 0; e < 8; ++e) { R[(0 * 16 + rg) * 257 + 8 * cq + e] = a[0][e]; R[(1 * 16 + rg) * 257 + 8 * cq + e] = a[1][e]; }
  wgbar();
  {
    const int tok = tid >> 8, col = tid & 255;
    float sacc = 0.f;
#pragma unroll
    for (int r = 0; r < 16; ++r) sacc += R[(tok * 16 + r) * 257 + col];
    st4(rs, OFF_YP + ((kc * 2 + tok) * DM + 256 * cb + col) * 4, f2u(sacc));
  }
  DRAIN_VM();
  wgbar();
  if (tid == 0) __hip_atomic_fetch_add(ctrl + W_M2, 1u, RLX_AGENT);
}

__device__ __forceinline__ void attn_task(const Params& p, rsrc_t rs, int s, int h, bool& dead) {
  const int lane = threadIdx.x & 63, r16 = lane & 15, g = lane >> 4, gg = g & 1;
  gu32* ctrl = (gu32*)p.ws;
  const int blk = s >> 2;
  wait_flags2(ctrl, W_FKV + blk * 128 + 2 * h, W_FKV + blk * 128 + 64 + 2 * h, 0x50u, dead);
  wait_flags2(ctrl, W_FQ + blk * 64 + 2 * h, W_FQ + blk * 64 + 2 * h, 0x51u, dead);
  CFENCE();
  const unsigned fo = (unsigned)(s * NH + h) * 8192u + lane * 16;
  v4u KX[2][4], QX[2][4], VX[8], F[4]; v2u VM[8];
#pragma unroll
  for (int i = 0; i < 8; ++i) { KX[i >> 2][i & 3] = ld16(rs, OFF_KX + fo + i * 1024); QX[i >> 2][i & 3] = ld16(rs, OFF_QX + fo + i * 1024); VX[i] = ld16(rs, OFF_VX + fo + i * 1024); }
  wait_ge(ctrl, W_PROG + 32 * h, (unsigned)(s + 1 < NSEG - 1 ? s + 1 : NSEG - 1), 0x52u, dead);
  const unsigned qo = OFF_QKV + (unsigned)(s * NH + h) * QKV_SZ;
  for (unsigned sp = 0;;) {
    CFENCE();
    unsigned m = 0;
#pragma unroll
    for (int u = 0; u < 4; ++u) { F[u] = ld16(rs, qo + u * 1024 + lane * 16); m = max(m, umax4(F[u])); }
#pragma unroll
    for (int e = 0; e < 8; ++e) { VM[e] = ld8(rs, qo + 4096 + (16 * e + r16) * 16 + gg * 8); m = max(m, max(VM[e].x, VM[e].y)); }
    if (__all(m != SENT) || dead) break;
    if (spin_fail(sp, ctrl, 0x53u)) { dead = true; break; }
  }
  const float scale = 0.08838834764831845f, l2e = 1.4426950408889634f;
#pragma unroll
  for (int qt = 0; qt < 2; ++qt) {
    f32x4 sA = {0.f, 0.f, 0.f, 0.f}, sB = sA, sM = sA;
#pragma unroll
    for (int u = 0; u < 4; ++u) { sM = mfma32(F[u], QX[qt][u], sM); sA = mfma32(KX[0][u], QX[qt][u], sA); if (qt == 1) sB = mfma32(KX[1][u], QX[qt][u], sB); }
    const float neg = -1000000000.0f;
#pragma unroll
    for (int r = 0; r < 4; ++r) {
      sA[r] = bfr(bfr(sA[r]) * scale); sB[r] = bfr(bfr(sB[r]) * scale); sM[r] = bfr(bfr(sM[r]) * scale);
      const int kx = 4 * g + r;
      if (qt == 0) { if (kx > r16) sA[r] = bfr(neg); sB[r] = bfr(neg); }
      else { if (kx > r16) sB[r] = bfr(neg); }
      if (g < 2) sM[r] = -INFINITY;
    }
    float mx = fmaxf(fmaxf(fmaxf(sA[0], sA[1]), fmaxf(sA[2], sA[3])), fmaxf(fmaxf(sB[0], sB[1]), fmaxf(sB[2], sB[3])));
    mx = fmaxf(mx, fmaxf(fmaxf(sM[0], sM[1]), fmaxf(sM[2], sM[3])));
    mx = xmax32(xmax16(mx));
    float sum = 0.f;
#pragma unroll
    for (int r = 0; r < 4; ++r) {
      sA[r] = __builtin_amdgcn_exp2f((sA[r] - mx) * l2e); sB[r] = __builtin_amdgcn_exp2f((sB[r] - mx) * l2e); sM[r] = __builtin_amdgcn_exp2f((sM[r] - mx) * l2e);
      sum += sA[r] + sB[r] + sM[r];
    }
    sum = xadd32(xadd16(sum));
    const float inv = 1.0f / sum;
    const v4u pb = {pk2(sA[0] * inv, sA[1] * inv), pk2(sA[2] * inv, sA[3] * inv), pk2(sB[0] * inv, sB[1] * inv), pk2(sB[2] * inv, sB[3] * inv)};
    const v2u pm = {pk2(sM[0] * inv, sM[1] * inv), pk2(sM[2] * inv, sM[3] * inv)};
    const unsigned ao = OFF_AX + (unsigned)(s * 32 + qt * 16 + r16) * (HD * 2) + h * 256 + g * 16;
#pragma unroll
    for (int v = 0; v < 4; ++v) {
      f32x4 o0 = mfma32(VX[2 * v], pb, f32x4{0.f, 0.f, 0.f, 0.f}), o1 = mfma32(VX[2 * v + 1], pb, f32x4{0.f, 0.f, 0.f, 0.f});
      o0 = mfma16(VM[2 * v], pm, o0); o1 = mfma16(VM[2 * v + 1], pm, o1);
      st16(rs, ao + v * 64, v4u{pk2(o0[0], o0[1]), pk2(o0[2], o0[3]), pk2(o1[0], o1[1]), pk2(o1[2], o1[3])});
    }
  }
  DRAIN_VM();
  if (lane == 0) __hip_atomic_fetch_add(ctrl + W_T2 + 64 * blk, 1u, RLX_AGENT);
}

__device__ __forceinline__ void oproj_task(const Params& p, LAS u8* L, rsrc_t rs, int blk, int ct, bool& dead) {
  const int tid = threadIdx.x, lane = tid & 63, w = __builtin_amdgcn_readfirstlane(tid >> 6), wm = w >> 2, wn = w & 3, r16 = lane & 15, g = lane >> 4;
  gu32* ctrl = (gu32*)p.ws;
  if (w == 0) {
    wait_ge(ctrl, W_M2, 56, 0x60u, dead);
    wait_ge(ctrl, W_T2 + 64 * blk, 128, 0x61u, dead);
    __builtin_amdgcn_fence(__ATOMIC_ACQUIRE, "agent");
    DRAIN_VM();
  }
  f32x4 acc[4][2];
  gemm_tile<128, M_O>(L, (const u16*)(p.ws + OFF_AX) + (size_t)blk * 128 * HD, HD, p.Wo + ct * 128, DM, HD / 64, acc);
  const v4u top = ld16(rs, W_TOP * 4);
#pragma unroll
  for (int m = 0; m < 4; ++m) {
    const int tok = blk * 128 + 64 * wm + 16 * m + r16;
    const float gt = u2f(ld4(rs, OFF_G + tok * 4)), og = bfr(1.0f - gt);
    const int sel = tok == (int)top.x ? 0 : tok == (int)top.y ? 1 : -1;
#pragma unroll
    for (int n = 0; n < 2; ++n) {
      const int col = ct * 128 + 32 * wn + 16 * n + 4 * g;
      const v2u xv = *(const GAS v2u*)(p.x + (size_t)tok * DM + col);
      float bv[4] = {bflo(xv.x), bfhi(xv.x), bflo(xv.y), bfhi(xv.y)};
      if (sel >= 0) {
        f32x4 y = {0.f, 0.f, 0.f, 0.f};
        for (int kc = 0; kc < 14; ++kc) y += __builtin_bit_cast(f32x4, ld16(rs, OFF_YP + ((kc * 2 + sel) * DM + col) * 4));
        const float rsel = u2f(sel == 0 ? top.z : top.w);
#pragma unroll
        for (int r = 0; r < 4; ++r) bv[r] = bfr(bv[r] + bfr(bfr(y[r]) * rsel));
      }
      float o[4];
#pragma unroll
      for (int r = 0; r < 4; ++r) o[r] = bfr(gt * bfr(acc[m][n][r])) + bfr(og * bv[r]);
      *(GAS v2u*)(p.out + (size_t)tok * DM + col) = v2u{pk2(o[0], o[1]), pk2(o[2], o[3])};
    }
  }
}

__device__ __forceinline__ void worker_role(const Params& p, LAS u8* L, rsrc_t rs, int b) {
  const int tid = threadIdx.x, lane = tid & 63, w = __builtin_amdgcn_readfirstlane(tid >> 6);
  const int wk = b - NCHAIN, x8 = wk & 7, r = wk >> 3;
  gu32* ctrl = (gu32*)p.ws;
  bool dead = false;
  {
    const int cg = x8 * 16 + r;
    for (int blk = 0; blk < 2; ++blk) {
      if (cg < 64) proj_task<64, M_K>(p, L, rs, blk, 0, cg * 64); else proj_task<64, M_V>(p, L, rs, blk, 1, (cg - 64) * 64);
      if (blk == 0) { arm_share(rs, b, tid); arm_signal(ctrl, b, tid); }
    }
  }
  rg_task(p, rs, wk);
  for (int rnd = 0; rnd < 6; ++rnd) {
    const int ct = x8 * 4 + (r & 3), bq = r >> 2;
    if (rnd < 4) {
      const int blk = 2 + rnd * 4 + bq;
      if (blk < 16) { if (ct < 16) proj_task<256, M_K>(p, L, rs, blk, 0, ct * 256); else proj_task<256, M_V>(p, L, rs, blk, 1, (ct - 16) * 256); }
      else { const int t = (blk - 16) * 32 + ct; if (t < 56) mlp1_task(p, L, rs, t, dead); }
    } else {
      const int qct = x8 * 2 + (r & 1), blk = (rnd - 4) * 8 + (r >> 1);
      proj_task<256, M_K>(p, L, rs, blk, 2, qct * 256);
    }
  }
  if (wk < 56) mlp2_task(p, L, rs, wk, dead);
  wait_armed(ctrl, lane, dead);
  {
    const int blk = x8 + 8 * (r >> 3), m = r & 7;
    for (int i = 0; i < 2; ++i) { const int t = (i * 8 + m) * 8 + w; attn_task(p, rs, blk * 4 + (t >> 5), t & 31, dead); }
    oproj_task(p, L, rs, blk, m, dead);
  }
}

__global__ void __launch_bounds__(NTHR, 2) rmt_mod_kernel(Params p) {
  extern __shared__ __attribute__((aligned(16))) u8 smem[];
  LAS u8* L = (LAS u8*)smem;
  const rsrc_t rs = __builtin_amdgcn_make_buffer_rsrc(p.ws, 0, WS_END, 0x00020000);
  const int b = blockIdx.x;
  if (b < NCHAIN) chain_role(p, L, rs, b); else worker_role(p, L, rs, b);
}

}

extern "C" void kernel_launch(void* const* d_in, const int* in_sizes, int n_in, void* d_out, int out_size, void* d_ws, size_t ws_size,
                              hipStream_t stream) {
  (void)in_sizes; (void)n_in; (void)out_size; (void)ws_size;
  Params p;
  p.x = (const u16*)d_in[0]; p.mem0 = (const u16*)d_in[1]; p.Wq = (const u16*)d_in[2]; p.Wk = (const u16*)d_in[3];
  p.Wv = (const u16*)d_in[4]; p.Wo = (const u16*)d_in[5]; p.wr = (const u16*)d_in[8]; p.wg = (const u16*)d_in[9];
  p.wu = (const u16*)d_in[10]; p.wd = (const u16*)d_in[11]; p.gw = (const u16*)d_in[12];
  p.out = (u16*)d_out; p.ws = (u8*)d_ws;
  (void)hipFuncSetAttribute((const void*)rmt_mod_kernel, hipFuncAttributeMaxDynamicSharedMemorySize, LDS_BYTES);
  (void)hipMemsetAsync(d_ws, 0, CTRL_BYTES, stream);
  rmt_mod_kernel<<<NWG, NTHR, LDS_BYTES, stream>>>(p);
}
```

```cpp
#include <hip/hip_runtime.h>
#include <hip/hip_bf16.h>
#include <stdint.h>

#define GAS __attribute__((address_space(1)))
#define LAS __attribute__((address_space(3)))
typedef unsigned v4u __attribute__((ext_vector_type(4)));
typedef unsigned v2u __attribute__((ext_vector_type(2)));
typedef unsigned v8u __attribute__((ext_vector_type(8)));
typedef float f32x4 __attribute__((ext_vector_type(4)));
typedef __bf16 bfx8 __attribute__((ext_vector_type(8)));
typedef __bf16 bfx16 __attribute__((ext_vector_type(16)));
typedef short s16x4 __attribute__((ext_vector_type(4)));
typedef GAS unsigned gu32;
typedef unsigned short u16;
typedef unsigned char u8;
#define RLX_AGENT __ATOMIC_RELAXED, __HIP_MEMORY_SCOPE_AGENT

#define SPLIT_XCD 2
namespace {

constexpr int SEQ = 2048, DM = 1024, NH = 32, HD = 4096, NSEG = 64, FF = 3584;
constexpr int NCHAIN = 128, NWORK = 128, NWG = 256, NTHR = 512;
constexpr unsigned SENT = 0xFFFFFFFFu;
constexpr int AUX_SC1 = 16;

constexpr unsigned CTRL_BYTES = 73728;
constexpr int LN = 32;
constexpr int W_TMO = 0, W_RG = 1 * LN, W_M1 = 2 * LN, W_TOP = 3 * LN  , W_CFG = 4 * LN  ,
              W_XCC = 5 * LN, W_PROG = 8 * LN  , W_FKV = 64 * LN  , W_FQ = 128 * LN  ,
              W_ARM = 160 * LN  , W_M2 = 288 * LN  , W_T2 = 344 * LN  ,
              W_SA = 408 * LN  , W_SP = 472 * LN  ;
constexpr unsigned OFF_MEM = 98304;
constexpr unsigned MEM_ST = 32768;
constexpr unsigned OFF_QKV = OFF_MEM + NSEG * MEM_ST;
constexpr unsigned QH = 32, PR = 128;
constexpr unsigned OFF_PART = OFF_QKV + NSEG * QH * 16384;
constexpr unsigned PART_SZ = PR * 16384;
#define NSLOT 3
constexpr unsigned OFF_ARM_END = OFF_PART + NSLOT * PART_SZ;
constexpr unsigned OFF_CAL = OFF_ARM_END;
constexpr unsigned OFF_KX = OFF_CAL + 16384 + 16384;
constexpr unsigned OFF_VX = OFF_KX + NSEG * NH * 8192;
constexpr unsigned OFF_QX = OFF_VX + NSEG * NH * 8192;
constexpr unsigned OFF_AX = OFF_QX + NSEG * NH * 8192;
constexpr unsigned OFF_R = OFF_AX + SEQ * HD * 2;
constexpr unsigned OFF_G = OFF_R + SEQ * 4;
constexpr unsigned OFF_HID = OFF_G + SEQ * 4;
constexpr unsigned OFF_YP = OFF_HID + 2 * FF * 4;
constexpr unsigned OFF_PS = OFF_YP + 14 * 2 * DM * 4;
constexpr unsigned WS_END = OFF_PS + 32 * 32 * 16384;
constexpr int NBULK = 8;
constexpr int LDS_BYTES = 160 * 1024;
constexpr unsigned SPIN_CAP = 1u << 18;

#define W_A 320
#define W_B 80
#define W_C 320

struct Params {
  const u16 *x, *mem0, *Wq, *Wk, *Wv, *Wo, *wr, *wg, *wu, *wd, *gw;
  u16* out;
  u8* ws;
};

__device__ __forceinline__ float u2f(unsigned u) { return __builtin_bit_cast(float, u); }
__device__ __forceinline__ unsigned f2u(float f) { return __builtin_bit_cast(unsigned, f); }
__device__ __forceinline__ float bf2f(unsigned b) { return u2f(b << 16); }
__device__ __forceinline__ float bflo(unsigned d) { return u2f(d << 16); }
__device__ __forceinline__ float bfhi(unsigned d) { return u2f(d & 0xFFFF0000u); }
__device__ __forceinline__ unsigned pk2(float lo, float hi) {
  typedef __bf16 bfx2 __attribute__((ext_vector_type(2)));
  bfx2 v; v.x = (__bf16)lo; v.y = (__bf16)hi;
  return __builtin_bit_cast(unsigned, v);
}
__device__ __forceinline__ float bfr(float f) { return (float)(__bf16)f; }
__device__ __forceinline__ unsigned canon(unsigned d) { return min(d, 0xFFFFFFFEu); }
__device__ __forceinline__ f32x4 mfma32(v4u a, v4u b, f32x4 c) {
  return __builtin_amdgcn_mfma_f32_16x16x32_bf16(__builtin_bit_cast(bfx8, a), __builtin_bit_cast(bfx8, b), c, 0, 0, 0);
}
__device__ __forceinline__ f32x4 mfma16(v2u a, v2u b, f32x4 c) { return mfma32(v4u{a.x, a.y, 0u, 0u}, v4u{b.x, b.y, 0u, 0u}, c); }
__device__ __forceinline__ f32x4 smf64(v4u a, v8u b, f32x4 c, int idx) {
  return __builtin_amdgcn_smfmac_f32_16x16x64_bf16(__builtin_bit_cast(bfx8, a), __builtin_bit_cast(bfx16, b), c, idx, 0, 0);
}
__device__ __forceinline__ v2u trread(unsigned lds_addr) {
  return __builtin_bit_cast(v2u, __builtin_amdgcn_ds_read_tr16_b64_v4i16((LAS s16x4*)(size_t)lds_addr));
}
typedef float f32x2 __attribute__((ext_vector_type(2)));
__device__ __forceinline__ f32x2 fold32(f32x4 v) {
  const auto r0 = __builtin_amdgcn_permlane32_swap(f2u(v[0]), f2u(v[2]), false, false);
  const auto r1 = __builtin_amdgcn_permlane32_swap(f2u(v[1]), f2u(v[3]), false, false);
  return f32x2{u2f(r0[0]), u2f(r1[0])} + f32x2{u2f(r0[1]), u2f(r1[1])};
}
__device__ __forceinline__ float xmax16(float a) { auto r = __builtin_amdgcn_permlane16_swap(f2u(a), f2u(a), false, false); return __builtin_amdgcn_fmed3f(u2f(r[0]), u2f(r[1]), INFINITY); }
__device__ __forceinline__ float xmax32(float a) { auto r = __builtin_amdgcn_permlane32_swap(f2u(a), f2u(a), false, false); return __builtin_amdgcn_fmed3f(u2f(r[0]), u2f(r[1]), INFINITY); }
__device__ __forceinline__ float xadd16(float a) { auto r = __builtin_amdgcn_permlane16_swap(f2u(a), f2u(a), false, false); return u2f(r[0]) + u2f(r[1]); }
__device__ __forceinline__ float xadd32(float a) { auto r = __builtin_amdgcn_permlane32_swap(f2u(a), f2u(a), false, false); return u2f(r[0]) + u2f(r[1]); }

__device__ __forceinline__ float wave_sum(float v) {
  v += __shfl_xor(v, 1); v += __shfl_xor(v, 2); v += __shfl_xor(v, 4); v += __shfl_xor(v, 8);
  return xadd32(xadd16(v));
}
#define CFENCE() asm volatile("" ::: "memory")
#define DRAIN_VM() asm volatile("s_waitcnt vmcnt(0)" ::: "memory")
#define WAIT_LGKM() asm volatile("s_waitcnt lgkmcnt(0)" ::: "memory")
__device__ __forceinline__ void wgbar() { WAIT_LGKM(); __builtin_amdgcn_s_barrier(); CFENCE(); }

typedef __amdgpu_buffer_rsrc_t rsrc_t;
__device__ __forceinline__ v4u ld16(rsrc_t rs, unsigned off) { return __builtin_amdgcn_raw_buffer_load_b128(rs, off, 0, AUX_SC1); }
__device__ __forceinline__ v2u ld8(rsrc_t rs, unsigned off) { return __builtin_amdgcn_raw_buffer_load_b64(rs, off, 0, AUX_SC1); }
__device__ __forceinline__ unsigned ld4(rsrc_t rs, unsigned off) { return __builtin_amdgcn_raw_buffer_load_b32(rs, off, 0, AUX_SC1); }
__device__ __forceinline__ void st16(rsrc_t rs, unsigned off, v4u v) { __builtin_amdgcn_raw_buffer_store_b128(v, rs, off, 0, AUX_SC1); }
__device__ __forceinline__ void st8(rsrc_t rs, unsigned off, v2u v) { __builtin_amdgcn_raw_buffer_store_b64(v, rs, off, 0, AUX_SC1); }
__device__ __forceinline__ void st4(rsrc_t rs, unsigned off, unsigned v) { __builtin_amdgcn_raw_buffer_store_b32(v, rs, off, 0, AUX_SC1); }
__device__ __forceinline__ unsigned umax4(v4u v) { return max(max(v.x, v.y), max(v.z, v.w)); }

template <int N> __device__ __forceinline__ void delay_cyc() {
#pragma unroll
  for (int i = 0; i < N / 64; ++i) asm volatile("s_nop 15");
  if ((N % 64) >= 4) asm volatile("s_nop %0" :: "i"((N % 64) / 4 - 1));
}

#define PACE 0
#define PACE_GAIN 0.3f
#define PACE_CLAMP 2.0f
#define GAP_A 6
#define DB3 5
#define EXTRA_B 0
#define EXTRA_C 0
#define GAP_C 6
#define AD_DEC 0
#define AD_INC 0
__device__ __forceinline__ void delay_rt(int n) {
  asm volatile("s_cmp_lt_i32 %0, 1\n\ts_cbranch_scc1 2f\n\ts_mov_b32 vcc_lo, %0\n1:\n\ts_nop 1\n\ts_sub_i32 vcc_lo, vcc_lo, 1\n\ts_cmp_gt_i32 vcc_lo, 0\n\ts_cbranch_scc1 1b\n2:" :: "s"(n) : "vcc", "scc");
}
__device__ __forceinline__ void adapt(int& n, int misses) { n = misses ? min(n + AD_INC, 120) : max(n - AD_DEC, 0); }

__device__ __forceinline__ bool spin_fail(unsigned& spins, gu32* ctrl, unsigned code) {
  ++spins;
  if (spins > SPIN_CAP) { __hip_atomic_store(ctrl + W_TMO, 1u, RLX_AGENT); __hip_atomic_fetch_max(ctrl + W_TMO + 1, code, RLX_AGENT); return true; }
  return (spins & 255u) == 0u && __builtin_amdgcn_readfirstlane(__hip_atomic_load(ctrl + W_TMO, RLX_AGENT)) != 0u;
}
constexpr int RETRY_CAP = 1 << 15;
__device__ __forceinline__ void wait_ge(gu32* ctrl, int word, unsigned want, unsigned code, bool& dead) {
  if (dead) return;
  for (unsigned sp = 0;;) {
    if ((unsigned)__builtin_amdgcn_readfirstlane(__hip_atomic_load(ctrl + word, RLX_AGENT)) >= want) return;
    __builtin_amdgcn_s_sleep(4);
    if (spin_fail(sp, ctrl, code)) { dead = true; return; }
  }
}
__device__ __forceinline__ void wait_flags2(gu32* ctrl, int w0, int w1, unsigned code, bool& dead) {
  if (dead) return;
  for (unsigned sp = 0;;) {
    const unsigned long long a = __hip_atomic_load((GAS unsigned long long*)(ctrl + w0), RLX_AGENT);
    const unsigned long long b = __hip_atomic_load((GAS unsigned long long*)(ctrl + w1), RLX_AGENT);
    const unsigned long long m = a & b;
    if (__builtin_amdgcn_readfirstlane((unsigned)m & (unsigned)(m >> 32)) != 0u) return;
    __builtin_amdgcn_s_sleep(4);
    if (spin_fail(sp, ctrl, code)) { dead = true; return; }
  }
}
__device__ __forceinline__ void wait_lines(gu32* ctrl, int word, int stride, int n, unsigned want, int lane, unsigned code, bool& dead) {
  if (dead) return;
  gu32* mine = ctrl + word + (lane < n ? lane : 0) * stride;
  for (unsigned sp = 0;;) {
    if (__all(__hip_atomic_load(mine, RLX_AGENT) >= want)) return;
    __builtin_amdgcn_s_sleep(2);
    if (spin_fail(sp, ctrl, code)) { dead = true; return; }
  }
}
constexpr int ARM_AHEAD = 4;
__device__ __forceinline__ void arm_share(rsrc_t rs, unsigned al, int c, int tid) {
  const v4u s = {SENT, SENT, SENT, SENT};
  const unsigned lo = (tid & 63) * 16, w = tid >> 6;
  constexpr int NB = PR / 64;
  for (int sl = 0; sl < NSLOT; ++sl)
#pragma unroll
    for (int k = 0; k < NB; ++k) st16(rs, al + OFF_PART + sl * PART_SZ + (c * NB + k) * 8192 + w * 1024 + lo, s);
  if (w < 6) {
#pragma unroll
    for (int k = 0; k < NB; ++k) st16(rs, al + OFF_QKV + (c * NB + k) * 8192 + w * 1024 + lo, s);
  } else if (w == 6) st16(rs, al + OFF_MEM + MEM_ST + c * 1024 + lo, s);
  else if (c < 32) st16(rs, al + OFF_MEM + 5 * MEM_ST + c * 1024 + lo, s);
}
struct Near {
  unsigned al, va21, nz, k;
  __device__ __forceinline__ unsigned rel(int c) const { return k >> 2; }
  __device__ __forceinline__ unsigned blk(unsigned pair_off, unsigned iod) const { return al + pair_off + ((((va21 + pair_off) >> 21) ^ nz ^ iod) & 1u) * 8192u; }
};
__device__ __forceinline__ Near near_init(const u8* ws) {
  Near n; const unsigned lo = (unsigned)(size_t)ws;
  n.al = (0u - lo) & 16383u; n.va21 = lo + n.al; n.nz = 0; n.k = 0; return n;
}
__device__ __forceinline__ unsigned xcc_id() { return (unsigned)__builtin_amdgcn_s_getreg(20 | (3 << 11)) & 7u; }
__device__ __forceinline__ void near_wait(gu32* ctrl, Near& n, bool& dead) {
  wait_ge(ctrl, W_XCC, 16u, 0x11u, dead);
  n.k = __builtin_amdgcn_readfirstlane(__hip_atomic_load(ctrl + W_XCC, RLX_AGENT)) & 7u;
  n.nz = __builtin_amdgcn_readfirstlane(__hip_atomic_load(ctrl + W_CFG, RLX_AGENT)) & 1u;
}

#define CSTAMP(wv, slot)
#define PT(id)
#define CTRIES(slot, n)
#define WSTAMP()
template <bool SC1>
__device__ __forceinline__ void dma16(const void* base, unsigned voff, unsigned lds_addr) {
  unsigned keep;
  if (SC1) asm volatile("s_mov_b32 %0, m0\n\ts_mov_b32 m0, %3\n\ts_nop 0\n\tglobal_load_lds_dwordx4 %1, %2 sc1\n\ts_mov_b32 m0, %0" : "=&s"(keep) : "v"(voff), "s"(base), "s"(lds_addr) : "memory");
  else asm volatile("s_mov_b32 %0, m0\n\ts_mov_b32 m0, %3\n\ts_nop 0\n\tglobal_load_lds_dwordx4 %1, %2\n\ts_mov_b32 m0, %0" : "=&s"(keep) : "v"(voff), "s"(base), "s"(lds_addr) : "memory");
}

template <int N> __device__ __forceinline__ void wait_vm() { static_assert(N >= 0 && N < 64, "vmcnt"); asm volatile("s_waitcnt vmcnt(%0)" :: "i"(N) : "memory"); }
template <int V> struct IC { static constexpr int v = V; };
template <int N, int I = 0, class F> __device__ __forceinline__ void static_for(F&& f) { if constexpr (I < N) { f(IC<I>{}); static_for<N, I + 1>(f); } }
__device__ __forceinline__ void slice_dma(const u16* src, int ld, unsigned dst, int lane) {
  const unsigned vo = ((lane >> 2) * ld + 8 * (lane & 3)) * 2;
#pragma unroll
  for (int p = 0; p < 8; ++p) dma16<false>(src + (size_t)16 * p * ld, vo, dst + p * 1024);
}
template <bool PERM>
__device__ __forceinline__ void slice_frags(unsigned base, int lane, v8u (&f)[2][2]) {
  const int gB = lane >> 4, q = (lane & 15) >> 2, p = lane & 3;
#pragma unroll
  for (int cc = 0; cc < 2; ++cc)
#pragma unroll
    for (int rr = 0; rr < 4; ++rr) {
      const int kp = 64 * cc + 32 * (rr >> 1) + 16 * (gB >> 1) + 4 * (gB & 1) + 2 * (rr & 1) + (q & 1) + 8 * (q >> 1);
      const int row = PERM ? ((kp & ~31) + 16 * (kp & 1) + ((kp & 31) >> 1)) : kp;
#pragma unroll
      for (int tb = 0; tb < 2; ++tb) {
        const v2u v = trread(base + row * 64 + tb * 32 + p * 8);
        f[tb][cc][2 * rr] = v.x; f[tb][cc][2 * rr + 1] = v.y;
      }
    }
}

__device__ __forceinline__ void chain_role(const Params& p, LAS u8* L, rsrc_t rs, int b) {
  const int tid = threadIdx.x, lane = tid & 63, w = __builtin_amdgcn_readfirstlane(tid >> 6);
  const int c = (b & 3) * 32 + (b >> 3);
  const int h = c >> 2, j = c & 3;
  const int dbgi = (c >> 4) + 8 * (c & 15); (void)dbgi;
  const int r16 = lane & 15, g = lane >> 4;
  gu32* ctrl = (gu32*)p.ws;
  bool dead = false;
  const unsigned lbase = (unsigned)(size_t)L;
  Near nr = near_init(p.ws);
  if (b == 0 && tid == 0) __hip_atomic_store(ctrl + W_XCC, 16u | xcc_id(), RLX_AGENT);
  arm_share(rs, nr.al, c, tid);

  v8u W1[3][2][2];
  v8u WO[2][2];
  {
    const unsigned mine = lbase + w * 16384;
    const size_t wcol = (size_t)h * 128 + 32 * j;
    slice_dma(p.Wk + (size_t)128 * w * HD + wcol, HD, mine, lane);
    slice_dma(p.Wv + (size_t)128 * w * HD + wcol, HD, mine + 8192, lane);
    asm volatile("s_waitcnt vmcnt(8)" ::: "memory");
    slice_frags<true>(lbase + w * 16384, lane, W1[1]);
    WAIT_LGKM(); __builtin_amdgcn_sched_barrier(0);
    slice_dma(p.Wq + (size_t)128 * w * HD + wcol, HD, mine, lane);
    asm volatile("s_waitcnt vmcnt(8)" ::: "memory");
    slice_frags<true>(lbase + w * 16384 + 8192, lane, W1[2]);
    WAIT_LGKM(); __builtin_amdgcn_sched_barrier(0);
    slice_dma(p.Wo + (size_t)128 * h * DM + 256 * j + 32 * w, DM, mine + 8192, lane);
    asm volatile("s_waitcnt vmcnt(8)" ::: "memory");
    slice_frags<true>(lbase + w * 16384, lane, W1[0]);
    WAIT_LGKM(); __builtin_amdgcn_sched_barrier(0);
    DRAIN_VM();
    slice_frags<false>(lbase + w * 16384 + 8192, lane, WO);
    WAIT_LGKM(); __builtin_amdgcn_sched_barrier(0);
  }
  wgbar();
  if (tid == 0) __hip_atomic_store(ctrl + W_ARM + c * LN, 1u, RLX_AGENT);
  if (w < 2) wait_lines(ctrl, W_ARM + w * 64 * LN, LN, 64, 1u, lane, 0x10u, dead);
  wgbar();
  near_wait(ctrl, nr, dead);
  const unsigned myrel = nr.rel(c);

  LAS float* RED = (LAS float*)L;
  LAS u8* AHF = L + 24576;
  volatile LAS unsigned* FOK = (volatile LAS unsigned*)(L + 26624 + 4096);
  LAS u8* KVL = L + 49152;
  const int sidx = r16 < 8 ? 0x4444 : 0xEEEE;
  const int gg = g & 1, hi = g >> 1, rp = 2 * gg + hi;
  int gone = dead ? 1 : 0;

  auto fetch_kv_issue = [&](int s, int lo, int hi) {
    if ((s & 3) == 0) { bool d = gone != 0; const int blk = s >> 2; wait_flags2(ctrl, W_FKV + blk * 128 + 2 * h, W_FKV + blk * 128 + 64 + 2 * h, 0x20u, d); gone = __builtin_amdgcn_readfirstlane(d ? 1 : 0); }
    CFENCE();
    const u8* kb = p.ws + OFF_KX + (size_t)(s * NH + h) * 8192u;
    const unsigned dst = lbase + 49152 + (s & 1) * 16384;
#pragma unroll
    for (int i = lo; i < hi; ++i) dma16<true>(kb + (i >> 3) * (OFF_VX - OFF_KX) + (i & 7) * 1024, lane * 16, dst + i * 1024);
  };
  if (tid == 0) { FOK[0] = 0u; FOK[1] = 0u; }
  if (w == 5) { fetch_kv_issue(0, 0, 16); DRAIN_VM(); }
  int dA = W_A / 20, dB = W_B / 20, dC = W_C / 20;
  wgbar();

  auto run = [&](auto role) {
    constexpr int ROLE = decltype(role)::v;
    LAS f32x2* redw = (LAS f32x2*)(RED + w * 768) + (rp * 16 + r16) * 2;
    const int R = 32 * j + 4 * w + (r16 >> 2);
    const unsigned rrel = nr.rel(R), part_w = (R & (PR - 1)) * 16384u, part_l = h * 256 + rp * 64 + (r16 & 3) * 16;
    const unsigned pub_l = ROLE == 0 ? j * 1024 + (8 * w + 2 * g + (r16 & 1) + 16 * (r16 >> 2)) * 16 + (r16 & 2) * 4 : 4096 + (32 * j + r16) * 16 + g * 4;
    f32x4 acc[6];
    unsigned qo, a_part, mo, a_pub;
    int slot = 0, lim = gone ? 0 : RETRY_CAP;
    auto prepare = [&](int s, int sl) {
#pragma unroll
      for (int t = 0; t < 6; ++t) { acc[t] = f32x4{0.f, 0.f, 0.f, 0.f}; asm volatile("" : "+v"(acc[t])); }
      qo = nr.blk(OFF_QKV + (unsigned)(s * QH + (h & (QH - 1))) * 16384u, myrel);
      a_part = nr.blk(OFF_PART + (unsigned)sl * PART_SZ + part_w, rrel) + part_l;
      mo = nr.blk(OFF_MEM + (unsigned)s * MEM_ST + (w >> 2) * 16384u, myrel) + (w & 3) * 2048 + lane * 16;
      a_pub = qo + pub_l;
      asm volatile("" : "+v"(a_part), "+v"(mo), "+v"(a_pub));
    };
    prepare(0, 0);
    for (int s = 0;; ++s) {
      PT(0);
      delay_rt(dA);
      PT(1);
      v4u a0, a1;
      {
        int tries = 1;
        CFENCE();
        a0 = ld16(rs, mo); a1 = ld16(rs, mo + 1024);
        if (__builtin_expect(__ballot(max(umax4(a0), umax4(a1)) == SENT) != 0ull, 0)) {
          bool again;
          do {
            CFENCE();
            const bool b0 = umax4(a0) == SENT, b1 = umax4(a1) == SENT;
            const v4u n0 = ld16(rs, b0 ? mo : 0xFFFFFF00u), n1 = ld16(rs, b1 ? mo + 1024 : 0xFFFFFF00u);
            a0 = b0 ? n0 : a0; a1 = b1 ? n1 : a1;
            again = (__ballot(max(umax4(a0), umax4(a1)) == SENT) != 0ull) & (tries < lim);
            ++tries;
          } while (again);
        }
        lim = tries > lim ? 0 : lim;
        adapt(dA, tries - 1);
        if (w == 0) CTRIES(0, tries - 1);
      }
      CSTAMP(0, 0);
      PT(2);
#pragma unroll
      for (int t = 0; t < 6; ++t) acc[t] = smf64(a0, W1[t >> 1][t & 1][0], acc[t], sidx);
#pragma unroll
      for (int t = 0; t < 6; ++t) acc[t] = smf64(a1, W1[t >> 1][t & 1][1], acc[t], sidx);
#pragma unroll
      for (int t = 0; t < 6; ++t) redw[(t >> 1) * 128 + (t & 1)] = fold32(acc[t]);
      PT(3);
      wgbar();
      PT(4);
      CSTAMP(0, 1);
      if (ROLE <= 2) {
        const LAS f32x4* redr = (const LAS f32x4*)RED + w * 64 + lane;
        f32x4 t0 = redr[0], t1 = redr[192];
#pragma unroll
        for (int k = 2; k < 8; k += 2) { t0 += redr[k * 192]; t1 += redr[(k + 1) * 192]; }
        t0 += t1;
        if (ROLE == 0) {
          const unsigned d0 = pk2(t0[0], t0[2]), d1 = pk2(t0[1], t0[3]);
          const unsigned got = __builtin_amdgcn_mov_dpp((r16 & 1) ? d0 : d1, 0xB1, 0xF, 0xF, true);
          st8(rs, a_pub, (r16 & 1) ? v2u{canon(got), canon(d1)} : v2u{canon(d0), canon(got)});
        } else {
          st4(rs, a_pub, canon(pk2(t0[0], t0[1])));
          st4(rs, a_pub + 256, canon(pk2(t0[2], t0[3])));
        }
      }
      CSTAMP(0, 2);
      PT(5);
      if (s == NSEG - 1) break;
      acc[0] = f32x4{0.f, 0.f, 0.f, 0.f}; acc[1] = acc[0];
      asm volatile("" : "+v"(acc[0]), "+v"(acc[1]));
      if (ROLE <= 3) {
        v4u KX[2][4], VX[2], F[4]; v2u VM[2];
        const LAS u8* kv = KVL + (s & 1) * 16384 + lane * 16;
#pragma unroll
        for (int i = 0; i < 8; ++i) KX[i >> 2][i & 3] = *(const LAS v4u*)(kv + i * 1024);
        VX[0] = *(const LAS v4u*)(kv + 8192 + 2 * w * 1024); VX[1] = *(const LAS v4u*)(kv + 8192 + (2 * w + 1) * 1024);
        const unsigned fo = qo + lane * 16, vo = qo + 4096 + (32 * w + r16) * 16 + gg * 8;
        __builtin_amdgcn_sched_barrier(0);
        delay_rt((ROLE == 3 ? dB + DB3 : dB));
        PT(6);
        {
          int tries = 0; bool again;
          do {
            CFENCE();
#pragma unroll
            for (int u = 0; u < 4; ++u) F[u] = ld16(rs, fo + u * 1024);
            VM[0] = ld8(rs, vo); VM[1] = ld8(rs, vo + 256);
            const unsigned m = max(max(max(umax4(F[0]), umax4(F[1])), max(umax4(F[2]), umax4(F[3]))), max(max(VM[0].x, VM[0].y), max(VM[1].x, VM[1].y)));
            again = (__ballot(m == SENT) != 0ull) & (tries < lim);
            ++tries;
          } while (__builtin_expect(again, 0));
          lim = tries > lim ? 0 : lim;
          adapt(dB, tries - 1);
          if (w == 0) CTRIES(1, tries - 1);
        }
        if (ROLE == 2) FOK[0] = (unsigned)(s + 1);
        CSTAMP(0, 3);
        PT(7);
        const float sc2 = 0.08838834764831845f * 1.4426950408889634f;
        f32x4 sA = {0.f, 0.f, 0.f, 0.f}, sB = sA, sM = sA;
#pragma unroll
        for (int u = 0; u < 4; ++u) { sM = mfma32(F[u], F[u], sM); sA = mfma32(KX[0][u], F[u], sA); sB = mfma32(KX[1][u], F[u], sB); }
        sA *= sc2; sB *= sc2; sM *= sc2;
        if (g < 2) sM = f32x4{-INFINITY, -INFINITY, -INFINITY, -INFINITY};
        float mx = fmaxf(fmaxf(fmaxf(fmaxf(sA[0], sA[1]), sA[2]), fmaxf(fmaxf(sA[3], sB[0]), sB[1])), fmaxf(fmaxf(fmaxf(sB[2], sB[3]), sM[0]), fmaxf(fmaxf(sM[1], sM[2]), sM[3])));
        mx = xmax32(xmax16(mx));
        sA -= mx; sB -= mx; sM -= mx;
#pragma unroll
        for (int r = 0; r < 4; ++r) { sA[r] = __builtin_amdgcn_exp2f(sA[r]); sB[r] = __builtin_amdgcn_exp2f(sB[r]); sM[r] = __builtin_amdgcn_exp2f(sM[r]); }
        const v4u pb = {pk2(sA[0], sA[1]), pk2(sA[2], sA[3]), pk2(sB[0], sB[1]), pk2(sB[2], sB[3])};
        const v2u pm = {pk2(sM[0], sM[1]), pk2(sM[2], sM[3])};
        f32x4 e0 = mfma32(VX[0], pb, f32x4{0.f, 0.f, 0.f, 0.f}), e1 = mfma32(VX[1], pb, f32x4{0.f, 0.f, 0.f, 0.f});
        e0 = mfma16(VM[0], pm, e0); e1 = mfma16(VM[1], pm, e1);
        const f32x4 ss = sA + sB + sM;
        const float inv = __builtin_amdgcn_rcpf(xadd32(xadd16((ss[0] + ss[1]) + (ss[2] + ss[3]))));
        e0 *= inv; e1 *= inv;
        LAS u8* ah = AHF + (r16 < 8 ? (4 * w + hi) * 128 + r16 * 16 + gg * 8 : 4096 + lane * 8);
        *(LAS v2u*)ah = v2u{pk2(e0[0], e0[1]), pk2(e0[2], e0[3])};
        *(LAS v2u*)(ah + 256) = v2u{pk2(e1[0], e1[1]), pk2(e1[2], e1[3])};
      } else {
        for (unsigned sp = 0; sp < (1u << 20); ++sp) {
          if ((unsigned)__builtin_amdgcn_readfirstlane(FOK[0]) == (unsigned)(s + 1)) break;
          __builtin_amdgcn_s_sleep(1);
        }
        if (ROLE == 6) {
          const v4u sv = {SENT, SENT, SENT, SENT};
          if (s > 0 && NSLOT == 3) {
            const unsigned po = nr.blk(OFF_PART + (unsigned)(slot == 0 ? 2 : slot - 1) * PART_SZ + (c & (PR - 1)) * 16384u, myrel) + lane * 16;
#pragma unroll
            for (int i = 0; i < 8; ++i) st16(rs, po + i * 1024, sv);
          }
          if (s + ARM_AHEAD < NSEG) {
            const unsigned qn = nr.blk(OFF_QKV + (unsigned)((s + ARM_AHEAD) * QH + (h & (QH - 1))) * 16384u, myrel);
            st16(rs, qn + j * 1024 + lane * 16, sv);
            if (lane < 32) st16(rs, qn + 4096 + j * 512 + lane * 16, sv);
            else if (lane < 48 && s + ARM_AHEAD + 2 < NSEG)
              st16(rs, nr.al + OFF_MEM + (unsigned)(s + ARM_AHEAD + 2) * MEM_ST + (c >> 6) * 16384u + ((lane >> 3) & 1) * 8192u + (c & 63) * 128 + (lane & 7) * 16, sv);
          }
          if (j == 0 && lane == 0) __hip_atomic_store(ctrl + W_PROG + LN * h, (unsigned)(s + 1), RLX_AGENT);
        } else {
          fetch_kv_issue(s + 1, ROLE == 4 ? 0 : ROLE == 5 ? 6 : 12, ROLE == 4 ? 6 : ROLE == 5 ? 12 : 16);
        }
      }
      PT(9);
      wgbar();
      PT(10);
      CSTAMP(7, 4);
      {
        const v4u b0 = *(LAS v4u*)(AHF + lane * 16), b1 = *(LAS v4u*)(AHF + 1024 + lane * 16);
        acc[0] = smf64(b0, WO[0][0], acc[0], sidx); acc[1] = smf64(b0, WO[1][0], acc[1], sidx);
        acc[0] = smf64(b1, WO[0][1], acc[0], sidx); acc[1] = smf64(b1, WO[1][1], acc[1], sidx);
        const f32x2 f0 = fold32(acc[0]), f1 = fold32(acc[1]);
        const v4u pv = {canon(f2u(f0[0])), canon(f2u(f1[0])), canon(f2u(f0[1])), canon(f2u(f1[1]))};
        st16(rs, a_part, pv);
      }
      __builtin_amdgcn_sched_barrier(0);
      CSTAMP(7, 5);
      PT(11);
      const int nslot = slot == NSLOT - 1 ? 0 : slot + 1;
      if (ROLE == 7) {
        unsigned po = nr.blk(OFF_PART + (unsigned)slot * PART_SZ + (c & (PR - 1)) * 16384u, myrel) + lane * 16;
        unsigned a_mem = nr.al + OFF_MEM + (unsigned)(s + 1) * MEM_ST + (c >> 6) * 16384u + gg * 8192u
                         + (c & 63) * 128 + (2 * (r16 >> 2) + hi) * 16 + (r16 & 3) * 4;
        prepare(s + 1, nslot);
        asm volatile("" : "+v"(a_mem), "+v"(po));
        delay_rt(dC);
        PT(12);
        v4u pr[8];
        {
          int tries = 1;
          CFENCE();
#pragma unroll
          for (int i = 0; i < 8; ++i) pr[i] = ld16(rs, po + i * 1024);
          unsigned m = 0;
#pragma unroll
          for (int i = 0; i < 8; ++i) m = max(m, umax4(pr[i]));
          if (__builtin_expect(__ballot(m == SENT) != 0ull, 0)) {
            bool again;
            do {
              CFENCE();
              m = 0;
#pragma unroll
              for (int i = 0; i < 8; ++i) {
                const bool bad = umax4(pr[i]) == SENT;
                const v4u n = ld16(rs, bad ? po + i * 1024 : 0xFFFFFF00u);
                pr[i] = bad ? n : pr[i];
              }
#pragma unroll
              for (int i = 0; i < 8; ++i) m = max(m, umax4(pr[i]));
              again = (__ballot(m == SENT) != 0ull) & (tries < lim);
              ++tries;
            } while (again);
          }
          lim = tries > lim ? 0 : lim;
          adapt(dC, tries - 1);
          CTRIES(2, tries - 1);
        }
        CSTAMP(7, 6);
        PT(13);
        const f32x4 t = ((__builtin_bit_cast(f32x4, pr[0]) + __builtin_bit_cast(f32x4, pr[1])) + (__builtin_bit_cast(f32x4, pr[2]) + __builtin_bit_cast(f32x4, pr[3]))) +
                        ((__builtin_bit_cast(f32x4, pr[4]) + __builtin_bit_cast(f32x4, pr[5])) + (__builtin_bit_cast(f32x4, pr[6]) + __builtin_bit_cast(f32x4, pr[7])));
        const f32x2 f = fold32(t);
        const unsigned md = canon(pk2(xadd16(f[0]), xadd16(f[1])));
        st4(rs, a_mem, md);
      } else {
        prepare(s + 1, nslot);
        if (ROLE >= 4) DRAIN_VM();
      }
      slot = nslot;
      PT(14);
      wgbar();
      CSTAMP(7, 7);
    }
    if (lim == 0 && lane == 0) __hip_atomic_store(ctrl + W_TMO, 1u, RLX_AGENT);
  };
  switch (w) {
    case 0: case 1: run(IC<0>{}); break;
    case 2: run(IC<2>{}); break;
    case 3: run(IC<3>{}); break;
    case 4: run(IC<4>{}); break;
    case 5: run(IC<5>{}); break;
    case 6: run(IC<6>{}); break;
    default: run(IC<7>{}); break;
  }
}

enum { M_K = 0, M_V = 1, M_O = 2 };

template <int BN, int MODE>
__device__ __forceinline__ void gemm_tile(LAS u8* L, const u16* A, int lda, const u16* W, int ldw, int nkt, f32x4 (&acc)[4][BN / 64]) {
  constexpr int NT = BN / 64, NCG = BN / 64, STG = 16384 + BN * 128, NB = BN / 64;
  constexpr int NSTG = LDS_BYTES / STG;
  const int tid = threadIdx.x, lane = tid & 63, w = __builtin_amdgcn_readfirstlane(tid >> 6);
  const int wm = w >> 2, wn = w & 3, r16 = lane & 15, g = lane >> 4, q = r16 >> 2, pp = lane & 3;
  const int arow = 16 * w + (lane >> 3);
  const unsigned avo0 = (arow * lda + 8 * ((lane & 7) ^ ((arow >> 1) & 7))) * 2;
  const unsigned avo1 = ((arow + 8) * lda + 8 * ((lane & 7) ^ (((arow + 8) >> 1) & 7))) * 2;
  const int kr = 8 * w + (lane >> 3);
  const int krs = MODE == M_O ? ((kr & ~31) + 16 * ((kr >> 2) & 1) + 4 * ((kr >> 3) & 3) + (kr & 3)) : kr;
  const int fsw = ((kr >> 1) & 1) | (((kr >> 3) & 1) << 1);
  const unsigned wvo = (krs * ldw + 16 * (((lane & 7) >> 1) ^ fsw) + 8 * (lane & 1)) * 2;
  const unsigned lb = (unsigned)(size_t)L;
  auto stage = [&](int kt, int buf) {
    const unsigned sb = lb + buf * STG;
    if (MODE == M_O) {
      const u8* ab = (const u8*)A + (size_t)((w >> 1) * NH + (kt >> 1)) * 8192 + (w & 1) * 4096 + (kt & 1) * 2048;
      dma16<false>(ab, lane * 16, sb + (2 * w) * 1024);
      dma16<false>(ab + 1024, lane * 16, sb + (2 * w + 1) * 1024);
    } else {
      dma16<false>(A + kt * 64, avo0, sb + (2 * w) * 1024);
      dma16<false>(A + kt * 64, avo1, sb + (2 * w + 1) * 1024);
    }
#pragma unroll
    for (int i = 0; i < NB; ++i) dma16<false>(W + (size_t)kt * 64 * ldw + 64 * i, wvo, sb + 16384 + (w * NB + i) * 1024);
  };
  const unsigned aoff = lb + (64 * wm + r16) * 128 + ((g ^ ((r16 >> 1) & 7)) * 16);
  const int fr = ((q >> 1) & 1) | ((g & 1) << 1);
  const int cgw = (wn * NT) >> 2, c0 = (wn * NT) & 3;
  unsigned boff[NT];
#pragma unroll
  for (int n = 0; n < NT; ++n) boff[n] = lb + 16384 + (g * NCG + cgw) * 1024 + q * 128 + (((c0 + n) ^ fr) * 32) + pp * 8;
#pragma unroll
  for (int m = 0; m < 4; ++m)
#pragma unroll
    for (int n = 0; n < NT; ++n) acc[m][n] = f32x4{0.f, 0.f, 0.f, 0.f};

  auto compute = [&](int buf) {
    const unsigned so = buf * STG;
    v4u af[2][4], bf[2][NT];
#pragma unroll
    for (int ks = 0; ks < 2; ++ks) {
#pragma unroll
      for (int n = 0; n < NT; ++n) {
        const v2u x0 = trread(boff[n] + so + ks * 4 * NCG * 1024), x1 = trread(boff[n] + so + ks * 4 * NCG * 1024 + 512);
        bf[ks][n] = v4u{x0.x, x0.y, x1.x, x1.y};
      }
#pragma unroll
      for (int m = 0; m < 4; ++m)
        af[ks][m] = MODE == M_O ? *(LAS v4u*)(size_t)(lb + so + wm * 8192 + lane * 16 + m * 2048 + ks * 1024) : *(LAS v4u*)(size_t)(((aoff + so) ^ (ks * 64)) + m * 2048);
    }
    __builtin_amdgcn_sched_barrier(0);
#pragma unroll
    for (int ks = 0; ks < 2; ++ks)
#pragma unroll
      for (int m = 0; m < 4; ++m)
#pragma unroll
        for (int n = 0; n < NT; ++n) acc[m][n] = MODE == M_V ? mfma32(af[ks][m], bf[ks][n], acc[m][n]) : mfma32(bf[ks][n], af[ks][m], acc[m][n]);
    WAIT_LGKM();
  };
  wgbar();
#pragma unroll
  for (int i = 0; i < NSTG - 1; ++i) stage(i, i);
  int buf = 0;
  for (int kt = 0; kt < nkt - (NSTG - 1); ++kt) {
    wait_vm<(NSTG - 2) * (2 + NB)>();
    __builtin_amdgcn_s_barrier(); CFENCE();
    stage(kt + NSTG - 1, buf >= 1 ? buf - 1 : NSTG - 1);
    compute(buf);
    buf = buf == NSTG - 1 ? 0 : buf + 1;
  }
  static_for<NSTG - 1>([&](auto ic) {
    wait_vm<(NSTG - 2 - decltype(ic)::v) * (2 + NB)>();
    __builtin_amdgcn_s_barrier(); CFENCE();
    compute(buf);
    buf = buf == NSTG - 1 ? 0 : buf + 1;
  });
}

template <int BN, int MODE>
__device__ __forceinline__ void proj_task(const Params& p, LAS u8* L, rsrc_t rs, int blk, int mtx, int col0) {
  constexpr int NT = BN / 64;
  const int tid = threadIdx.x, lane = tid & 63, w = __builtin_amdgcn_readfirstlane(tid >> 6), wm = w >> 2, wn = w & 3;
  f32x4 acc[4][NT];
  const u16* W = (mtx == 0 ? p.Wk : mtx == 1 ? p.Wv : p.Wq) + col0;
  gemm_tile<BN, MODE>(L, p.x + (size_t)blk * 128 * DM, DM, W, HD, DM / 64, acc);
  const int cw = col0 + wn * (BN / 4), h = cw >> 7;
  const unsigned base = (mtx == 0 ? OFF_KX : mtx == 1 ? OFF_VX : OFF_QX) + (unsigned)((blk * 4 + 2 * wm) * NH + h) * 8192u + lane * 16;
  if (MODE == M_K) {
#pragma unroll
    for (int m = 0; m < 4; ++m) {
      const unsigned o = base + (m >> 1) * (NH * 8192u) + ((m & 1) * 4 + ((cw & 127) >> 5)) * 1024;
      if (NT == 1) {
#pragma unroll
        for (int r = 0; r < 4; ++r)
          __builtin_amdgcn_raw_buffer_store_b16((short)(pk2(acc[m][0][r], 0.f) & 0xFFFFu), rs, o + r * 4 + ((cw >> 4) & 1) * 2, 0, AUX_SC1);
      } else {
#pragma unroll
        for (int v = 0; v < NT / 2; ++v)
          st16(rs, o + v * 1024, v4u{pk2(acc[m][2 * v][0], acc[m][2 * v + 1][0]), pk2(acc[m][2 * v][1], acc[m][2 * v + 1][1]),
                                     pk2(acc[m][2 * v][2], acc[m][2 * v + 1][2]), pk2(acc[m][2 * v][3], acc[m][2 * v + 1][3])});
      }
    }
  } else {
#pragma unroll
    for (int sg = 0; sg < 2; ++sg)
#pragma unroll
      for (int n = 0; n < NT; ++n)
        st16(rs, base + sg * (NH * 8192u) + (((cw & 127) >> 4) + n) * 1024,
             v4u{pk2(acc[2 * sg][n][0], acc[2 * sg][n][1]), pk2(acc[2 * sg][n][2], acc[2 * sg][n][3]),
                 pk2(acc[2 * sg + 1][n][0], acc[2 * sg + 1][n][1]), pk2(acc[2 * sg + 1][n][2], acc[2 * sg + 1][n][3])});
  }
  DRAIN_VM();
  wgbar();
  gu32* ctrl = (gu32*)p.ws;
  if (tid < NT) {
    const int word = mtx == 2 ? W_FQ + blk * 64 + (col0 >> 6) + tid : W_FKV + blk * 128 + mtx * 64 + (col0 >> 6) + tid;
    __hip_atomic_store(ctrl + word, 1u, RLX_AGENT);
  }
}

__device__ __forceinline__ float sigmoidf_(float v) { return 1.0f / (1.0f + __expf(-v)); }

__device__ __forceinline__ void rg_task(const Params& p, rsrc_t rs, int wk) {
  const int tid = threadIdx.x, lane = tid & 63, w = __builtin_amdgcn_readfirstlane(tid >> 6);
  v4u wr[2], gw[2];
#pragma unroll
  for (int i = 0; i < 2; ++i) { wr[i] = *(const GAS v4u*)(p.wr + i * 512 + lane * 8); gw[i] = *(const GAS v4u*)(p.gw + i * 512 + lane * 8); }
#pragma unroll
  for (int t = 0; t < 2; ++t) {
    const int tok = wk * 16 + 2 * w + t;
    float r = 0.f, gs = 0.f;
#pragma unroll
    for (int i = 0; i < 2; ++i) {
      const v4u xv = *(const GAS v4u*)(p.x + (size_t)tok * DM + i * 512 + lane * 8);
#pragma unroll
      for (int d = 0; d < 4; ++d) {
        r = fmaf(bflo(xv[d]), bflo(wr[i][d]), r); r = fmaf(bfhi(xv[d]), bfhi(wr[i][d]), r);
        gs += bfr(bflo(xv[d]) * bflo(gw[i][d])); gs += bfr(bfhi(xv[d]) * bfhi(gw[i][d]));
      }
    }
    r = bfr(wave_sum(r)); gs = bfr(wave_sum(gs));
    if (lane == 0) { st4(rs, OFF_R + tok * 4, f2u(r)); st4(rs, OFF_G + tok * 4, f2u(bfr(sigmoidf_(gs)))); }
  }
  DRAIN_VM();
  wgbar();
  if (tid == 0) __hip_atomic_fetch_add((gu32*)p.ws + W_RG, 1u, RLX_AGENT);
}

__device__ __forceinline__ unsigned long long top_key(unsigned bits, int idx) {
  const unsigned o = (bits & 0x7FFFFFFFu) > 0x7F800000u ? 0u : ((bits & 0x80000000u) ? ~bits : (bits | 0x80000000u));
  return ((unsigned long long)o << 32) | (unsigned)(0x7FFFFFFF - idx);
}
__device__ __forceinline__ void top_ins(unsigned long long k, unsigned long long& a, unsigned long long& b) {
  const unsigned long long lo = k < a ? k : a;
  a = k < a ? a : k;
  b = lo > b ? lo : b;
}
__device__ __forceinline__ void top2(rsrc_t rs, LAS u8* L, int& i0, int& i1) {
  const int tid = threadIdx.x, lane = tid & 63, w = tid >> 6;
  const v4u rv = ld16(rs, OFF_R + tid * 16);
  unsigned long long a = 0ull, b = 0ull;
#pragma unroll
  for (int d = 0; d < 4; ++d) top_ins(top_key(rv[d], tid * 4 + d), a, b);
#pragma unroll
  for (int o = 1; o < 64; o <<= 1) {
    const unsigned long long c0 = __shfl_xor(a, o), c1 = __shfl_xor(b, o);
    top_ins(c0, a, b); top_ins(c1, a, b);
  }
  LAS unsigned long long* tv = (LAS unsigned long long*)L;
  wgbar();
  if (lane == 0) { tv[2 * w] = a; tv[2 * w + 1] = b; }
  wgbar();
  a = b = 0ull;
#pragma unroll
  for (int k = 0; k < 16; ++k) top_ins(tv[k], a, b);
  wgbar();
  i0 = (0x7FFFFFFF - (int)(unsigned)a) & 2047; i1 = (0x7FFFFFFF - (int)(unsigned)b) & 2047;
}

__device__ __forceinline__ void mlp1_task(const Params& p, LAS u8* L, rsrc_t rs, int t, bool& dead) {
  const int tid = threadIdx.x;
  gu32* ctrl = (gu32*)p.ws;
  wait_ge(ctrl, W_RG, NWORK, 0x40u, dead);
  int i0, i1;
  top2(rs, L, i0, i1);
  if (t == 0 && tid == 0) {
    const float r0 = u2f(ld4(rs, OFF_R + i0 * 4)), r1 = u2f(ld4(rs, OFF_R + i1 * 4));
    st16(rs, W_TOP * 4, v4u{(unsigned)i0, (unsigned)i1, f2u(bfr(sigmoidf_(r0))), f2u(bfr(sigmoidf_(r1)))});
  }
  const int rg = tid >> 3, cq = tid & 7;
  float ag[2][8], au[2][8];
#pragma unroll
  for (int e = 0; e < 8; ++e) { ag[0][e] = ag[1][e] = au[0][e] = au[1][e] = 0.f; }
#pragma unroll 4
  for (int i = 0; i < 16; ++i) {
    const int k = rg + 64 * i;
    const v4u gv = *(const GAS v4u*)(p.wg + (size_t)k * FF + 64 * t + 8 * cq), uv = *(const GAS v4u*)(p.wu + (size_t)k * FF + 64 * t + 8 * cq);
    const float x0 = bf2f(p.x[(size_t)i0 * DM + k]), x1 = bf2f(p.x[(size_t)i1 * DM + k]);
#pragma unroll
    for (int d = 0; d < 4; ++d) {
      ag[0][2 * d] = fmaf(x0, bflo(gv[d]), ag[0][2 * d]); ag[0][2 * d + 1] = fmaf(x0, bfhi(gv[d]), ag[0][2 * d + 1]);
      ag[1][2 * d] = fmaf(x1, bflo(gv[d]), ag[1][2 * d]); ag[1][2 * d + 1] = fmaf(x1, bfhi(gv[d]), ag[1][2 * d + 1]);
      au[0][2 * d] = fmaf(x0, bflo(uv[d]), au[0][2 * d]); au[0][2 * d + 1] = fmaf(x0, bfhi(uv[d]), au[0][2 * d + 1]);
      au[1][2 * d] = fmaf(x1, bflo(uv[d]), au[1][2 * d]); au[1][2 * d + 1] = fmaf(x1, bfhi(uv[d]), au[1][2 * d + 1]);
    }
  }
  LAS float* R = (LAS float*)L;
#pragma unroll
  for (int e = 0; e < 8; ++e) {
    R[(0 * 64 + rg) * 65 + 8 * cq + e] = ag[0][e]; R[(1 * 64 + rg) * 65 + 8 * cq + e] = ag[1][e];
    R[(2 * 64 + rg) * 65 + 8 * cq + e] = au[0][e]; R[(3 * 64 + rg) * 65 + 8 * cq + e] = au[1][e];
  }
  wgbar();
  if (tid < 128) {
    const int tok = tid >> 6, col = tid & 63;
    float gs = 0.f, us = 0.f;
    for (int r = 0; r < 64; ++r) { gs += R[(tok * 64 + r) * 65 + col]; us += R[((2 + tok) * 64 + r) * 65 + col]; }
    gs = bfr(gs); us = bfr(us);
    const float act = bfr(gs * sigmoidf_(gs));
    st4(rs, OFF_HID + (tok * FF + 64 * t + col) * 4, f2u(bfr(act * us)));
  }
  DRAIN_VM();
  wgbar();
  if (tid == 0) __hip_atomic_fetch_add(ctrl + W_M1, 1u, RLX_AGENT);
}

__device__ __forceinline__ void mlp2_task(const Params& p, LAS u8* L, rsrc_t rs, int t, bool& dead) {
  const int tid = threadIdx.x;
  gu32* ctrl = (gu32*)p.ws;
  wait_ge(ctrl, W_M1, 56, 0x41u, dead);
  CFENCE();
  const int kc = t >> 2, cb = t & 3, rg = tid >> 5, cq = tid & 31;
  float a[2][8];
#pragma unroll
  for (int e = 0; e < 8; ++e) a[0][e] = a[1][e] = 0.f;
#pragma unroll 4
  for (int i = 0; i < 16; ++i) {
    const int k = 256 * kc + rg + 16 * i;
    const v4u dv = *(const GAS v4u*)(p.wd + (size_t)k * DM + 256 * cb + 8 * cq);
    const float h0 = u2f(ld4(rs, OFF_HID + k * 4)), h1 = u2f(ld4(rs, OFF_HID + (FF + k) * 4));
#pragma unroll
    for (int d = 0; d < 4; ++d) {
      a[0][2 * d] = fmaf(h0, bflo(dv[d]), a[0][2 * d]); a[0][2 * d + 1] = fmaf(h0, bfhi(dv[d]), a[0][2 * d + 1]);
      a[1][2 * d] = fmaf(h1, bflo(dv[d]), a[1][2 * d]); a[1][2 * d + 1] = fmaf(h1, bfhi(dv[d]), a[1][2 * d + 1]);
    }
  }
  LAS float* R = (LAS float*)L;
  wgbar();
#pragma unroll
  for (int e = 0; e < 8; ++e) { R[(0 * 16 + rg) * 257 + 8 * cq + e] = a[0][e]; R[(1 * 16 + rg) * 257 + 8 * cq + e] = a[1][e]; }
  wgbar();
  {
    const int tok = tid >> 8, col = tid & 255;
    float sacc = 0.f;
#pragma unroll
    for (int r = 0; r < 16; ++r) sacc += R[(tok * 16 + r) * 257 + col];
    st4(rs, OFF_YP + ((kc * 2 + tok) * DM + 256 * cb + col) * 4, f2u(sacc));
  }
  DRAIN_VM();
  wgbar();
  if (tid == 0) __hip_atomic_store(ctrl + W_M2 + t * LN, 1u, RLX_AGENT);
}

template <int QT0, int QT1, int V0, int V1, bool CHECK>
__device__ __forceinline__ void attn_task(const Params& p, rsrc_t rs, const Near& nr, int s, int h, bool& dead) {
  const int lane = threadIdx.x & 63, r16 = lane & 15, g = lane >> 4, gg = g & 1;
  gu32* ctrl = (gu32*)p.ws;
  const int blk = s >> 2;
  if (CHECK) {
    wait_flags2(ctrl, W_FKV + blk * 128 + 2 * h, W_FKV + blk * 128 + 64 + 2 * h, 0x50u, dead);
    wait_flags2(ctrl, W_FQ + blk * 64 + 2 * h, W_FQ + blk * 64 + 2 * h, 0x51u, dead);
  }
  CFENCE();
  const unsigned fo = (unsigned)(s * NH + h) * 8192u + lane * 16;
  v4u KX[2][4], QX[2][4], VX[8], F[4]; v2u VM[8];
#pragma unroll
  for (int i = 0; i < 8; ++i) {
    if ((i >> 2) <= QT1) KX[i >> 2][i & 3] = ld16(rs, OFF_KX + fo + i * 1024);
    if ((i >> 2) >= QT0 && (i >> 2) <= QT1) QX[i >> 2][i & 3] = ld16(rs, OFF_QX + fo + i * 1024);
    if (i >= 2 * V0 && i < 2 * V1) VX[i] = ld16(rs, OFF_VX + fo + i * 1024);
  }
  wait_ge(ctrl, W_PROG + LN * h, (unsigned)(s + 1 < NSEG - 1 ? s + 1 : NSEG - 1), 0x52u, dead);
  const unsigned qo = nr.blk(OFF_QKV + (unsigned)(s * QH + (h & (QH - 1))) * 16384u, nr.rel(h * 4));
  for (unsigned sp = 0;;) {
    CFENCE();
    unsigned m = 0;
#pragma unroll
    for (int u = 0; u < 4; ++u) { F[u] = ld16(rs, qo + u * 1024 + lane * 16); m = max(m, umax4(F[u])); }
#pragma unroll
    for (int e = 2 * V0; e < 2 * V1; ++e) { VM[e] = ld8(rs, qo + 4096 + (16 * e + r16) * 16 + gg * 8); m = max(m, max(VM[e].x, VM[e].y)); }
    if (__all(m != SENT) || dead) break;
    if (spin_fail(sp, ctrl, 0x53u)) { dead = true; break; }
  }
  const float scale = 0.08838834764831845f, l2e = 1.4426950408889634f;
#pragma unroll
  for (int qt = QT0; qt <= QT1; ++qt) {
    f32x4 sA = {0.f, 0.f, 0.f, 0.f}, sB = sA, sM = sA;
#pragma unroll
    for (int u = 0; u < 4; ++u) { sM = mfma32(F[u], QX[qt][u], sM); sA = mfma32(KX[0][u], QX[qt][u], sA); if (qt == 1) sB = mfma32(KX[1][u], QX[qt][u], sB); }
    const float neg = -1000000000.0f;
#pragma unroll
    for (int r = 0; r < 4; ++r) {
      sA[r] = bfr(bfr(sA[r]) * scale); sB[r] = bfr(bfr(sB[r]) * scale); sM[r] = bfr(bfr(sM[r]) * scale);
      const int kx = 4 * g + r;
      if (qt == 0) { if (kx > r16) sA[r] = bfr(neg); sB[r] = bfr(neg); }
      else { if (kx > r16) sB[r] = bfr(neg); }
      if (g < 2) sM[r] = -INFINITY;
    }
    float mx = fmaxf(fmaxf(fmaxf(sA[0], sA[1]), fmaxf(sA[2], sA[3])), fmaxf(fmaxf(sB[0], sB[1]), fmaxf(sB[2], sB[3])));
    mx = fmaxf(mx, fmaxf(fmaxf(sM[0], sM[1]), fmaxf(sM[2], sM[3])));
    mx = xmax32(xmax16(mx));
    float sum = 0.f;
#pragma unroll
    for (int r = 0; r < 4; ++r) {
      sA[r] = __builtin_amdgcn_exp2f((sA[r] - mx) * l2e); sB[r] = __builtin_amdgcn_exp2f((sB[r] - mx) * l2e); sM[r] = __builtin_amdgcn_exp2f((sM[r] - mx) * l2e);
      sum += sA[r] + sB[r] + sM[r];
    }
    sum = xadd32(xadd16(sum));
    const float inv = 1.0f / sum;
    const v4u pb = {pk2(sA[0] * inv, sA[1] * inv), pk2(sA[2] * inv, sA[3] * inv), pk2(sB[0] * inv, sB[1] * inv), pk2(sB[2] * inv, sB[3] * inv)};
    const v2u pm = {pk2(sM[0] * inv, sM[1] * inv), pk2(sM[2] * inv, sM[3] * inv)};
    const unsigned ao = OFF_AX + fo + qt * 4096;
#pragma unroll
    for (int v = V0; v < V1; ++v) {
      f32x4 o0 = mfma32(VX[2 * v], pb, f32x4{0.f, 0.f, 0.f, 0.f}), o1 = mfma32(VX[2 * v + 1], pb, f32x4{0.f, 0.f, 0.f, 0.f});
      o0 = mfma16(VM[2 * v], pm, o0); o1 = mfma16(VM[2 * v + 1], pm, o1);
      st16(rs, ao + v * 1024, v4u{pk2(o0[0], o0[1]), pk2(o0[2], o0[3]), pk2(o1[0], o1[1]), pk2(o1[2], o1[3])});
    }
  }
  DRAIN_VM();
}

__device__ __forceinline__ void mix_store(const Params& p, rsrc_t rs, const v4u& top, int tok, int col, f32x4 a) {
  const float gt = u2f(ld4(rs, OFF_G + tok * 4)), og = bfr(1.0f - gt);
  const int sel = tok == (int)top.x ? 0 : tok == (int)top.y ? 1 : -1;
  const v2u xv = *(const GAS v2u*)(p.x + (size_t)tok * DM + col);
  float bv[4] = {bflo(xv.x), bfhi(xv.x), bflo(xv.y), bfhi(xv.y)};
  if (sel >= 0) {
    f32x4 y = {0.f, 0.f, 0.f, 0.f};
    for (int kc = 0; kc < 14; ++kc) y += __builtin_bit_cast(f32x4, ld16(rs, OFF_YP + ((kc * 2 + sel) * DM + col) * 4));
    const float rsel = u2f(sel == 0 ? top.z : top.w);
#pragma unroll
    for (int r = 0; r < 4; ++r) bv[r] = bfr(bv[r] + bfr(bfr(y[r]) * rsel));
  }
  float o[4];
#pragma unroll
  for (int r = 0; r < 4; ++r) o[r] = bfr(gt * bfr(a[r])) + bfr(og * bv[r]);
  *(GAS v2u*)(p.out + (size_t)tok * DM + col) = v2u{pk2(o[0], o[1]), pk2(o[2], o[3])};
}

__device__ __forceinline__ void oproj_task(const Params& p, LAS u8* L, rsrc_t rs, int blk, int ct, bool& dead) {
  const int tid = threadIdx.x, lane = tid & 63, w = __builtin_amdgcn_readfirstlane(tid >> 6), wm = w >> 2, wn = w & 3, r16 = lane & 15, g = lane >> 4;
  gu32* ctrl = (gu32*)p.ws;
  if (w == 0) {
    wait_lines(ctrl, W_M2, LN, 56, 1u, lane, 0x60u, dead);
    wait_lines(ctrl, W_T2 + 8 * blk * LN, LN, 8, 1u, lane, 0x61u, dead);
    __builtin_amdgcn_fence(__ATOMIC_ACQUIRE, "agent");
    DRAIN_VM();
  }
  f32x4 acc[4][2];
  gemm_tile<128, M_O>(L, (const u16*)(p.ws + OFF_AX + (size_t)blk * 4 * NH * 8192), HD, p.Wo + ct * 128, DM, HD / 64, acc);
  const v4u top = ld16(rs, W_TOP * 4);
#pragma unroll
  for (int m = 0; m < 4; ++m)
#pragma unroll
    for (int n = 0; n < 2; ++n) mix_store(p, rs, top, blk * 128 + 64 * wm + 16 * m + r16, ct * 128 + 32 * wn + 16 * n + 4 * g, acc[m][n]);
}

__device__ __forceinline__ void stream_role(const Params& p, LAS u8* L, rsrc_t rs, const Near& nr, int set, int q, bool& dead) {
  int wst = 100; const int wk = set * 32 + q; (void)wst; (void)wk;
  const int tid = threadIdx.x, lane = tid & 63, w = __builtin_amdgcn_readfirstlane(tid >> 6), r16 = lane & 15, g = lane >> 4;
  const int ks = q >> 3, ct = q & 7;
  gu32* ctrl = (gu32*)p.ws;
  const unsigned lb = (unsigned)(size_t)L;
  v4u Wf[32];
  LAS u8* Wl = L + 65536 + w * 8192 + lane * 16;
  {
    const int kr = 8 * w + (lane >> 3);
    const int krs = (kr & ~31) + 16 * ((kr >> 2) & 1) + 4 * ((kr >> 3) & 3) + (kr & 3);
    const int fsw = ((kr >> 1) & 1) | (((kr >> 3) & 1) << 1);
    const unsigned wvo = (krs * DM + 16 * (((lane & 7) >> 1) ^ fsw) + 8 * (lane & 1)) * 2;
    const int qq = r16 >> 2, fr = ((qq >> 1) & 1) | ((g & 1) << 1);
    const unsigned boff = lb + (g * 2 + (w >> 2)) * 1024 + qq * 128 + (((w & 3) ^ fr) * 32) + (lane & 3) * 8;
    static_for<2>([&](auto rc) {
      constexpr int RD = decltype(rc)::v;
      wgbar();
#pragma unroll
      for (int kt = 0; kt < 8; ++kt) {
        const u16* wb = p.Wo + (size_t)(1024 * ks + 64 * (8 * RD + kt)) * DM + 128 * ct;
        dma16<false>(wb, wvo, lb + kt * 16384 + (w * 2) * 1024);
        dma16<false>(wb + 64, wvo, lb + kt * 16384 + (w * 2 + 1) * 1024);
      }
      DRAIN_VM();
      wgbar();
#pragma unroll
      for (int i = 0; i < 16; ++i) {
        const v2u x0 = trread(boff + (i >> 1) * 16384 + (i & 1) * 8192), x1 = trread(boff + (i >> 1) * 16384 + (i & 1) * 8192 + 512);
        Wf[16 * RD + i] = v4u{x0.x, x0.y, x1.x, x1.y};
      }
    });
  }
  wgbar();
#pragma unroll
  for (int i = 0; i < 8; ++i) *(LAS v4u*)(Wl + i * 1024) = Wf[24 + i];
  wait_lines(ctrl, W_M2, LN, 56, 1u, lane, 0x70u, dead);
  {
    const int fb = NBULK + (lane & 7), kind = (lane >> 3) % 3, hf = lane >= 24 ? 1 : 0;
    gu32* f = ctrl + (kind == 2 ? W_FQ + fb * 64 : W_FKV + fb * 128 + kind * 64) + 2 * q + hf;
    for (unsigned sp = 0; !dead;) {
      if (__all(__hip_atomic_load(f, RLX_AGENT) != 0u)) break;
      __builtin_amdgcn_s_sleep(8);
      if (spin_fail(sp, ctrl, 0x73u)) dead = true;
    }
  }
  CFENCE();
  const v4u top = ld16(rs, W_TOP * 4);
  auto finish = [&](int s) {
    const int v = w - 4;
    wait_lines(ctrl, W_SP + (set * 32 + ct) * LN, 8 * LN, 4, (unsigned)(s + 1), lane, 0x72u, dead);
    CFENCE();
    const unsigned fo = OFF_PS + (unsigned)((s - NBULK * 4) * 8 + ct) * 65536u + (4 * ks + v) * 1024 + lane * 16;
    const f32x4 sum = (__builtin_bit_cast(f32x4, ld16(rs, fo)) + __builtin_bit_cast(f32x4, ld16(rs, fo + 16384))) +
                      (__builtin_bit_cast(f32x4, ld16(rs, fo + 32768)) + __builtin_bit_cast(f32x4, ld16(rs, fo + 49152)));
    mix_store(p, rs, top, s * 32 + 16 * (v & 1) + r16, ct * 128 + 16 * (2 * ks + (v >> 1)) + 4 * g, sum);
  };
  for (int s = NBULK * 4 + set; s < NSEG; s += 2) {
    const int si = s - NBULK * 4;
    if (w == 0) attn_task<0, 0, 0, 2, false>(p, rs, nr, s, q, dead);
    if (w == 1) attn_task<1, 1, 0, 2, false>(p, rs, nr, s, q, dead);
    if (w == 2) attn_task<0, 0, 2, 4, false>(p, rs, nr, s, q, dead);
    if (w == 3) attn_task<1, 1, 2, 4, false>(p, rs, nr, s, q, dead);
    if (w >= 4 && si >= 2) finish(s - 2);
    wgbar();
    WSTAMP();
    if (tid == 0) __hip_atomic_store(ctrl + W_SA + (set * 32 + q) * LN, (unsigned)(s + 1), RLX_AGENT);
    if (w == 4) wait_lines(ctrl, W_SA + (set * 32 + ks * 8) * LN, LN, 8, (unsigned)(s + 1), lane, 0x71u, dead);
    wgbar();
    WSTAMP();
    {
      const unsigned ao = OFF_AX + (unsigned)(s * NH + 8 * ks + w) * 8192u + lane * 16;
      v4u t[8];
#pragma unroll
      for (int i = 0; i < 8; ++i) t[i] = ld16(rs, ao + i * 1024);
#pragma unroll
      for (int i = 0; i < 8; ++i) *(LAS v4u*)(L + w * 8192 + i * 1024 + lane * 16) = t[i];
    }
    wgbar();
    f32x4 a0 = {0.f, 0.f, 0.f, 0.f}, a1 = a0;
#pragma unroll
    for (int k = 0; k < 32; ++k) {
      const LAS u8* fp = L + (k >> 2) * 8192 + (k & 3) * 1024 + lane * 16;
      const v4u wf = k < 24 ? Wf[k] : *(const LAS v4u*)(Wl + (k - 24) * 1024);
      a0 = mfma32(wf, *(const LAS v4u*)fp, a0);
      a1 = mfma32(wf, *(const LAS v4u*)(fp + 4096), a1);
    }
    WSTAMP();
    const unsigned pb = OFF_PS + (unsigned)(si * 8 + ct) * 65536u + lane * 16;
    st16(rs, pb + ks * 16384 + (2 * w) * 1024, __builtin_bit_cast(v4u, a0));
    st16(rs, pb + ks * 16384 + (2 * w + 1) * 1024, __builtin_bit_cast(v4u, a1));
    DRAIN_VM();
    wgbar();
    if (tid == 0) __hip_atomic_store(ctrl + W_SP + (set * 32 + q) * LN, (unsigned)(s + 1), RLX_AGENT);
    WSTAMP();
  }
  if (w >= 4) finish(NSEG - 2 + set);
}

__device__ __forceinline__ void worker_role(const Params& p, LAS u8* L, rsrc_t rs, int b) {
  const int tid = threadIdx.x, lane = tid & 63, w = __builtin_amdgcn_readfirstlane(tid >> 6);
  const int x8 = 2 * (b & 3) + (b >> 7), r = (b >> 3) & 15, wk = r * 8 + x8;
  gu32* ctrl = (gu32*)p.ws;
  bool dead = false;
  int wst = 0; (void)wst;
  Near nr = near_init(p.ws);
  WSTAMP();
  {
    const int cg = x8 * 16 + r;
    for (int blk = 0; blk < 2; ++blk) {
      if (cg < 64) proj_task<64, M_K>(p, L, rs, blk, 0, cg * 64); else proj_task<64, M_V>(p, L, rs, blk, 1, (cg - 64) * 64);
      WSTAMP();
      if (blk == 0) WSTAMP();
    }
  }
  rg_task(p, rs, wk);
  WSTAMP();
  for (int rnd = 0; rnd < 6; ++rnd) {
    const int ct = x8 * 4 + (r & 3), bq = r >> 2;
    if (rnd < 4) {
      const int blk = 2 + rnd * 4 + bq;
      if (blk < 16) { if (ct < 16) proj_task<256, M_K>(p, L, rs, blk, 0, ct * 256); else proj_task<256, M_V>(p, L, rs, blk, 1, (ct - 16) * 256); }
      else { const int t = (blk - 16) * 32 + ct; if (t < 56) mlp1_task(p, L, rs, t, dead); }
    } else {
      const int qct = x8 * 2 + (r & 1), blk = (rnd - 4) * 8 + (r >> 1);
      proj_task<256, M_K>(p, L, rs, blk, 2, qct * 256);
    }
    WSTAMP();
  }
  if (wk < 56) mlp2_task(p, L, rs, wk, dead);
  near_wait(ctrl, nr, dead);
  WSTAMP();
  if (r < 8) {
    const int blk = x8, m = r;
    for (int i = 0; i < 2; ++i) { const int t = (i * 8 + m) * 8 + w; attn_task<0, 1, 0, 4, true>(p, rs, nr, blk * 4 + (t >> 5), t & 31, dead); WSTAMP(); }
    wgbar();
    if (tid == 0) __hip_atomic_store(ctrl + W_T2 + (8 * blk + m) * LN, 1u, RLX_AGENT);
    oproj_task(p, L, rs, blk, m, dead);
    WSTAMP();
  } else {
    const int sid = x8 * 8 + (r - 8);
    stream_role(p, L, rs, nr, sid >> 5, sid & 31, dead);
    WSTAMP();
  }
}

__global__ void __launch_bounds__(256) rmt_init_kernel(u8* ws, const u16* mem0) {
  const int tid = threadIdx.x;
  if (blockIdx.x >= 2) {
    const Near n = near_init(ws);
    const int i = (blockIdx.x - 2) * 256 + tid, cb = i >> 3, row = i & 7;
    const u16* m0 = mem0 + row * DM + (cb >> 2) * 32 + (cb & 3) * 4;
    const v2u lo = *(const GAS v2u*)m0, hi = *(const GAS v2u*)(m0 + 16);
    const v4u v = {canon(__builtin_amdgcn_perm(hi.x, lo.x, 0x05040100u)), canon(__builtin_amdgcn_perm(hi.x, lo.x, 0x07060302u)),
                   canon(__builtin_amdgcn_perm(hi.y, lo.y, 0x05040100u)), canon(__builtin_amdgcn_perm(hi.y, lo.y, 0x07060302u))};
    u8* d = ws + n.al + OFF_MEM + (cb >> 6) * 16384 + (cb & 63) * 128 + row * 16;
    *(GAS v4u*)d = v; *(GAS v4u*)(d + 8192) = v;
    return;
  }
  if (blockIdx.x == 0) {
    for (int i = tid; i < (int)(CTRL_BYTES / 16); i += 256)
      if (i != W_CFG / 4) ((GAS v4u*)ws)[i] = v4u{0u, 0u, 0u, 0u};
  } else if (tid == 0) {
    const Near n = near_init(ws);
    gu32* a0 = (gu32*)(ws + n.al + OFF_CAL); gu32* a1 = a0 + 2048;
    unsigned long long b0 = ~0ull, b1 = ~0ull;
    for (int i = 0; i < 3; ++i) {
      const unsigned long long t0 = __builtin_amdgcn_s_memtime();
      __hip_atomic_store(a0, 0u, RLX_AGENT); DRAIN_VM();
      const unsigned long long t1 = __builtin_amdgcn_s_memtime();
      __hip_atomic_store(a1, 0u, RLX_AGENT); DRAIN_VM();
      const unsigned long long t2 = __builtin_amdgcn_s_memtime();
      b0 = t1 - t0 < b0 ? t1 - t0 : b0; b1 = t2 - t1 < b1 ? t2 - t1 : b1;
    }
    const unsigned nz = ((b1 < b0 ? 1u : 0u) ^ ((n.va21 + OFF_CAL) >> 21) ^ (xcc_id() >> 2)) & 1u;
    ((GAS v4u*)ws)[W_CFG / 4] = v4u{nz, 0u, 0u, 0u};
  }
}

__global__ void __launch_bounds__(NTHR, 2) rmt_mod_kernel(Params p) {
  extern __shared__ __attribute__((aligned(16))) u8 smem[];
  LAS u8* L = (LAS u8*)smem;
  const rsrc_t rs = __builtin_amdgcn_make_buffer_rsrc(p.ws, 0, WS_END, 0x00020000);
  const int b = blockIdx.x;
  if ((b & 4) == 0) chain_role(p, L, rs, b); else worker_role(p, L, rs, b);
}

}

extern "C" void kernel_launch(void* const* d_in, const int* in_sizes, int n_in, void* d_out, int out_size, void* d_ws, size_t ws_size,
                              hipStream_t stream) {
  (void)in_sizes; (void)n_in; (void)out_size; (void)ws_size;
  Params p;
  p.x = (const u16*)d_in[0]; p.mem0 = (const u16*)d_in[1]; p.Wq = (const u16*)d_in[2]; p.Wk = (const u16*)d_in[3];
  p.Wv = (const u16*)d_in[4]; p.Wo = (const u16*)d_in[5]; p.wr = (const u16*)d_in[8]; p.wg = (const u16*)d_in[9];
  p.wu = (const u16*)d_in[10]; p.wd = (const u16*)d_in[11]; p.gw = (const u16*)d_in[12];
  p.out = (u16*)d_out; p.ws = (u8*)d_ws;
  (void)hipFuncSetAttribute((const void*)rmt_mod_kernel, hipFuncAttributeMaxDynamicSharedMemorySize, LDS_BYTES);
  rmt_init_kernel<<<6, 256, 0, stream>>>(p.ws, p.mem0);
  rmt_mod_kernel<<<NWG, NTHR, LDS_BYTES, stream>>>(p);
}
```

```cpp
#include <hip/hip_runtime.h>
#include <hip/hip_bf16.h>
#include <stdint.h>

#define GAS __attribute__((address_space(1)))
#define LAS __attribute__((address_space(3)))
typedef unsigned v4u __attribute__((ext_vector_type(4)));
typedef unsigned v2u __attribute__((ext_vector_type(2)));
typedef unsigned v8u __attribute__((ext_vector_type(8)));
typedef float f32x4 __attribute__((ext_vector_type(4)));
typedef __bf16 bfx8 __attribute__((ext_vector_type(8)));
typedef __bf16 bfx16 __attribute__((ext_vector_type(16)));
typedef short s16x4 __attribute__((ext_vector_type(4)));
typedef GAS unsigned gu32;
typedef unsigned short u16;
typedef unsigned char u8;
#define RLX_AGENT __ATOMIC_RELAXED, __HIP_MEMORY_SCOPE_AGENT

#define PBF 1
#define SPLIT_XCD 2
namespace {

constexpr int SEQ = 2048, DM = 1024, NH = 32, HD = 4096, NSEG = 64, FF = 3584;
constexpr int NCHAIN = 128, NWORK = 128, NWG = 256, NTHR = 512;
constexpr unsigned SENT = 0xFFFFFFFFu;
constexpr int AUX_SC1 = 16;

constexpr unsigned CTRL_BYTES = 73728;
constexpr int LN = 32;
constexpr int W_TMO = 0, W_RG = 1 * LN, W_M1 = 2 * LN, W_TOP = 3 * LN  , W_CFG = 4 * LN  ,
              W_XCC = 5 * LN, W_PROG = 8 * LN  , W_FKV = 64 * LN  , W_FQ = 128 * LN  ,
              W_ARM = 160 * LN  , W_M2 = 288 * LN  , W_T2 = 344 * LN  ,
              W_SA = 408 * LN  , W_SP = 472 * LN  ;
constexpr unsigned OFF_MEM = 98304;
constexpr unsigned MEM_ST = 32768;
constexpr unsigned OFF_QKV = OFF_MEM + NSEG * MEM_ST;
constexpr unsigned QH = 32, PR = 128;
constexpr unsigned OFF_PART = OFF_QKV + NSEG * QH * 16384;
constexpr unsigned PART_SZ = PR * 16384;
#define NSLOT 3
constexpr unsigned OFF_ARM_END = OFF_PART + NSLOT * PART_SZ;
constexpr unsigned OFF_CAL = OFF_ARM_END;
constexpr unsigned OFF_KX = OFF_CAL + 16384 + 16384;
constexpr unsigned OFF_VX = OFF_KX + NSEG * NH * 8192;
constexpr unsigned OFF_QX = OFF_VX + NSEG * NH * 8192;
constexpr unsigned OFF_AX = OFF_QX + NSEG * NH * 8192;
constexpr unsigned OFF_R = OFF_AX + SEQ * HD * 2;
constexpr unsigned OFF_G = OFF_R + SEQ * 4;
constexpr unsigned OFF_HID = OFF_G + SEQ * 4;
constexpr unsigned OFF_YP = OFF_HID + 2 * FF * 4;
constexpr unsigned OFF_PS = OFF_YP + 14 * 2 * DM * 4;
constexpr unsigned WS_END = OFF_PS + 32 * 64 * 16384;
constexpr int NBULK = 8;
constexpr int LDS_BYTES = 160 * 1024;
constexpr unsigned SPIN_CAP = 1u << 18;

#define W_A 300
#define W_B 80
#define W_C 160

struct Params {
  const u16 *x, *mem0, *Wq, *Wk, *Wv, *Wo, *wr, *wg, *wu, *wd, *gw;
  u16* out;
  u8* ws;
};

__device__ __forceinline__ float u2f(unsigned u) { return __builtin_bit_cast(float, u); }
__device__ __forceinline__ unsigned f2u(float f) { return __builtin_bit_cast(unsigned, f); }
__device__ __forceinline__ float bf2f(unsigned b) { return u2f(b << 16); }
__device__ __forceinline__ float bflo(unsigned d) { return u2f(d << 16); }
__device__ __forceinline__ float bfhi(unsigned d) { return u2f(d & 0xFFFF0000u); }
__device__ __forceinline__ unsigned pk2(float lo, float hi) {
  typedef __bf16 bfx2 __attribute__((ext_vector_type(2)));
  bfx2 v; v.x = (__bf16)lo; v.y = (__bf16)hi;
  return __builtin_bit_cast(unsigned, v);
}
__device__ __forceinline__ float bfr(float f) { return (float)(__bf16)f; }
__device__ __forceinline__ unsigned canon(unsigned d) { return min(d, 0xFFFFFFFEu); }
__device__ __forceinline__ f32x4 mfma32(v4u a, v4u b, f32x4 c) {
  return __builtin_amdgcn_mfma_f32_16x16x32_bf16(__builtin_bit_cast(bfx8, a), __builtin_bit_cast(bfx8, b), c, 0, 0, 0);
}
__device__ __forceinline__ f32x4 mfma16(v2u a, v2u b, f32x4 c) { return mfma32(v4u{a.x, a.y, 0u, 0u}, v4u{b.x, b.y, 0u, 0u}, c); }
__device__ __forceinline__ f32x4 smf64(v4u a, v8u b, f32x4 c, int idx) {
  return __builtin_amdgcn_smfmac_f32_16x16x64_bf16(__builtin_bit_cast(bfx8, a), __builtin_bit_cast(bfx16, b), c, idx, 0, 0);
}
__device__ __forceinline__ v2u trread(unsigned lds_addr) {
  return __builtin_bit_cast(v2u, __builtin_amdgcn_ds_read_tr16_b64_v4i16((LAS s16x4*)(size_t)lds_addr));
}
typedef float f32x2 __attribute__((ext_vector_type(2)));
__device__ __forceinline__ f32x2 fold32(f32x4 v) {
  const auto r0 = __builtin_amdgcn_permlane32_swap(f2u(v[0]), f2u(v[2]), false, false);
  const auto r1 = __builtin_amdgcn_permlane32_swap(f2u(v[1]), f2u(v[3]), false, false);
  return f32x2{u2f(r0[0]), u2f(r1[0])} + f32x2{u2f(r0[1]), u2f(r1[1])};
}
__device__ __forceinline__ float xmax16(float a) { auto r = __builtin_amdgcn_permlane16_swap(f2u(a), f2u(a), false, false); return __builtin_amdgcn_fmed3f(u2f(r[0]), u2f(r[1]), INFINITY); }
__device__ __forceinline__ float xmax32(float a) { auto r = __builtin_amdgcn_permlane32_swap(f2u(a), f2u(a), false, false); return __builtin_amdgcn_fmed3f(u2f(r[0]), u2f(r[1]), INFINITY); }
__device__ __forceinline__ float xadd16(float a) { auto r = __builtin_amdgcn_permlane16_swap(f2u(a), f2u(a), false, false); return u2f(r[0]) + u2f(r[1]); }
__device__ __forceinline__ float xadd32(float a) { auto r = __builtin_amdgcn_permlane32_swap(f2u(a), f2u(a), false, false); return u2f(r[0]) + u2f(r[1]); }

__device__ __forceinline__ float wave_sum(float v) {
  v += __shfl_xor(v, 1); v += __shfl_xor(v, 2); v += __shfl_xor(v, 4); v += __shfl_xor(v, 8);
  return xadd32(xadd16(v));
}
#define CFENCE() asm volatile("" ::: "memory")
#define DRAIN_VM() asm volatile("s_waitcnt vmcnt(0)" ::: "memory")
#define WAIT_LGKM() asm volatile("s_waitcnt lgkmcnt(0)" ::: "memory")
__device__ __forceinline__ void wgbar() { WAIT_LGKM(); __builtin_amdgcn_s_barrier(); CFENCE(); }

typedef __amdgpu_buffer_rsrc_t rsrc_t;
__device__ __forceinline__ v4u ld16(rsrc_t rs, unsigned off) { return __builtin_amdgcn_raw_buffer_load_b128(rs, off, 0, AUX_SC1); }
__device__ __forceinline__ v2u ld8(rsrc_t rs, unsigned off) { return __builtin_amdgcn_raw_buffer_load_b64(rs, off, 0, AUX_SC1); }
__device__ __forceinline__ unsigned ld4(rsrc_t rs, unsigned off) { return __builtin_amdgcn_raw_buffer_load_b32(rs, off, 0, AUX_SC1); }
__device__ __forceinline__ void st16(rsrc_t rs, unsigned off, v4u v) { __builtin_amdgcn_raw_buffer_store_b128(v, rs, off, 0, AUX_SC1); }
__device__ __forceinline__ void st8(rsrc_t rs, unsigned off, v2u v) { __builtin_amdgcn_raw_buffer_store_b64(v, rs, off, 0, AUX_SC1); }
__device__ __forceinline__ void st4(rsrc_t rs, unsigned off, unsigned v) { __builtin_amdgcn_raw_buffer_store_b32(v, rs, off, 0, AUX_SC1); }
__device__ __forceinline__ unsigned umax4(v4u v) { return max(max(v.x, v.y), max(v.z, v.w)); }

template <int N> __device__ __forceinline__ void delay_cyc() {
#pragma unroll
  for (int i = 0; i < N / 64; ++i) asm volatile("s_nop 15");
  if ((N % 64) >= 4) asm volatile("s_nop %0" :: "i"((N % 64) / 4 - 1));
}

#define PACE 0
#define PACE_GAIN 0.3f
#define PACE_CLAMP 2.0f
#define GAP_A 6
#define DB3 5
#define EXTRA_B 0
#define EXTRA_C 0
#define GAP_C 6
#define AD_DEC 0
#define AD_INC 0
__device__ __forceinline__ void delay_rt(int n) {
  asm volatile("s_cmp_lt_i32 %0, 1\n\ts_cbranch_scc1 2f\n\ts_mov_b32 vcc_lo, %0\n1:\n\ts_nop 1\n\ts_sub_i32 vcc_lo, vcc_lo, 1\n\ts_cmp_gt_i32 vcc_lo, 0\n\ts_cbranch_scc1 1b\n2:" :: "s"(n) : "vcc", "scc");
}
__device__ __forceinline__ void adapt(int& n, int misses) { n = misses ? min(n + AD_INC, 120) : max(n - AD_DEC, 0); }

__device__ __forceinline__ bool spin_fail(unsigned& spins, gu32* ctrl, unsigned code) {
  ++spins;
  if (spins > SPIN_CAP) { __hip_atomic_store(ctrl + W_TMO, 1u, RLX_AGENT); __hip_atomic_fetch_max(ctrl + W_TMO + 1, code, RLX_AGENT); return true; }
  return (spins & 255u) == 0u && __builtin_amdgcn_readfirstlane(__hip_atomic_load(ctrl + W_TMO, RLX_AGENT)) != 0u;
}
constexpr int RETRY_CAP = 1 << 15;
__device__ __forceinline__ void wait_ge(gu32* ctrl, int word, unsigned want, unsigned code, bool& dead) {
  if (dead) return;
  for (unsigned sp = 0;;) {
    if ((unsigned)__builtin_amdgcn_readfirstlane(__hip_atomic_load(ctrl + word, RLX_AGENT)) >= want) return;
    __builtin_amdgcn_s_sleep(4);
    if (spin_fail(sp, ctrl, code)) { dead = true; return; }
  }
}
__device__ __forceinline__ void wait_flags2(gu32* ctrl, int w0, int w1, unsigned code, bool& dead) {
  if (dead) return;
  for (unsigned sp = 0;;) {
    const unsigned long long a = __hip_atomic_load((GAS unsigned long long*)(ctrl + w0), RLX_AGENT);
    const unsigned long long b = __hip_atomic_load((GAS unsigned long long*)(ctrl + w1), RLX_AGENT);
    const unsigned long long m = a & b;
    if (__builtin_amdgcn_readfirstlane((unsigned)m & (unsigned)(m >> 32)) != 0u) return;
    __builtin_amdgcn_s_sleep(4);
    if (spin_fail(sp, ctrl, code)) { dead = true; return; }
  }
}
__device__ __forceinline__ void wait_lines(gu32* ctrl, int word, int stride, int n, unsigned want, int lane, unsigned code, bool& dead) {
  if (dead) return;
  gu32* mine = ctrl + word + (lane < n ? lane : 0) * stride;
  for (unsigned sp = 0;;) {
    if (__all(__hip_atomic_load(mine, RLX_AGENT) >= want)) return;
    __builtin_amdgcn_s_sleep(2);
    if (spin_fail(sp, ctrl, code)) { dead = true; return; }
  }
}
constexpr int ARM_AHEAD = 4;
__device__ __forceinline__ void arm_share(rsrc_t rs, unsigned al, int c, int tid) {
  const v4u s = {SENT, SENT, SENT, SENT};
  const unsigned lo = (tid & 63) * 16, w = tid >> 6;
  constexpr int NB = PR / 64;
  for (int sl = 0; sl < NSLOT; ++sl)
#pragma unroll
    for (int k = 0; k < NB; ++k) if (!PBF || w < 4) st16(rs, al + OFF_PART + sl * PART_SZ + (c * NB + k) * 8192 + w * 1024 + lo, s);
  if (w < 6) {
#pragma unroll
    for (int k = 0; k < NB; ++k) st16(rs, al + OFF_QKV + (c * NB + k) * 8192 + w * 1024 + lo, s);
  } else if (w == 6) st16(rs, al + OFF_MEM + MEM_ST + c * 1024 + lo, s);
  else if (c < 32) st16(rs, al + OFF_MEM + 5 * MEM_ST + c * 1024 + lo, s);
}
struct Near {
  unsigned al, va21, nz, k;
  __device__ __forceinline__ unsigned rel(int c) const { return k >> 2; }
  __device__ __forceinline__ unsigned blk(unsigned pair_off, unsigned iod) const { return al + pair_off + ((((va21 + pair_off) >> 21) ^ nz ^ iod) & 1u) * 8192u; }
};
__device__ __forceinline__ Near near_init(const u8* ws) {
  Near n; const unsigned lo = (unsigned)(size_t)ws;
  n.al = (0u - lo) & 16383u; n.va21 = lo + n.al; n.nz = 0; n.k = 0; return n;
}
__device__ __forceinline__ unsigned xcc_id() { return (unsigned)__builtin_amdgcn_s_getreg(20 | (3 << 11)) & 7u; }
__device__ __forceinline__ void near_wait(gu32* ctrl, Near& n, bool& dead) {
  wait_ge(ctrl, W_XCC, 16u, 0x11u, dead);
  n.k = __builtin_amdgcn_readfirstlane(__hip_atomic_load(ctrl + W_XCC, RLX_AGENT)) & 7u;
  n.nz = __builtin_amdgcn_readfirstlane(__hip_atomic_load(ctrl + W_CFG, RLX_AGENT)) & 1u;
}

#define CSTAMP(wv, slot)
#define PT(id)
#define CTRIES(slot, n)
#define WSTAMP()
template <bool SC1>
__device__ __forceinline__ void dma16(const void* base, unsigned voff, unsigned lds_addr) {
  unsigned keep;
  if (SC1) asm volatile("s_mov_b32 %0, m0\n\ts_mov_b32 m0, %3\n\ts_nop 0\n\tglobal_load_lds_dwordx4 %1, %2 sc1\n\ts_mov_b32 m0, %0" : "=&s"(keep) : "v"(voff), "s"(base), "s"(lds_addr) : "memory");
  else asm volatile("s_mov_b32 %0, m0\n\ts_mov_b32 m0, %3\n\ts_nop 0\n\tglobal_load_lds_dwordx4 %1, %2\n\ts_mov_b32 m0, %0" : "=&s"(keep) : "v"(voff), "s"(base), "s"(lds_addr) : "memory");
}

template <int N> __device__ __forceinline__ void wait_vm() { static_assert(N >= 0 && N < 64, "vmcnt"); asm volatile("s_waitcnt vmcnt(%0)" :: "i"(N) : "memory"); }
template <int V> struct IC { static constexpr int v = V; };
template <int N, int I = 0, class F> __device__ __forceinline__ void static_for(F&& f) { if constexpr (I < N) { f(IC<I>{}); static_for<N, I + 1>(f); } }
__device__ __forceinline__ void slice_dma(const u16* src, int ld, unsigned dst, int lane) {
  const unsigned vo = ((lane >> 2) * ld + 8 * (lane & 3)) * 2;
#pragma unroll
  for (int p = 0; p < 8; ++p) dma16<false>(src + (size_t)16 * p * ld, vo, dst + p * 1024);
}
template <bool PERM>
__device__ __forceinline__ void slice_frags(unsigned base, int lane, v8u (&f)[2][2]) {
  const int gB = lane >> 4, q = (lane & 15) >> 2, p = lane & 3;
#pragma unroll
  for (int cc = 0; cc < 2; ++cc)
#pragma unroll
    for (int rr = 0; rr < 4; ++rr) {
      const int kp = 64 * cc + 32 * (rr >> 1) + 16 * (gB >> 1) + 4 * (gB & 1) + 2 * (rr & 1) + (q & 1) + 8 * (q >> 1);
      const int row = PERM ? ((kp & ~31) + 16 * (kp & 1) + ((kp & 31) >> 1)) : kp;
#pragma unroll
      for (int tb = 0; tb < 2; ++tb) {
        const v2u v = trread(base + row * 64 + tb * 32 + p * 8);
        f[tb][cc][2 * rr] = v.x; f[tb][cc][2 * rr + 1] = v.y;
      }
    }
}

__device__ __forceinline__ void chain_role(const Params& p, LAS u8* L, rsrc_t rs, int b) {
  const int tid = threadIdx.x, lane = tid & 63, w = __builtin_amdgcn_readfirstlane(tid >> 6);
  const int c = (b & 3) * 32 + (b >> 3);
  const int h = c >> 2, j = c & 3;
  const int dbgi = (c >> 4) + 8 * (c & 15); (void)dbgi;
  const int r16 = lane & 15, g = lane >> 4;
  gu32* ctrl = (gu32*)p.ws;
  bool dead = false;
  const unsigned lbase = (unsigned)(size_t)L;
  Near nr = near_init(p.ws);
  if (b == 0 && tid == 0) __hip_atomic_store(ctrl + W_XCC, 16u | xcc_id(), RLX_AGENT);

  v8u W1[3][2][2];
  v8u WO[2][2] = {};
  {
    const unsigned mine = lbase + w * 16384;
    const size_t wcol = (size_t)h * 128 + 32 * j;
    slice_dma(p.Wk + (size_t)128 * w * HD + wcol, HD, mine, lane);
    slice_dma(p.Wv + (size_t)128 * w * HD + wcol, HD, mine + 8192, lane);
    asm volatile("s_waitcnt vmcnt(8)" ::: "memory");
    slice_frags<true>(lbase + w * 16384, lane, W1[1]);
    WAIT_LGKM(); __builtin_amdgcn_sched_barrier(0);
    slice_dma(p.Wq + (size_t)128 * w * HD + wcol, HD, mine, lane);
    asm volatile("s_waitcnt vmcnt(8)" ::: "memory");
    slice_frags<true>(lbase + w * 16384 + 8192, lane, W1[2]);
    WAIT_LGKM(); __builtin_amdgcn_sched_barrier(0);
    DRAIN_VM();
    slice_frags<true>(lbase + w * 16384, lane, W1[0]);
    WAIT_LGKM(); __builtin_amdgcn_sched_barrier(0);
  }
  near_wait(ctrl, nr, dead);
  const unsigned myrel = nr.rel(c);

  LAS float* RED = (LAS float*)L;
  LAS u8* AHF = L + 24576;
  volatile LAS unsigned* FOK = (volatile LAS unsigned*)(L + 26624 + 4096);
  LAS u8* KVL = L + 49152;
  const int sidx = r16 < 8 ? 0x4444 : 0xEEEE;
  const int gg = g & 1, hi = g >> 1, rp = 2 * gg + hi;
  int gone = dead ? 1 : 0;

  auto fetch_kv_issue = [&](int s, int lo, int hi) {
    if ((s & 3) == 0) { bool d = gone != 0; const int blk = s >> 2; wait_flags2(ctrl, W_FKV + blk * 128 + 2 * h, W_FKV + blk * 128 + 64 + 2 * h, 0x20u, d); gone = __builtin_amdgcn_readfirstlane(d ? 1 : 0); }
    CFENCE();
    const u8* kb = p.ws + OFF_KX + (size_t)(s * NH + h) * 8192u;
    const unsigned dst = lbase + 49152 + (s & 1) * 16384;
#pragma unroll
    for (int i = lo; i < hi; ++i) dma16<true>(kb + (i >> 3) * (OFF_VX - OFF_KX) + (i & 7) * 1024, lane * 16, dst + i * 1024);
  };
  if (tid == 0) { FOK[0] = 0u; FOK[1] = 0u; }
  int dA = W_A / 20, dB = W_B / 20, dC = W_C / 20;
  wgbar();

  auto run = [&](auto role) {
    constexpr int ROLE = decltype(role)::v;
    LAS f32x2* redw = (LAS f32x2*)(RED + w * 768) + (rp * 16 + r16) * 2;
    const int R = 32 * j + 4 * w + (r16 >> 2);
    const unsigned rrel = nr.rel(R), part_w = (R & (PR - 1)) * 16384u, part_l = PBF ? h * 128 + rp * 32 + (r16 & 3) * 8 : h * 256 + rp * 64 + (r16 & 3) * 16;
    const unsigned pub_l = ROLE == 0 ? j * 1024 + (8 * w + 2 * g + (r16 & 1) + 16 * (r16 >> 2)) * 16 + (r16 & 2) * 4 : 4096 + (32 * j + r16) * 16 + g * 4;
    f32x4 acc[6];
    unsigned qo, a_part, mo, a_pub;
    int slot = 0, lim = gone ? 0 : RETRY_CAP;
    auto prepare = [&](int s, int sl) {
#pragma unroll
      for (int t = 0; t < 6; ++t) { acc[t] = f32x4{0.f, 0.f, 0.f, 0.f}; asm volatile("" : "+v"(acc[t])); }
      qo = nr.blk(OFF_QKV + (unsigned)(s * QH + (h & (QH - 1))) * 16384u, myrel);
      a_part = nr.blk(OFF_PART + (unsigned)sl * PART_SZ + part_w, rrel) + part_l;
      mo = nr.blk(OFF_MEM + (unsigned)s * MEM_ST + (w >> 2) * 16384u, myrel) + (w & 3) * 2048 + lane * 16;
      a_pub = qo + pub_l;
      asm volatile("" : "+v"(a_part), "+v"(mo), "+v"(a_pub));
    };
    prepare(0, 0);
    for (int s = 0;; ++s) {
      PT(0);
      delay_rt(dA);
      PT(1);
      v4u a0, a1;
      {
        int tries = 1;
        CFENCE();
        a0 = ld16(rs, mo); a1 = ld16(rs, mo + 1024);
        if (__builtin_expect(__ballot(max(umax4(a0), umax4(a1)) == SENT) != 0ull, 0)) {
          bool again;
          do {
            CFENCE();
            const bool b0 = umax4(a0) == SENT, b1 = umax4(a1) == SENT;
            const v4u n0 = ld16(rs, b0 ? mo : 0xFFFFFF00u), n1 = ld16(rs, b1 ? mo + 1024 : 0xFFFFFF00u);
            a0 = b0 ? n0 : a0; a1 = b1 ? n1 : a1;
            again = (__ballot(max(umax4(a0), umax4(a1)) == SENT) != 0ull) & (tries < lim);
            ++tries;
          } while (again);
        }
        lim = tries > lim ? 0 : lim;
        adapt(dA, tries - 1);
        if (w == 0) CTRIES(0, tries - 1);
      }
      CSTAMP(0, 0);
      PT(2);
      if (__builtin_expect(s == 0, 0)) slice_dma(p.Wo + (size_t)128 * h * DM + 256 * j + 32 * w, DM, lbase + 98304 + w * 8192, lane);
#pragma unroll
      for (int t = 0; t < 6; ++t) acc[t] = smf64(a0, W1[t >> 1][t & 1][0], acc[t], sidx);
#pragma unroll
      for (int t = 0; t < 6; ++t) acc[t] = smf64(a1, W1[t >> 1][t & 1][1], acc[t], sidx);
#pragma unroll
      for (int t = 0; t < 6; ++t) redw[(t >> 1) * 128 + (t & 1)] = fold32(acc[t]);
      PT(3);
      wgbar();
      PT(4);
      CSTAMP(0, 1);
      if (ROLE <= 2) {
        const LAS f32x4* redr = (const LAS f32x4*)RED + w * 64 + lane;
        f32x4 t0 = redr[0], t1 = redr[192];
#pragma unroll
        for (int k = 2; k < 8; k += 2) { t0 += redr[k * 192]; t1 += redr[(k + 1) * 192]; }
        t0 += t1;
        if (ROLE == 0) {
          const unsigned d0 = pk2(t0[0], t0[2]), d1 = pk2(t0[1], t0[3]);
          const unsigned got = __builtin_amdgcn_mov_dpp((r16 & 1) ? d0 : d1, 0xB1, 0xF, 0xF, true);
          st8(rs, a_pub, (r16 & 1) ? v2u{canon(got), canon(d1)} : v2u{canon(d0), canon(got)});
        } else {
          st4(rs, a_pub, canon(pk2(t0[0], t0[1])));
          st4(rs, a_pub + 256, canon(pk2(t0[2], t0[3])));
        }
      }
      CSTAMP(0, 2);
      PT(5);
      if (s == NSEG - 1) break;
      if (__builtin_expect(s == 0, 0)) {
        if (ROLE >= 4) fetch_kv_issue(0, 4 * (ROLE - 4), 4 * (ROLE - 4) + 4);
        DRAIN_VM();
        slice_frags<false>(lbase + 98304 + w * 8192, lane, WO);
        wgbar();
      }
      acc[0] = f32x4{0.f, 0.f, 0.f, 0.f}; acc[1] = acc[0];
      asm volatile("" : "+v"(acc[0]), "+v"(acc[1]));
      if (ROLE <= 3) {
        v4u KX[2][4], VX[2], F[4]; v2u VM[2];
        const LAS u8* kv = KVL + (s & 1) * 16384 + lane * 16;
#pragma unroll
        for (int i = 0; i < 8; ++i) KX[i >> 2][i & 3] = *(const LAS v4u*)(kv + i * 1024);
        VX[0] = *(const LAS v4u*)(kv + 8192 + 2 * w * 1024); VX[1] = *(const LAS v4u*)(kv + 8192 + (2 * w + 1) * 1024);
        const unsigned fo = qo + lane * 16, vo = qo + 4096 + (32 * w + r16) * 16 + gg * 8;
        __builtin_amdgcn_sched_barrier(0);
        delay_rt((ROLE == 3 ? dB + DB3 : dB));
        PT(6);
        {
          int tries = 0; bool again;
          do {
            CFENCE();
#pragma unroll
            for (int u = 0; u < 4; ++u) F[u] = ld16(rs, fo + u * 1024);
            VM[0] = ld8(rs, vo); VM[1] = ld8(rs, vo + 256);
            const unsigned m = max(max(max(umax4(F[0]), umax4(F[1])), max(umax4(F[2]), umax4(F[3]))), max(max(VM[0].x, VM[0].y), max(VM[1].x, VM[1].y)));
            again = (__ballot(m == SENT) != 0ull) & (tries < lim);
            ++tries;
          } while (__builtin_expect(again, 0));
          lim = tries > lim ? 0 : lim;
          adapt(dB, tries - 1);
          if (w == 0) CTRIES(1, tries - 1);
        }
        if (ROLE == 2) FOK[0] = (unsigned)(s + 1);
        CSTAMP(0, 3);
        PT(7);
        const float sc2 = 0.08838834764831845f * 1.4426950408889634f;
        f32x4 sA = {0.f, 0.f, 0.f, 0.f}, sB = sA, sM = sA;
#pragma unroll
        for (int u = 0; u < 4; ++u) { sM = mfma32(F[u], F[u], sM); sA = mfma32(KX[0][u], F[u], sA); sB = mfma32(KX[1][u], F[u], sB); }
        sA *= sc2; sB *= sc2; sM *= sc2;
        if (g < 2) sM = f32x4{-INFINITY, -INFINITY, -INFINITY, -INFINITY};
        float mx = fmaxf(fmaxf(fmaxf(fmaxf(sA[0], sA[1]), sA[2]), fmaxf(fmaxf(sA[3], sB[0]), sB[1])), fmaxf(fmaxf(fmaxf(sB[2], sB[3]), sM[0]), fmaxf(fmaxf(sM[1], sM[2]), sM[3])));
        mx = xmax32(xmax16(mx));
        sA -= mx; sB -= mx; sM -= mx;
#pragma unroll
        for (int r = 0; r < 4; ++r) { sA[r] = __builtin_amdgcn_exp2f(sA[r]); sB[r] = __builtin_amdgcn_exp2f(sB[r]); sM[r] = __builtin_amdgcn_exp2f(sM[r]); }
        const v4u pb = {pk2(sA[0], sA[1]), pk2(sA[2], sA[3]), pk2(sB[0], sB[1]), pk2(sB[2], sB[3])};
        const v2u pm = {pk2(sM[0], sM[1]), pk2(sM[2], sM[3])};
        f32x4 e0 = mfma32(VX[0], pb, f32x4{0.f, 0.f, 0.f, 0.f}), e1 = mfma32(VX[1], pb, f32x4{0.f, 0.f, 0.f, 0.f});
        e0 = mfma16(VM[0], pm, e0); e1 = mfma16(VM[1], pm, e1);
        const f32x4 ss = sA + sB + sM;
        const float inv = __builtin_amdgcn_rcpf(xadd32(xadd16((ss[0] + ss[1]) + (ss[2] + ss[3]))));
        e0 *= inv; e1 *= inv;
        LAS u8* ah = AHF + (r16 < 8 ? (4 * w + hi) * 128 + r16 * 16 + gg * 8 : 4096 + lane * 8);
        *(LAS v2u*)ah = v2u{pk2(e0[0], e0[1]), pk2(e0[2], e0[3])};
        *(LAS v2u*)(ah + 256) = v2u{pk2(e1[0], e1[1]), pk2(e1[2], e1[3])};
      } else {
        for (unsigned sp = 0; sp < (1u << 20); ++sp) {
          if ((unsigned)__builtin_amdgcn_readfirstlane(FOK[0]) == (unsigned)(s + 1)) break;
          __builtin_amdgcn_s_sleep(1);
        }
        constexpr int Q = ROLE - 4;
        const v4u sv = {SENT, SENT, SENT, SENT};
        fetch_kv_issue(s + 1, 4 * Q, 4 * Q + 4);
        if (s > 0 && NSLOT == 3) {
          const unsigned po = nr.blk(OFF_PART + (unsigned)(slot == 0 ? 2 : slot - 1) * PART_SZ + (c & (PR - 1)) * 16384u, myrel) + lane * 16;
          if (PBF) st16(rs, po + Q * 1024, sv); else { st16(rs, po + 2 * Q * 1024, sv); st16(rs, po + (2 * Q + 1) * 1024, sv); }
        }
        if (s + ARM_AHEAD < NSEG) {
          const unsigned qn = nr.blk(OFF_QKV + (unsigned)((s + ARM_AHEAD) * QH + (h & (QH - 1))) * 16384u, myrel);
          if (ROLE == 4) st16(rs, qn + j * 1024 + lane * 16, sv);
          if (ROLE == 5)
            st16(rs, lane < 32 ? qn + 4096 + j * 512 + lane * 16
                               : (lane < 48 && s + ARM_AHEAD + 2 < NSEG) ? nr.al + OFF_MEM + (unsigned)(s + ARM_AHEAD + 2) * MEM_ST + (c >> 6) * 16384u + ((lane >> 3) & 1) * 8192u + (c & 63) * 128 + (lane & 7) * 16
                                                                         : 0xFFFFFF00u, sv);
        }
        if (ROLE == 6 && j == 0 && lane == 0) __hip_atomic_store(ctrl + W_PROG + LN * h, (unsigned)(s + 1), RLX_AGENT);
      }
      PT(9);
      wgbar();
      PT(10);
      CSTAMP(7, 4);
      {
        const v4u b0 = *(LAS v4u*)(AHF + lane * 16), b1 = *(LAS v4u*)(AHF + 1024 + lane * 16);
        acc[0] = smf64(b0, WO[0][0], acc[0], sidx); acc[1] = smf64(b0, WO[1][0], acc[1], sidx);
        acc[0] = smf64(b1, WO[0][1], acc[0], sidx); acc[1] = smf64(b1, WO[1][1], acc[1], sidx);
        const f32x2 f0 = fold32(acc[0]), f1 = fold32(acc[1]);
        const v4u pv = {canon(f2u(f0[0])), canon(f2u(f1[0])), canon(f2u(f0[1])), canon(f2u(f1[1]))};
        if (PBF) st8(rs, a_part, v2u{canon(pk2(f0[0], f1[0])), canon(pk2(f0[1], f1[1]))}); else st16(rs, a_part, pv);
      }
      __builtin_amdgcn_sched_barrier(0);
      CSTAMP(7, 5);
      PT(11);
      const int nslot = slot == NSLOT - 1 ? 0 : slot + 1;
      if (ROLE == 7) {
        unsigned po = nr.blk(OFF_PART + (unsigned)slot * PART_SZ + (c & (PR - 1)) * 16384u, myrel) + lane * 16;
        unsigned a_mem = nr.al + OFF_MEM + (unsigned)(s + 1) * MEM_ST + (c >> 6) * 16384u + gg * 8192u
                         + (c & 63) * 128 + (2 * (r16 >> 2) + hi) * 16 + (r16 & 3) * 4;
        prepare(s + 1, nslot);
        asm volatile("" : "+v"(a_mem), "+v"(po));
        delay_rt(dC);
        PT(12);
        v2u pq[8];
        {
          int tries = 0; bool again;
          const unsigned p8 = po - lane * 8;
          do {
            CFENCE();
#pragma unroll
            for (int i = 0; i < 8; ++i) pq[i] = ld8(rs, p8 + i * 512);
            unsigned m = 0;
#pragma unroll
            for (int i = 0; i < 8; ++i) m = max(m, max(pq[i].x, pq[i].y));
            again = (__ballot(m == SENT) != 0ull) & (tries < lim);
            ++tries;
          } while (__builtin_expect(again, 0));
          lim = tries > lim ? 0 : lim;
          CTRIES(2, tries - 1);
        }
        f32x4 t = {0.f, 0.f, 0.f, 0.f};
#pragma unroll
        for (int i = 0; i < 8; ++i) t += f32x4{bflo(pq[i].x), bfhi(pq[i].x), bflo(pq[i].y), bfhi(pq[i].y)};
        const f32x2 f = fold32(t);
        const unsigned md = canon(pk2(xadd16(f[0]), xadd16(f[1])));
        st4(rs, a_mem, md);
      } else {
        prepare(s + 1, nslot);
        if (ROLE >= 4) DRAIN_VM();
      }
      slot = nslot;
      PT(14);
      wgbar();
      CSTAMP(7, 7);
    }
    if (lim == 0 && lane == 0) __hip_atomic_store(ctrl + W_TMO, 1u, RLX_AGENT);
  };
  switch (w) {
    case 0: case 1: run(IC<0>{}); break;
    case 2: run(IC<2>{}); break;
    case 3: run(IC<3>{}); break;
    case 4: run(IC<4>{}); break;
    case 5: run(IC<5>{}); break;
    case 6: run(IC<6>{}); break;
    default: run(IC<7>{}); break;
  }
}

enum { M_K = 0, M_V = 1, M_O = 2 };

template <int BN, int MODE, int WM = 2>
__device__ __forceinline__ void gemm_tile(LAS u8* L, const u16* A, int lda, const u16* W, int ldw, int nkt, f32x4 (&acc)[8 / WM][BN * WM / 128]) {
  constexpr int MT = 8 / WM, NT = BN * WM / 128, NCG = BN / 64, STG = 16384 + BN * 128, NB = BN / 64;
  constexpr int NSTG = LDS_BYTES / STG;
  const int tid = threadIdx.x, lane = tid & 63, w = __builtin_amdgcn_readfirstlane(tid >> 6);
  const int wm = w / (8 / WM), wn = w % (8 / WM), r16 = lane & 15, g = lane >> 4, q = r16 >> 2, pp = lane & 3;
  const int arow = 16 * w + (lane >> 3);
  const unsigned avo0 = (arow * lda + 8 * ((lane & 7) ^ ((arow >> 1) & 7))) * 2;
  const unsigned avo1 = ((arow + 8) * lda + 8 * ((lane & 7) ^ (((arow + 8) >> 1) & 7))) * 2;
  const int kr = 8 * w + (lane >> 3);
  const int krs = MODE == M_O ? ((kr & ~31) + 16 * ((kr >> 2) & 1) + 4 * ((kr >> 3) & 3) + (kr & 3)) : kr;
  const int fsw = ((kr >> 1) & 1) | (((kr >> 3) & 1) << 1);
  const unsigned wvo = (krs * ldw + 16 * (((lane & 7) >> 1) ^ fsw) + 8 * (lane & 1)) * 2;
  const unsigned lb = (unsigned)(size_t)L;
  auto stage = [&](int kt, int buf) {
    const unsigned sb = lb + buf * STG;
    if (MODE == M_O) {
      const u8* ab = (const u8*)A + (size_t)((w >> 1) * NH + (kt >> 1)) * 8192 + (w & 1) * 4096 + (kt & 1) * 2048;
      dma16<false>(ab, lane * 16, sb + (2 * w) * 1024);
      dma16<false>(ab + 1024, lane * 16, sb + (2 * w + 1) * 1024);
    } else {
      dma16<false>(A + kt * 64, avo0, sb + (2 * w) * 1024);
      dma16<false>(A + kt * 64, avo1, sb + (2 * w + 1) * 1024);
    }
#pragma unroll
    for (int i = 0; i < NB; ++i) dma16<false>(W + (size_t)kt * 64 * ldw + 64 * i, wvo, sb + 16384 + (w * NB + i) * 1024);
  };
  const unsigned aoff = lb + (16 * MT * wm + r16) * 128 + ((g ^ ((r16 >> 1) & 7)) * 16);
  const int fr = ((q >> 1) & 1) | ((g & 1) << 1);
  const int cgw = (wn * NT) >> 2, c0 = (wn * NT) & 3;
  unsigned boff[NT];
#pragma unroll
  for (int n = 0; n < NT; ++n) boff[n] = lb + 16384 + (g * NCG + cgw) * 1024 + q * 128 + (((c0 + n) ^ fr) * 32) + pp * 8;
#pragma unroll
  for (int m = 0; m < MT; ++m)
#pragma unroll
    for (int n = 0; n < NT; ++n) acc[m][n] = f32x4{0.f, 0.f, 0.f, 0.f};

  auto compute = [&](int buf) {
    const unsigned so = buf * STG;
    v4u af[2][MT], bf[2][NT];
#pragma unroll
    for (int ks = 0; ks < 2; ++ks) {
#pragma unroll
      for (int n = 0; n < NT; ++n) {
        const v2u x0 = trread(boff[n] + so + ks * 4 * NCG * 1024), x1 = trread(boff[n] + so + ks * 4 * NCG * 1024 + 512);
        bf[ks][n] = v4u{x0.x, x0.y, x1.x, x1.y};
      }
#pragma unroll
      for (int m = 0; m < MT; ++m)
        af[ks][m] = MODE == M_O ? *(LAS v4u*)(size_t)(lb + so + wm * 8192 + lane * 16 + m * 2048 + ks * 1024) : *(LAS v4u*)(size_t)(((aoff + so) ^ (ks * 64)) + m * 2048);
    }
    __builtin_amdgcn_sched_barrier(0);
#pragma unroll
    for (int ks = 0; ks < 2; ++ks)
#pragma unroll
      for (int m = 0; m < MT; ++m)
#pragma unroll
        for (int n = 0; n < NT; ++n) acc[m][n] = MODE == M_V ? mfma32(af[ks][m], bf[ks][n], acc[m][n]) : mfma32(bf[ks][n], af[ks][m], acc[m][n]);
    WAIT_LGKM();
  };
  wgbar();
#pragma unroll
  for (int i = 0; i < NSTG - 1; ++i) stage(i, i);
  int buf = 0;
  for (int kt = 0; kt < nkt - (NSTG - 1); ++kt) {
    wait_vm<(NSTG - 2) * (2 + NB)>();
    __builtin_amdgcn_s_barrier(); CFENCE();
    stage(kt + NSTG - 1, buf >= 1 ? buf - 1 : NSTG - 1);
    compute(buf);
    buf = buf == NSTG - 1 ? 0 : buf + 1;
  }
  static_for<NSTG - 1>([&](auto ic) {
    wait_vm<(NSTG - 2 - decltype(ic)::v) * (2 + NB)>();
    __builtin_amdgcn_s_barrier(); CFENCE();
    compute(buf);
    buf = buf == NSTG - 1 ? 0 : buf + 1;
  });
}

template <int BN, int MODE, int WM = 2>
__device__ __forceinline__ void proj_task(const Params& p, LAS u8* L, rsrc_t rs, int blk, int mtx, int col0) {
  constexpr int MT = 8 / WM, NT = BN * WM / 128;
  const int tid = threadIdx.x, lane = tid & 63, w = __builtin_amdgcn_readfirstlane(tid >> 6), wm = w / (8 / WM), wn = w % (8 / WM);
  f32x4 acc[MT][NT];
  const u16* W = (mtx == 0 ? p.Wk : mtx == 1 ? p.Wv : p.Wq) + col0;
  gemm_tile<BN, MODE, WM>(L, p.x + (size_t)blk * 128 * DM, DM, W, HD, DM / 64, acc);
  const int cw = col0 + wn * 16 * NT, h = cw >> 7;
  const unsigned base = (mtx == 0 ? OFF_KX : mtx == 1 ? OFF_VX : OFF_QX) + (unsigned)((blk * 4 + (MT / 2) * wm) * NH + h) * 8192u + lane * 16;
  if (MODE == M_K) {
#pragma unroll
    for (int m = 0; m < MT; ++m) {
      const unsigned o = base + (m >> 1) * (NH * 8192u) + ((m & 1) * 4 + ((cw & 127) >> 5)) * 1024;
#pragma unroll
      for (int v = 0; v < NT / 2; ++v)
        st16(rs, o + v * 1024, v4u{pk2(acc[m][2 * v][0], acc[m][2 * v + 1][0]), pk2(acc[m][2 * v][1], acc[m][2 * v + 1][1]),
                                   pk2(acc[m][2 * v][2], acc[m][2 * v + 1][2]), pk2(acc[m][2 * v][3], acc[m][2 * v + 1][3])});
    }
  } else {
#pragma unroll
    for (int sg = 0; sg < MT / 2; ++sg)
#pragma unroll
      for (int n = 0; n < NT; ++n)
        st16(rs, base + sg * (NH * 8192u) + (((cw & 127) >> 4) + n) * 1024,
             v4u{pk2(acc[2 * sg][n][0], acc[2 * sg][n][1]), pk2(acc[2 * sg][n][2], acc[2 * sg][n][3]),
                 pk2(acc[2 * sg + 1][n][0], acc[2 * sg + 1][n][1]), pk2(acc[2 * sg + 1][n][2], acc[2 * sg + 1][n][3])});
  }
  DRAIN_VM();
  wgbar();
  gu32* ctrl = (gu32*)p.ws;
  if (tid < BN / 64) {
    const int word = mtx == 2 ? W_FQ + blk * 64 + (col0 >> 6) + tid : W_FKV + blk * 128 + mtx * 64 + (col0 >> 6) + tid;
    __hip_atomic_store(ctrl + word, 1u, RLX_AGENT);
  }
}

__device__ __forceinline__ float sigmoidf_(float v) { return 1.0f / (1.0f + __expf(-v)); }

__device__ __forceinline__ void rg_task(const Params& p, rsrc_t rs, int wk) {
  const int tid = threadIdx.x, lane = tid & 63, w = __builtin_amdgcn_readfirstlane(tid >> 6);
  v4u wr[2], gw[2];
#pragma unroll
  for (int i = 0; i < 2; ++i) { wr[i] = *(const GAS v4u*)(p.wr + i * 512 + lane * 8); gw[i] = *(const GAS v4u*)(p.gw + i * 512 + lane * 8); }
#pragma unroll
  for (int t = 0; t < 2; ++t) {
    const int tok = wk * 16 + 2 * w + t;
    float r = 0.f, gs = 0.f;
#pragma unroll
    for (int i = 0; i < 2; ++i) {
      const v4u xv = *(const GAS v4u*)(p.x + (size_t)tok * DM + i * 512 + lane * 8);
#pragma unroll
      for (int d = 0; d < 4; ++d) {
        r = fmaf(bflo(xv[d]), bflo(wr[i][d]), r); r = fmaf(bfhi(xv[d]), bfhi(wr[i][d]), r);
        gs += bfr(bflo(xv[d]) * bflo(gw[i][d])); gs += bfr(bfhi(xv[d]) * bfhi(gw[i][d]));
      }
    }
    r = bfr(wave_sum(r)); gs = bfr(wave_sum(gs));
    if (lane == 0) { st4(rs, OFF_R + tok * 4, f2u(r)); st4(rs, OFF_G + tok * 4, f2u(bfr(sigmoidf_(gs)))); }
  }
  DRAIN_VM();
  wgbar();
  if (tid == 0) __hip_atomic_fetch_add((gu32*)p.ws + W_RG, 1u, RLX_AGENT);
}

__device__ __forceinline__ unsigned long long top_key(unsigned bits, int idx) {
  const unsigned o = (bits & 0x7FFFFFFFu) > 0x7F800000u ? 0u : ((bits & 0x80000000u) ? ~bits : (bits | 0x80000000u));
  return ((unsigned long long)o << 32) | (unsigned)(0x7FFFFFFF - idx);
}
__device__ __forceinline__ void top_ins(unsigned long long k, unsigned long long& a, unsigned long long& b) {
  const unsigned long long lo = k < a ? k : a;
  a = k < a ? a : k;
  b = lo > b ? lo : b;
}
__device__ __forceinline__ void top2(rsrc_t rs, LAS u8* L, int& i0, int& i1) {
  const int tid = threadIdx.x, lane = tid & 63, w = tid >> 6;
  const v4u rv = ld16(rs, OFF_R + tid * 16);
  unsigned long long a = 0ull, b = 0ull;
#pragma unroll
  for (int d = 0; d < 4; ++d) top_ins(top_key(rv[d], tid * 4 + d), a, b);
#pragma unroll
  for (int o = 1; o < 64; o <<= 1) {
    const unsigned long long c0 = __shfl_xor(a, o), c1 = __shfl_xor(b, o);
    top_ins(c0, a, b); top_ins(c1, a, b);
  }
  LAS unsigned long long* tv = (LAS unsigned long long*)L;
  wgbar();
  if (lane == 0) { tv[2 * w] = a; tv[2 * w + 1] = b; }
  wgbar();
  a = b = 0ull;
#pragma unroll
  for (int k = 0; k < 16; ++k) top_ins(tv[k], a, b);
  wgbar();
  i0 = (0x7FFFFFFF - (int)(unsigned)a) & 2047; i1 = (0x7FFFFFFF - (int)(unsigned)b) & 2047;
}

__device__ __forceinline__ void mlp1_task(const Params& p, LAS u8* L, rsrc_t rs, int t, bool& dead) {
  const int tid = threadIdx.x;
  gu32* ctrl = (gu32*)p.ws;
  wait_ge(ctrl, W_RG, NWORK, 0x40u, dead);
  int i0, i1;
  top2(rs, L, i0, i1);
  if (t == 0 && tid == 0) {
    const float r0 = u2f(ld4(rs, OFF_R + i0 * 4)), r1 = u2f(ld4(rs, OFF_R + i1 * 4));
    st16(rs, W_TOP * 4, v4u{(unsigned)i0, (unsigned)i1, f2u(bfr(sigmoidf_(r0))), f2u(bfr(sigmoidf_(r1)))});
  }
  const int rg = tid >> 3, cq = tid & 7;
  float ag[2][8], au[2][8];
#pragma unroll
  for (int e = 0; e < 8; ++e) { ag[0][e] = ag[1][e] = au[0][e] = au[1][e] = 0.f; }
#pragma unroll 4
  for (int i = 0; i < 16; ++i) {
    const int k = rg + 64 * i;
    const v4u gv = *(const GAS v4u*)(p.wg + (size_t)k * FF + 64 * t + 8 * cq), uv = *(const GAS v4u*)(p.wu + (size_t)k * FF + 64 * t + 8 * cq);
    const float x0 = bf2f(p.x[(size_t)i0 * DM + k]), x1 = bf2f(p.x[(size_t)i1 * DM + k]);
#pragma unroll
    for (int d = 0; d < 4; ++d) {
      ag[0][2 * d] = fmaf(x0, bflo(gv[d]), ag[0][2 * d]); ag[0][2 * d + 1] = fmaf(x0, bfhi(gv[d]), ag[0][2 * d + 1]);
      ag[1][2 * d] = fmaf(x1, bflo(gv[d]), ag[1][2 * d]); ag[1][2 * d + 1] = fmaf(x1, bfhi(gv[d]), ag[1][2 * d + 1]);
      au[0][2 * d] = fmaf(x0, bflo(uv[d]), au[0][2 * d]); au[0][2 * d + 1] = fmaf(x0, bfhi(uv[d]), au[0][2 * d + 1]);
      au[1][2 * d] = fmaf(x1, bflo(uv[d]), au[1][2 * d]); au[1][2 * d + 1] = fmaf(x1, bfhi(uv[d]), au[1][2 * d + 1]);
    }
  }
  LAS float* R = (LAS float*)L;
#pragma unroll
  for (int e = 0; e < 8; ++e) {
    R[(0 * 64 + rg) * 65 + 8 * cq + e] = ag[0][e]; R[(1 * 64 + rg) * 65 + 8 * cq + e] = ag[1][e];
    R[(2 * 64 + rg) * 65 + 8 * cq + e] = au[0][e]; R[(3 * 64 + rg) * 65 + 8 * cq + e] = au[1][e];
  }
  wgbar();
  if (tid < 128) {
    const int tok = tid >> 6, col = tid & 63;
    float gs = 0.f, us = 0.f;
    for (int r = 0; r < 64; ++r) { gs += R[(tok * 64 + r) * 65 + col]; us += R[((2 + tok) * 64 + r) * 65 + col]; }
    gs = bfr(gs); us = bfr(us);
    const float act = bfr(gs * sigmoidf_(gs));
    st4(rs, OFF_HID + (tok * FF + 64 * t + col) * 4, f2u(bfr(act * us)));
  }
  DRAIN_VM();
  wgbar();
  if (tid == 0) __hip_atomic_fetch_add(ctrl + W_M1, 1u, RLX_AGENT);
}

__device__ __forceinline__ void mlp2_task(const Params& p, LAS u8* L, rsrc_t rs, int t, bool& dead) {
  const int tid = threadIdx.x;
  gu32* ctrl = (gu32*)p.ws;
  wait_ge(ctrl, W_M1, 56, 0x41u, dead);
  CFENCE();
  const int kc = t >> 2, cb = t & 3, rg = tid >> 5, cq = tid & 31;
  float a[2][8];
#pragma unroll
  for (int e = 0; e < 8; ++e) a[0][e] = a[1][e] = 0.f;
#pragma unroll 4
  for (int i = 0; i < 16; ++i) {
    const int k = 256 * kc + rg + 16 * i;
    const v4u dv = *(const GAS v4u*)(p.wd + (size_t)k * DM + 256 * cb + 8 * cq);
    const float h0 = u2f(ld4(rs, OFF_HID + k * 4)), h1 = u2f(ld4(rs, OFF_HID + (FF + k) * 4));
#pragma unroll
    for (int d = 0; d < 4; ++d) {
      a[0][2 * d] = fmaf(h0, bflo(dv[d]), a[0][2 * d]); a[0][2 * d + 1] = fmaf(h0, bfhi(dv[d]), a[0][2 * d + 1]);
      a[1][2 * d] = fmaf(h1, bflo(dv[d]), a[1][2 * d]); a[1][2 * d + 1] = fmaf(h1, bfhi(dv[d]), a[1][2 * d + 1]);
    }
  }
  LAS float* R = (LAS float*)L;
  wgbar();
#pragma unroll
  for (int e = 0; e < 8; ++e) { R[(0 * 16 + rg) * 257 + 8 * cq + e] = a[0][e]; R[(1 * 16 + rg) * 257 + 8 * cq + e] = a[1][e]; }
  wgbar();
  {
    const int tok = tid >> 8, col = tid & 255;
    float sacc = 0.f;
#pragma unroll
    for (int r = 0; r < 16; ++r) sacc += R[(tok * 16 + r) * 257 + col];
    st4(rs, OFF_YP + ((kc * 2 + tok) * DM + 256 * cb + col) * 4, f2u(sacc));
  }
  DRAIN_VM();
  wgbar();
  if (tid == 0) __hip_atomic_store(ctrl + W_M2 + t * LN, 1u, RLX_AGENT);
}

__device__ __forceinline__ void seg_softmax(const v4u (&F)[4], const v4u (&KX)[2][4], const v4u (&QX)[4], int tq, int g, v4u& pb, v2u& pm) {
  const float scale = 0.08838834764831845f, l2e = 1.4426950408889634f, neg = bfr(-1000000000.0f);
  f32x4 sA = {0.f, 0.f, 0.f, 0.f}, sB = sA, sM = sA;
#pragma unroll
  for (int u = 0; u < 4; ++u) { sM = mfma32(F[u], QX[u], sM); sA = mfma32(KX[0][u], QX[u], sA); sB = mfma32(KX[1][u], QX[u], sB); }
#pragma unroll
  for (int r = 0; r < 4; ++r) {
    sA[r] = 4 * g + r > tq ? neg : bfr(bfr(sA[r]) * scale);
    sB[r] = 16 + 4 * g + r > tq ? neg : bfr(bfr(sB[r]) * scale);
    sM[r] = g < 2 ? -INFINITY : bfr(bfr(sM[r]) * scale);
  }
  float mx = fmaxf(fmaxf(fmaxf(sA[0], sA[1]), fmaxf(sA[2], sA[3])), fmaxf(fmaxf(sB[0], sB[1]), fmaxf(sB[2], sB[3])));
  mx = fmaxf(mx, fmaxf(fmaxf(sM[0], sM[1]), fmaxf(sM[2], sM[3])));
  mx = xmax32(xmax16(mx));
  float sum = 0.f;
#pragma unroll
  for (int r = 0; r < 4; ++r) {
    sA[r] = __builtin_amdgcn_exp2f((sA[r] - mx) * l2e); sB[r] = __builtin_amdgcn_exp2f((sB[r] - mx) * l2e); sM[r] = __builtin_amdgcn_exp2f((sM[r] - mx) * l2e);
    sum += sA[r] + sB[r] + sM[r];
  }
  const float inv = 1.0f / xadd32(xadd16(sum));
  pb = v4u{pk2(sA[0] * inv, sA[1] * inv), pk2(sA[2] * inv, sA[3] * inv), pk2(sB[0] * inv, sB[1] * inv), pk2(sB[2] * inv, sB[3] * inv)};
  pm = v2u{pk2(sM[0] * inv, sM[1] * inv), pk2(sM[2] * inv, sM[3] * inv)};
}

__device__ __forceinline__ void attn_task(const Params& p, rsrc_t rs, const Near& nr, int s, int h, bool& dead) {
  const int lane = threadIdx.x & 63, r16 = lane & 15, g = lane >> 4, gg = g & 1;
  gu32* ctrl = (gu32*)p.ws;
  const int blk = s >> 2;
  wait_flags2(ctrl, W_FKV + blk * 128 + 2 * h, W_FKV + blk * 128 + 64 + 2 * h, 0x50u, dead);
  wait_flags2(ctrl, W_FQ + blk * 64 + 2 * h, W_FQ + blk * 64 + 2 * h, 0x51u, dead);
  CFENCE();
  const unsigned fo = (unsigned)(s * NH + h) * 8192u + lane * 16;
  v4u KX[2][4], QX[2][4], VX[8], F[4]; v2u VM[8];
#pragma unroll
  for (int i = 0; i < 8; ++i) { KX[i >> 2][i & 3] = ld16(rs, OFF_KX + fo + i * 1024); QX[i >> 2][i & 3] = ld16(rs, OFF_QX + fo + i * 1024); VX[i] = ld16(rs, OFF_VX + fo + i * 1024); }
  wait_ge(ctrl, W_PROG + LN * h, (unsigned)(s + 1), 0x52u, dead);
  const unsigned qo = nr.blk(OFF_QKV + (unsigned)(s * QH + (h & (QH - 1))) * 16384u, nr.rel(h * 4));
  for (unsigned sp = 0;;) {
    CFENCE();
    unsigned m = 0;
#pragma unroll
    for (int u = 0; u < 4; ++u) { F[u] = ld16(rs, qo + u * 1024 + lane * 16); m = max(m, umax4(F[u])); }
#pragma unroll
    for (int e = 0; e < 8; ++e) { VM[e] = ld8(rs, qo + 4096 + (16 * e + r16) * 16 + gg * 8); m = max(m, max(VM[e].x, VM[e].y)); }
    if (__all(m != SENT) || dead) break;
    if (spin_fail(sp, ctrl, 0x53u)) { dead = true; break; }
  }
#pragma unroll
  for (int qt = 0; qt < 2; ++qt) {
    v4u pb; v2u pm;
    seg_softmax(F, KX, QX[qt], 16 * qt + r16, g, pb, pm);
    const unsigned ao = OFF_AX + fo + qt * 4096;
#pragma unroll
    for (int v = 0; v < 4; ++v) {
      f32x4 o0 = mfma32(VX[2 * v], pb, f32x4{0.f, 0.f, 0.f, 0.f}), o1 = mfma32(VX[2 * v + 1], pb, f32x4{0.f, 0.f, 0.f, 0.f});
      o0 = mfma16(VM[2 * v], pm, o0); o1 = mfma16(VM[2 * v + 1], pm, o1);
      st16(rs, ao + v * 1024, v4u{pk2(o0[0], o0[1]), pk2(o0[2], o0[3]), pk2(o1[0], o1[1]), pk2(o1[2], o1[3])});
    }
  }
  DRAIN_VM();
}

__device__ __forceinline__ void mix_store(const Params& p, rsrc_t rs, const v4u& top, int tok, int col, f32x4 a) {
  const float gt = u2f(ld4(rs, OFF_G + tok * 4)), og = bfr(1.0f - gt);
  const int sel = tok == (int)top.x ? 0 : tok == (int)top.y ? 1 : -1;
  const v2u xv = *(const GAS v2u*)(p.x + (size_t)tok * DM + col);
  float bv[4] = {bflo(xv.x), bfhi(xv.x), bflo(xv.y), bfhi(xv.y)};
  if (sel >= 0) {
    f32x4 y = {0.f, 0.f, 0.f, 0.f};
    for (int kc = 0; kc < 14; ++kc) y += __builtin_bit_cast(f32x4, ld16(rs, OFF_YP + ((kc * 2 + sel) * DM + col) * 4));
    const float rsel = u2f(sel == 0 ? top.z : top.w);
#pragma unroll
    for (int r = 0; r < 4; ++r) bv[r] = bfr(bv[r] + bfr(bfr(y[r]) * rsel));
  }
  float o[4];
#pragma unroll
  for (int r = 0; r < 4; ++r) o[r] = bfr(gt * bfr(a[r])) + bfr(og * bv[r]);
  *(GAS v2u*)(p.out + (size_t)tok * DM + col) = v2u{pk2(o[0], o[1]), pk2(o[2], o[3])};
}

__device__ __forceinline__ void oproj_task(const Params& p, LAS u8* L, rsrc_t rs, int blk, int ct, bool& dead) {
  const int tid = threadIdx.x, lane = tid & 63, w = __builtin_amdgcn_readfirstlane(tid >> 6), wm = w >> 2, wn = w & 3, r16 = lane & 15, g = lane >> 4;
  gu32* ctrl = (gu32*)p.ws;
  if (w == 0) {
    wait_lines(ctrl, W_M2, LN, 56, 1u, lane, 0x60u, dead);
    wait_lines(ctrl, W_T2 + 8 * blk * LN, LN, 8, 1u, lane, 0x61u, dead);
    __builtin_amdgcn_fence(__ATOMIC_ACQUIRE, "agent");
    DRAIN_VM();
  }
  f32x4 acc[4][2];
  gemm_tile<128, M_O>(L, (const u16*)(p.ws + OFF_AX + (size_t)blk * 4 * NH * 8192), HD, p.Wo + ct * 128, DM, HD / 64, acc);
  const v4u top = ld16(rs, W_TOP * 4);
#pragma unroll
  for (int m = 0; m < 4; ++m)
#pragma unroll
    for (int n = 0; n < 2; ++n) mix_store(p, rs, top, blk * 128 + 64 * wm + 16 * m + r16, ct * 128 + 32 * wn + 16 * n + 4 * g, acc[m][n]);
}

__device__ __forceinline__ void stream_role(const Params& p, LAS u8* L, rsrc_t rs, const Near& nr, int set, int q, bool& dead) {
  int wst = 100; const int wk = set * 32 + q; (void)wst; (void)wk;
  const int tid = threadIdx.x, lane = tid & 63, w = __builtin_amdgcn_readfirstlane(tid >> 6), r16 = lane & 15, g = lane >> 4, gg = g & 1;
  const int ks = q >> 3, ct = q & 7, np = w & 3, kh = w >> 2;
  gu32* ctrl = (gu32*)p.ws;
  const unsigned lb = (unsigned)(size_t)L;
  v4u Wf[16][2];
  LAS u8* Wl = L + 65536 + w * 8192 + lane * 16;
  {
    const int kr = 8 * w + (lane >> 3);
    const int krs = (kr & ~31) + 16 * ((kr >> 2) & 1) + 4 * ((kr >> 3) & 3) + (kr & 3);
    const int fsw = ((kr >> 1) & 1) | (((kr >> 3) & 1) << 1);
    const unsigned wvo = (krs * DM + 16 * (((lane & 7) >> 1) ^ fsw) + 8 * (lane & 1)) * 2;
    const int qq = r16 >> 2, fr = ((qq >> 1) & 1) | ((g & 1) << 1);
    const unsigned boff = lb + kh * 65536 + (g * 2 + (np >> 1)) * 1024 + qq * 128 + (lane & 3) * 8;
    const unsigned b0 = boff + (((2 * (np & 1)) ^ fr) * 32), b1 = boff + (((2 * (np & 1) + 1) ^ fr) * 32);
    static_for<2>([&](auto rc) {
      constexpr int RD = decltype(rc)::v;
      wgbar();
#pragma unroll
      for (int kt = 0; kt < 8; ++kt) {
        const u16* wb = p.Wo + (size_t)(1024 * ks + 512 * (kt >> 2) + 64 * (4 * RD + (kt & 3))) * DM + 128 * ct;
        dma16<false>(wb, wvo, lb + kt * 16384 + (w * 2) * 1024);
        dma16<false>(wb + 64, wvo, lb + kt * 16384 + (w * 2 + 1) * 1024);
      }
      DRAIN_VM();
      wgbar();
#pragma unroll
      for (int i = 0; i < 8; ++i) {
        const unsigned o = (i >> 1) * 16384 + (i & 1) * 8192;
        const v2u x0 = trread(b0 + o), x1 = trread(b0 + o + 512), y0 = trread(b1 + o), y1 = trread(b1 + o + 512);
        Wf[8 * RD + i][0] = v4u{x0.x, x0.y, x1.x, x1.y}; Wf[8 * RD + i][1] = v4u{y0.x, y0.y, y1.x, y1.y};
      }
    });
  }
  wgbar();
#pragma unroll
  for (int i = 0; i < 8; ++i) *(LAS v4u*)(Wl + i * 1024) = Wf[12 + (i >> 1)][i & 1];
  wait_lines(ctrl, W_M2, LN, 56, 1u, lane, 0x70u, dead);
  {
    const int fb = NBULK + (lane & 7), kind = (lane >> 3) % 3, hf = lane >= 24 ? 1 : 0;
    gu32* f = ctrl + (kind == 2 ? W_FQ + fb * 64 : W_FKV + fb * 128 + kind * 64) + 2 * q + hf;
    for (unsigned sp = 0; !dead;) {
      if (__all(__hip_atomic_load(f, RLX_AGENT) != 0u)) break;
      __builtin_amdgcn_s_sleep(8);
      if (spin_fail(sp, ctrl, 0x73u)) dead = true;
    }
  }
  CFENCE();
  const v4u top = ld16(rs, W_TOP * 4);

  auto run = [&](auto role) {
    constexpr bool ATT = decltype(role)::v == 0;
    const int qt = w & 1, eh = w >> 1;
    v4u KX[2][4], QX[4], VX[4];
    auto prefetch = [&](int s) {
      const unsigned fo = (unsigned)(s * NH + q) * 8192u + lane * 16;
#pragma unroll
      for (int i = 0; i < 8; ++i) KX[i >> 2][i & 3] = ld16(rs, OFF_KX + fo + i * 1024);
#pragma unroll
      for (int i = 0; i < 4; ++i) { QX[i] = ld16(rs, OFF_QX + fo + qt * 4096 + i * 1024); VX[i] = ld16(rs, OFF_VX + fo + (4 * eh + i) * 1024); }
    };
    auto finish = [&](int s) {
      const int f = 4 * ks + (w - 4);
      wait_lines(ctrl, W_SP + (set * 32 + ct) * LN, 8 * LN, 4, (unsigned)(s + 1), lane, 0x72u, dead);
      CFENCE();
      const unsigned fo = OFF_PS + (unsigned)((s - NBULK * 4) * 8 + ct) * 131072u + f * 1024 + lane * 16;
      f32x4 sum = __builtin_bit_cast(f32x4, ld16(rs, fo));
#pragma unroll
      for (int k = 1; k < 8; ++k) sum += __builtin_bit_cast(f32x4, ld16(rs, fo + k * 16384));
      mix_store(p, rs, top, s * 32 + 16 * (f & 1) + r16, ct * 128 + 16 * (f >> 1) + 4 * g, sum);
    };
    unsigned prog = 0;
    if (ATT) prefetch(NBULK * 4 + set);
    for (int s = NBULK * 4 + set; s < NSEG; s += 2) {
      const int si = s - NBULK * 4;
      if (ATT) {
        const unsigned need = (unsigned)(s + 1 < NSEG - 1 ? s + 1 : NSEG - 1);
        if (prog < need) {
          for (unsigned sp = 0; !dead;) {
            prog = __builtin_amdgcn_readfirstlane(__hip_atomic_load(ctrl + W_PROG + LN * q, RLX_AGENT));
            if (prog >= need) break;
            __builtin_amdgcn_s_sleep(2);
            if (spin_fail(sp, ctrl, 0x52u)) dead = true;
          }
        }
        const unsigned qo = nr.blk(OFF_QKV + (unsigned)(s * QH + (q & (QH - 1))) * 16384u, nr.rel(q * 4));
        v4u F[4]; v2u VM[4];
        for (unsigned sp = 0;;) {
          CFENCE();
          unsigned m = 0;
#pragma unroll
          for (int u = 0; u < 4; ++u) { F[u] = ld16(rs, qo + u * 1024 + lane * 16); m = max(m, umax4(F[u])); }
#pragma unroll
          for (int e = 0; e < 4; ++e) { VM[e] = ld8(rs, qo + 4096 + (16 * (4 * eh + e) + r16) * 16 + gg * 8); m = max(m, max(VM[e].x, VM[e].y)); }
          if (__all(m != SENT) || dead) break;
          if (spin_fail(sp, ctrl, 0x53u)) { dead = true; break; }
        }
        v4u pb; v2u pm;
        seg_softmax(F, KX, QX, 16 * qt + r16, g, pb, pm);
        const unsigned ao = OFF_AX + (unsigned)(s * NH + q) * 8192u + qt * 4096 + eh * 2048 + lane * 16;
#pragma unroll
        for (int v = 0; v < 2; ++v) {
          f32x4 o0 = mfma32(VX[2 * v], pb, f32x4{0.f, 0.f, 0.f, 0.f}), o1 = mfma32(VX[2 * v + 1], pb, f32x4{0.f, 0.f, 0.f, 0.f});
          o0 = mfma16(VM[2 * v], pm, o0); o1 = mfma16(VM[2 * v + 1], pm, o1);
          st16(rs, ao + v * 1024, v4u{pk2(o0[0], o0[1]), pk2(o0[2], o0[3]), pk2(o1[0], o1[1]), pk2(o1[2], o1[3])});
        }
        DRAIN_VM();
      } else if (si >= 2) {
        finish(s - 2);
      }
      wgbar();
      WSTAMP();
      if (tid == 0) __hip_atomic_store(ctrl + W_SA + (set * 32 + q) * LN, (unsigned)(s + 1), RLX_AGENT);
      if (w == 4) wait_lines(ctrl, W_SA + (set * 32 + ks * 8) * LN, LN, 8, (unsigned)(s + 1), lane, 0x71u, dead);
      wgbar();
      WSTAMP();
      {
        const unsigned ao = OFF_AX + (unsigned)(s * NH + 8 * ks + w) * 8192u + lane * 16;
        v4u t[8];
#pragma unroll
        for (int i = 0; i < 8; ++i) t[i] = ld16(rs, ao + i * 1024);
#pragma unroll
        for (int i = 0; i < 8; ++i) *(LAS v4u*)(L + w * 8192 + i * 1024 + lane * 16) = t[i];
      }
      if (ATT && s + 2 < NSEG) prefetch(s + 2);
      wgbar();
      f32x4 a[2][2];
#pragma unroll
      for (int i = 0; i < 4; ++i) a[i >> 1][i & 1] = f32x4{0.f, 0.f, 0.f, 0.f};
#pragma unroll
      for (int k = 0; k < 16; ++k) {
        const LAS u8* fp = L + kh * 32768 + (k >> 2) * 8192 + (k & 3) * 1024 + lane * 16;
        const v4u x0 = *(const LAS v4u*)fp, x1 = *(const LAS v4u*)(fp + 4096);
#pragma unroll
        for (int n = 0; n < 2; ++n) {
          const v4u wf = k < 12 ? Wf[k][n] : *(const LAS v4u*)(Wl + ((k - 12) * 2 + n) * 1024);
          a[n][0] = mfma32(wf, x0, a[n][0]); a[n][1] = mfma32(wf, x1, a[n][1]);
        }
      }
      WSTAMP();
      const unsigned pb = OFF_PS + (unsigned)(si * 8 + ct) * 131072u + (2 * ks + kh) * 16384 + 4 * np * 1024 + lane * 16;
#pragma unroll
      for (int i = 0; i < 4; ++i) st16(rs, pb + i * 1024, __builtin_bit_cast(v4u, a[i >> 1][i & 1]));
      DRAIN_VM();
      wgbar();
      if (tid == 0) __hip_atomic_store(ctrl + W_SP + (set * 32 + q) * LN, (unsigned)(s + 1), RLX_AGENT);
      WSTAMP();
    }
    if (!ATT) finish(NSEG - 2 + set);
  };
  if (w < 4) run(IC<0>{}); else run(IC<1>{});
}

__device__ __forceinline__ void worker_role(const Params& p, LAS u8* L, rsrc_t rs, int b) {
  const int tid = threadIdx.x, lane = tid & 63, w = __builtin_amdgcn_readfirstlane(tid >> 6);
  const int x8 = 2 * (b & 3) + (b >> 7), r = (b >> 3) & 15, wk = r * 8 + x8;
  gu32* ctrl = (gu32*)p.ws;
  bool dead = false;
  int wst = 0; (void)wst;
  Near nr = near_init(p.ws);
  WSTAMP();
  {
    const int cg = x8 * 16 + r;
    for (int blk = 0; blk < 2; ++blk) {
      if (cg < 64) proj_task<64, M_K, 4>(p, L, rs, blk, 0, cg * 64); else proj_task<64, M_V, 4>(p, L, rs, blk, 1, (cg - 64) * 64);
      WSTAMP();
      if (blk == 0) WSTAMP();
    }
  }
  rg_task(p, rs, wk);
  WSTAMP();
  for (int rnd = 0; rnd < 6; ++rnd) {
    const int ct = x8 * 4 + (r & 3), bq = r >> 2;
    if (rnd < 4) {
      const int blk = 2 + rnd * 4 + bq;
      if (blk < 16) { if (ct < 16) proj_task<256, M_K>(p, L, rs, blk, 0, ct * 256); else proj_task<256, M_V>(p, L, rs, blk, 1, (ct - 16) * 256); }
      else { const int t = (blk - 16) * 32 + ct; if (t < 56) mlp1_task(p, L, rs, t, dead); }
    } else {
      const int qct = x8 * 2 + (r & 1), blk = (rnd - 4) * 8 + (r >> 1);
      proj_task<256, M_K>(p, L, rs, blk, 2, qct * 256);
    }
    WSTAMP();
  }
  if (wk < 56) mlp2_task(p, L, rs, wk, dead);
  near_wait(ctrl, nr, dead);
  WSTAMP();
  if (r < 8) {
    const int blk = x8, m = r;
    for (int i = 0; i < 2; ++i) { const int t = (i * 8 + m) * 8 + w; attn_task(p, rs, nr, blk * 4 + (t >> 5), t & 31, dead); WSTAMP(); }
    wgbar();
    if (tid == 0) __hip_atomic_store(ctrl + W_T2 + (8 * blk + m) * LN, 1u, RLX_AGENT);
    oproj_task(p, L, rs, blk, m, dead);
    WSTAMP();
  } else {
    const int sid = x8 * 8 + (r - 8);
    stream_role(p, L, rs, nr, sid >> 5, sid & 31, dead);
    WSTAMP();
  }
}

__global__ void __launch_bounds__(512) rmt_init_kernel(u8* ws, const u16* mem0) {
  const int tid = threadIdx.x;
  if (blockIdx.x >= 4) {
    const Near n = near_init(ws);
    arm_share(__builtin_amdgcn_make_buffer_rsrc(ws, 0, WS_END, 0x00020000), n.al, blockIdx.x - 4, tid);
    return;
  }
  if (blockIdx.x >= 2) {
    const Near n = near_init(ws);
    const int i = (blockIdx.x - 2) * 512 + tid, cb = i >> 3, row = i & 7;
    const u16* m0 = mem0 + row * DM + (cb >> 2) * 32 + (cb & 3) * 4;
    const v2u lo = *(const GAS v2u*)m0, hi = *(const GAS v2u*)(m0 + 16);
    const v4u v = {canon(__builtin_amdgcn_perm(hi.x, lo.x, 0x05040100u)), canon(__builtin_amdgcn_perm(hi.x, lo.x, 0x07060302u)),
                   canon(__builtin_amdgcn_perm(hi.y, lo.y, 0x05040100u)), canon(__builtin_amdgcn_perm(hi.y, lo.y, 0x07060302u))};
    u8* d = ws + n.al + OFF_MEM + (cb >> 6) * 16384 + (cb & 63) * 128 + row * 16;
    *(GAS v4u*)d = v; *(GAS v4u*)(d + 8192) = v;
    return;
  }
  if (blockIdx.x == 0) {
    for (int i = tid; i < (int)(CTRL_BYTES / 16); i += 512)
      if (i != W_CFG / 4) ((GAS v4u*)ws)[i] = v4u{0u, 0u, 0u, 0u};
  } else if (tid == 0) {
    const Near n = near_init(ws);
    gu32* a0 = (gu32*)(ws + n.al + OFF_CAL); gu32* a1 = a0 + 2048;
    unsigned long long b0 = ~0ull, b1 = ~0ull;
    for (int i = 0; i < 3; ++i) {
      const unsigned long long t0 = __builtin_amdgcn_s_memtime();
      __hip_atomic_store(a0, 0u, RLX_AGENT); DRAIN_VM();
      const unsigned long long t1 = __builtin_amdgcn_s_memtime();
      __hip_atomic_store(a1, 0u, RLX_AGENT); DRAIN_VM();
      const unsigned long long t2 = __builtin_amdgcn_s_memtime();
      b0 = t1 - t0 < b0 ? t1 - t0 : b0; b1 = t2 - t1 < b1 ? t2 - t1 : b1;
    }
    const unsigned nz = ((b1 < b0 ? 1u : 0u) ^ ((n.va21 + OFF_CAL) >> 21) ^ (xcc_id() >> 2)) & 1u;
    ((GAS v4u*)ws)[W_CFG / 4] = v4u{nz, 0u, 0u, 0u};
  }
}

__global__ void __launch_bounds__(NTHR, 2) rmt_mod_kernel(Params p) {
  extern __shared__ __attribute__((aligned(16))) u8 smem[];
  LAS u8* L = (LAS u8*)smem;
  const rsrc_t rs = __builtin_amdgcn_make_buffer_rsrc(p.ws, 0, WS_END, 0x00020000);
  const int b = blockIdx.x;
  if ((b & 4) == 0) chain_role(p, L, rs, b); else worker_role(p, L, rs, b);
}

}

extern "C" void kernel_launch(void* const* d_in, const int* in_sizes, int n_in, void* d_out, int out_size, void* d_ws, size_t ws_size,
                              hipStream_t stream) {
  (void)in_sizes; (void)n_in; (void)out_size; (void)ws_size;
  Params p;
  p.x = (const u16*)d_in[0]; p.mem0 = (const u16*)d_in[1]; p.Wq = (const u16*)d_in[2]; p.Wk = (const u16*)d_in[3];
  p.Wv = (const u16*)d_in[4]; p.Wo = (const u16*)d_in[5]; p.wr = (const u16*)d_in[8]; p.wg = (const u16*)d_in[9];
  p.wu = (const u16*)d_in[10]; p.wd = (const u16*)d_in[11]; p.gw = (const u16*)d_in[12];
  p.out = (u16*)d_out; p.ws = (u8*)d_ws;
  (void)hipFuncSetAttribute((const void*)rmt_mod_kernel, hipFuncAttributeMaxDynamicSharedMemorySize, LDS_BYTES);
  rmt_init_kernel<<<4 + NCHAIN, 512, 0, stream>>>(p.ws, p.mem0);
  rmt_mod_kernel<<<NWG, NTHR, LDS_BYTES, stream>>>(p);
}
```

```cpp
#include <hip/hip_runtime.h>
#include <hip/hip_bf16.h>
#include <stdint.h>

#define GAS __attribute__((address_space(1)))
#define LAS __attribute__((address_space(3)))
typedef unsigned v4u __attribute__((ext_vector_type(4)));
typedef unsigned v2u __attribute__((ext_vector_type(2)));
typedef unsigned v8u __attribute__((ext_vector_type(8)));
typedef float f32x4 __attribute__((ext_vector_type(4)));
typedef __bf16 bfx8 __attribute__((ext_vector_type(8)));
typedef __bf16 bfx16 __attribute__((ext_vector_type(16)));
typedef short s16x4 __attribute__((ext_vector_type(4)));
typedef GAS unsigned gu32;
typedef unsigned short u16;
typedef unsigned char u8;
#define RLX_AGENT __ATOMIC_RELAXED, __HIP_MEMORY_SCOPE_AGENT

#define PBF 1
#define SPLIT_XCD 2
namespace {

constexpr int SEQ = 2048, DM = 1024, NH = 32, HD = 4096, NSEG = 64, FF = 3584;
constexpr int NCHAIN = 128, NWORK = 128, NWG = 256, NTHR = 512;
constexpr unsigned SENT = 0xFFFFFFFFu;
constexpr int AUX_SC1 = 16;

constexpr unsigned CTRL_BYTES = 73728;
constexpr int LN = 32;
constexpr int W_TMO = 0, W_RG = 1 * LN, W_M1 = 2 * LN, W_TOP = 3 * LN  , W_CFG = 4 * LN  ,
              W_XCC = 5 * LN, W_PROG = 8 * LN  , W_FKV = 64 * LN  , W_FQ = 128 * LN  ,
              W_ARM = 160 * LN  , W_M2 = 288 * LN  , W_T2 = 344 * LN  ,
              W_SA = 408 * LN  , W_SP = 472 * LN  ;
constexpr unsigned OFF_MEM = 98304;
constexpr unsigned MEM_ST = 32768;
constexpr unsigned OFF_QKV = OFF_MEM + NSEG * MEM_ST;
constexpr unsigned QH = 32, PR = 128;
constexpr unsigned OFF_PART = OFF_QKV + NSEG * QH * 16384;
constexpr unsigned PART_SZ = PR * 16384;
#define NSLOT 3
constexpr unsigned OFF_ARM_END = OFF_PART + NSLOT * PART_SZ;
constexpr unsigned OFF_CAL = OFF_ARM_END;
constexpr unsigned OFF_KX = OFF_CAL + 16384 + 16384;
#define WFAR 1
constexpr unsigned FB = WFAR ? 16384 : 8192;
constexpr unsigned OFF_VX = OFF_KX + NSEG * NH * FB;
constexpr unsigned OFF_QX = OFF_VX + NSEG * NH * FB;
constexpr unsigned OFF_AX = OFF_QX + NSEG * NH * FB;
constexpr unsigned OFF_R = OFF_AX + NSEG * NH * FB + 512 * FB + 16384;
constexpr unsigned OFF_G = OFF_R + SEQ * 4;
constexpr unsigned OFF_HID = OFF_G + SEQ * 4;
constexpr unsigned OFF_YP = OFF_HID + 2 * FF * 4;
constexpr unsigned OFF_PS = OFF_AX + NSEG * NH * FB;
constexpr unsigned WS_END = OFF_YP + 14 * 2 * DM * 4;
constexpr int NBULK = 8;
constexpr int LDS_BYTES = 160 * 1024;
constexpr unsigned SPIN_CAP = 1u << 18;

#define TAGP(...) (__VA_ARGS__)
#define BG_DELAY 16
#define ARM_LATE 0
#define W_A 360
#define W_B 80
#define W_C 260

struct Params {
  const u16 *x, *mem0, *Wq, *Wk, *Wv, *Wo, *wr, *wg, *wu, *wd, *gw;
  u16* out;
  u8* ws;
};

__device__ __forceinline__ float u2f(unsigned u) { return __builtin_bit_cast(float, u); }
__device__ __forceinline__ unsigned f2u(float f) { return __builtin_bit_cast(unsigned, f); }
__device__ __forceinline__ float bf2f(unsigned b) { return u2f(b << 16); }
__device__ __forceinline__ float bflo(unsigned d) { return u2f(d << 16); }
__device__ __forceinline__ float bfhi(unsigned d) { return u2f(d & 0xFFFF0000u); }
__device__ __forceinline__ unsigned pk2(float lo, float hi) {
  typedef __bf16 bfx2 __attribute__((ext_vector_type(2)));
  bfx2 v; v.x = (__bf16)lo; v.y = (__bf16)hi;
  return __builtin_bit_cast(unsigned, v);
}
__device__ __forceinline__ float bfr(float f) { return (float)(__bf16)f; }
__device__ __forceinline__ unsigned canon(unsigned d) { return min(d, 0xFFFFFFFEu); }
__device__ __forceinline__ f32x4 mfma32(v4u a, v4u b, f32x4 c) {
  return __builtin_amdgcn_mfma_f32_16x16x32_bf16(__builtin_bit_cast(bfx8, a), __builtin_bit_cast(bfx8, b), c, 0, 0, 0);
}
__device__ __forceinline__ f32x4 mfma16(v2u a, v2u b, f32x4 c) { return mfma32(v4u{a.x, a.y, 0u, 0u}, v4u{b.x, b.y, 0u, 0u}, c); }
__device__ __forceinline__ f32x4 smf64(v4u a, v8u b, f32x4 c, int idx) {
  return __builtin_amdgcn_smfmac_f32_16x16x64_bf16(__builtin_bit_cast(bfx8, a), __builtin_bit_cast(bfx16, b), c, idx, 0, 0);
}
__device__ __forceinline__ v2u trread(unsigned lds_addr) {
  return __builtin_bit_cast(v2u, __builtin_amdgcn_ds_read_tr16_b64_v4i16((LAS s16x4*)(size_t)lds_addr));
}
typedef float f32x2 __attribute__((ext_vector_type(2)));
__device__ __forceinline__ f32x2 fold32(f32x4 v) {
  const auto r0 = __builtin_amdgcn_permlane32_swap(f2u(v[0]), f2u(v[2]), false, false);
  const auto r1 = __builtin_amdgcn_permlane32_swap(f2u(v[1]), f2u(v[3]), false, false);
  return f32x2{u2f(r0[0]), u2f(r1[0])} + f32x2{u2f(r0[1]), u2f(r1[1])};
}
__device__ __forceinline__ float xmax16(float a) { auto r = __builtin_amdgcn_permlane16_swap(f2u(a), f2u(a), false, false); return __builtin_amdgcn_fmed3f(u2f(r[0]), u2f(r[1]), INFINITY); }
__device__ __forceinline__ float xmax32(float a) { auto r = __builtin_amdgcn_permlane32_swap(f2u(a), f2u(a), false, false); return __builtin_amdgcn_fmed3f(u2f(r[0]), u2f(r[1]), INFINITY); }
__device__ __forceinline__ float xadd16(float a) { auto r = __builtin_amdgcn_permlane16_swap(f2u(a), f2u(a), false, false); return u2f(r[0]) + u2f(r[1]); }
__device__ __forceinline__ float xadd32(float a) { auto r = __builtin_amdgcn_permlane32_swap(f2u(a), f2u(a), false, false); return u2f(r[0]) + u2f(r[1]); }

__device__ __forceinline__ float wave_sum(float v) {
  v += __shfl_xor(v, 1); v += __shfl_xor(v, 2); v += __shfl_xor(v, 4); v += __shfl_xor(v, 8);
  return xadd32(xadd16(v));
}
#define CFENCE() asm volatile("" ::: "memory")
#define DRAIN_VM() asm volatile("s_waitcnt vmcnt(0)" ::: "memory")
#define WAIT_LGKM() asm volatile("s_waitcnt lgkmcnt(0)" ::: "memory")
__device__ __forceinline__ void wgbar() { WAIT_LGKM(); __builtin_amdgcn_s_barrier(); CFENCE(); }

typedef __amdgpu_buffer_rsrc_t rsrc_t;
__device__ __forceinline__ v4u ld16(rsrc_t rs, unsigned off) { return __builtin_amdgcn_raw_buffer_load_b128(rs, off, 0, AUX_SC1); }
__device__ __forceinline__ v2u ld8(rsrc_t rs, unsigned off) { return __builtin_amdgcn_raw_buffer_load_b64(rs, off, 0, AUX_SC1); }
__device__ __forceinline__ unsigned ld4(rsrc_t rs, unsigned off) { return __builtin_amdgcn_raw_buffer_load_b32(rs, off, 0, AUX_SC1); }
__device__ __forceinline__ void st16(rsrc_t rs, unsigned off, v4u v) { __builtin_amdgcn_raw_buffer_store_b128(v, rs, off, 0, AUX_SC1); }
__device__ __forceinline__ void st8(rsrc_t rs, unsigned off, v2u v) { __builtin_amdgcn_raw_buffer_store_b64(v, rs, off, 0, AUX_SC1); }
__device__ __forceinline__ void st4(rsrc_t rs, unsigned off, unsigned v) { __builtin_amdgcn_raw_buffer_store_b32(v, rs, off, 0, AUX_SC1); }
__device__ __forceinline__ unsigned umax4(v4u v) { return max(max(v.x, v.y), max(v.z, v.w)); }

template <int N> __device__ __forceinline__ void delay_cyc() {
#pragma unroll
  for (int i = 0; i < N / 64; ++i) asm volatile("s_nop 15");
  if ((N % 64) >= 4) asm volatile("s_nop %0" :: "i"((N % 64) / 4 - 1));
}

#define PACE 0
#define PACE_GAIN 0.3f
#define PACE_CLAMP 2.0f
#define GAP_A 6
#define DB3 5
#define EXTRA_B 0
#define EXTRA_C 0
#define GAP_C 6
#define AD_DEC 0
#define AD_INC 0
__device__ __forceinline__ void delay_rt(int n) {
  asm volatile("s_cmp_lt_i32 %0, 1\n\ts_cbranch_scc1 2f\n\ts_mov_b32 vcc_lo, %0\n1:\n\ts_nop 1\n\ts_sub_i32 vcc_lo, vcc_lo, 1\n\ts_cmp_gt_i32 vcc_lo, 0\n\ts_cbranch_scc1 1b\n2:" :: "s"(n) : "vcc", "scc");
}
__device__ __forceinline__ void adapt(int& n, int misses) { n = misses ? min(n + AD_INC, 120) : max(n - AD_DEC, 0); }

__device__ __forceinline__ bool spin_fail(unsigned& spins, gu32* ctrl, unsigned code) {
  ++spins;
  if (spins > SPIN_CAP) { __hip_atomic_store(ctrl + W_TMO, 1u, RLX_AGENT); __hip_atomic_fetch_max(ctrl + W_TMO + 1, code, RLX_AGENT); return true; }
  return (spins & 255u) == 0u && __builtin_amdgcn_readfirstlane(__hip_atomic_load(ctrl + W_TMO, RLX_AGENT)) != 0u;
}
constexpr int RETRY_CAP = 1 << 15;
__device__ __forceinline__ void wait_ge(gu32* ctrl, int word, unsigned want, unsigned code, bool& dead) {
  if (dead) return;
  for (unsigned sp = 0;;) {
    if ((unsigned)__builtin_amdgcn_readfirstlane(__hip_atomic_load(ctrl + word, RLX_AGENT)) >= want) return;
    __builtin_amdgcn_s_sleep(4);
    if (spin_fail(sp, ctrl, code)) { dead = true; return; }
  }
}
__device__ __forceinline__ void wait_flags2(gu32* ctrl, int w0, int w1, unsigned code, bool& dead) {
  if (dead) return;
  for (unsigned sp = 0;;) {
    const unsigned long long a = __hip_atomic_load((GAS unsigned long long*)(ctrl + w0), RLX_AGENT);
    const unsigned long long b = __hip_atomic_load((GAS unsigned long long*)(ctrl + w1), RLX_AGENT);
    const unsigned long long m = a & b;
    if (__builtin_amdgcn_readfirstlane((unsigned)m & (unsigned)(m >> 32)) != 0u) return;
    __builtin_amdgcn_s_sleep(4);
    if (spin_fail(sp, ctrl, code)) { dead = true; return; }
  }
}
__device__ __forceinline__ void wait_lines(gu32* ctrl, int word, int stride, int n, unsigned want, int lane, unsigned code, bool& dead) {
  if (dead) return;
  gu32* mine = ctrl + word + (lane < n ? lane : 0) * stride;
  for (unsigned sp = 0;;) {
    if (__all(__hip_atomic_load(mine, RLX_AGENT) >= want)) return;
    __builtin_amdgcn_s_sleep(2);
    if (spin_fail(sp, ctrl, code)) { dead = true; return; }
  }
}
constexpr int ARM_AHEAD = 4;
__device__ __forceinline__ void arm_share(rsrc_t rs, unsigned al, int c, int tid) {
  const v4u s = {SENT, SENT, SENT, SENT};
  const unsigned lo = (tid & 63) * 16, w = tid >> 6;
  constexpr int NB = PR / 64;
  for (int sl = 0; sl < NSLOT; ++sl)
#pragma unroll
    for (int k = 0; k < NB; ++k) if (!PBF || w < 4) st16(rs, al + OFF_PART + sl * PART_SZ + (c * NB + k) * 8192 + w * 1024 + lo, s);
  if (w < 6) {
#pragma unroll
    for (int k = 0; k < NB; ++k) st16(rs, al + OFF_QKV + (c * NB + k) * 8192 + w * 1024 + lo, s);
  } else if (w == 6) st16(rs, al + OFF_MEM + MEM_ST + c * 1024 + lo, s);
  else if (c < 32) st16(rs, al + OFF_MEM + 5 * MEM_ST + c * 1024 + lo, s);
}
struct Near {
  unsigned al, va21, nz, k;
  __device__ __forceinline__ unsigned rel(int c) const { return k >> 2; }
  __device__ __forceinline__ unsigned frag(unsigned reg, int s, int h) const { return WFAR ? blk(reg + (unsigned)(s * NH + h) * FB, (k >> 2) ^ 1u) : reg + (unsigned)(s * NH + h) * FB; }
  __device__ __forceinline__ unsigned blk(unsigned pair_off, unsigned iod) const { return al + pair_off + ((((va21 + pair_off) >> 21) ^ nz ^ iod) & 1u) * 8192u; }
};
__device__ __forceinline__ Near near_init(const u8* ws) {
  Near n; const unsigned lo = (unsigned)(size_t)ws;
  n.al = (0u - lo) & 16383u; n.va21 = lo + n.al; n.nz = 0; n.k = 0; return n;
}
__device__ __forceinline__ unsigned xcc_id() { return (unsigned)__builtin_amdgcn_s_getreg(20 | (3 << 11)) & 7u; }
__device__ __forceinline__ void near_wait(gu32* ctrl, Near& n, bool& dead) {
  wait_ge(ctrl, W_XCC, 16u, 0x11u, dead);
  n.k = __builtin_amdgcn_readfirstlane(__hip_atomic_load(ctrl + W_XCC, RLX_AGENT)) & 7u;
  n.nz = __builtin_amdgcn_readfirstlane(__hip_atomic_load(ctrl + W_CFG, RLX_AGENT)) & 1u;
}

#define CSTAMP(wv, slot)
#define PT(id)
#define CTRIES(slot, n)
#define WSTAMP()
template <bool SC1>
__device__ __forceinline__ void dma16(const void* base, unsigned voff, unsigned lds_addr) {
  unsigned keep;
  if (SC1) asm volatile("s_mov_b32 %0, m0\n\ts_mov_b32 m0, %3\n\ts_nop 0\n\tglobal_load_lds_dwordx4 %1, %2 sc1\n\ts_mov_b32 m0, %0" : "=&s"(keep) : "v"(voff), "s"(base), "s"(lds_addr) : "memory");
  else asm volatile("s_mov_b32 %0, m0\n\ts_mov_b32 m0, %3\n\ts_nop 0\n\tglobal_load_lds_dwordx4 %1, %2\n\ts_mov_b32 m0, %0" : "=&s"(keep) : "v"(voff), "s"(base), "s"(lds_addr) : "memory");
}

template <int N> __device__ __forceinline__ void wait_vm() { static_assert(N >= 0 && N < 64, "vmcnt"); asm volatile("s_waitcnt vmcnt(%0)" :: "i"(N) : "memory"); }
template <int V> struct IC { static constexpr int v = V; };
template <int N, int I = 0, class F> __device__ __forceinline__ void static_for(F&& f) { if constexpr (I < N) { f(IC<I>{}); static_for<N, I + 1>(f); } }
__device__ __forceinline__ void slice_dma(const u16* src, int ld, unsigned dst, int lane) {
  const unsigned vo = ((lane >> 2) * ld + 8 * (lane & 3)) * 2;
#pragma unroll
  for (int p = 0; p < 8; ++p) dma16<false>(src + (size_t)16 * p * ld, vo, dst + p * 1024);
}
template <bool PERM>
__device__ __forceinline__ void slice_frags(unsigned base, int lane, v8u (&f)[2][2]) {
  const int gB = lane >> 4, q = (lane & 15) >> 2, p = lane & 3;
#pragma unroll
  for (int cc = 0; cc < 2; ++cc)
#pragma unroll
    for (int rr = 0; rr < 4; ++rr) {
      const int kp = 64 * cc + 32 * (rr >> 1) + 16 * (gB >> 1) + 4 * (gB & 1) + 2 * (rr & 1) + (q & 1) + 8 * (q >> 1);
      const int row = PERM ? ((kp & ~31) + 16 * (kp & 1) + ((kp & 31) >> 1)) : kp;
#pragma unroll
      for (int tb = 0; tb < 2; ++tb) {
        const v2u v = trread(base + row * 64 + tb * 32 + p * 8);
        f[tb][cc][2 * rr] = v.x; f[tb][cc][2 * rr + 1] = v.y;
      }
    }
}

__device__ __forceinline__ void chain_role(const Params& p, LAS u8* L, rsrc_t rs, int b) {
  const int tid = threadIdx.x, lane = tid & 63, w = __builtin_amdgcn_readfirstlane(tid >> 6);
  const int c = (b & 3) * 32 + (b >> 3);
  const int h = c >> 2, j = c & 3;
  const int dbgi = (c >> 4) + 8 * (c & 15); (void)dbgi;
  const int r16 = lane & 15, g = lane >> 4;
  gu32* ctrl = (gu32*)p.ws;
  bool dead = false;
  const unsigned lbase = (unsigned)(size_t)L;
  Near nr = near_init(p.ws);
  if (b == 0 && tid == 0) __hip_atomic_store(ctrl + W_XCC, 16u | xcc_id(), RLX_AGENT);

  v8u W1[3][2][2];
  v8u WO[2][2];
  {
    const unsigned mine = lbase + w * 16384;
    const size_t wcol = (size_t)h * 128 + 32 * j;
    slice_dma(p.Wk + (size_t)128 * w * HD + wcol, HD, mine, lane);
    slice_dma(p.Wv + (size_t)128 * w * HD + wcol, HD, mine + 8192, lane);
    asm volatile("s_waitcnt vmcnt(8)" ::: "memory");
    slice_frags<true>(lbase + w * 16384, lane, W1[1]);
    WAIT_LGKM(); __builtin_amdgcn_sched_barrier(0);
    slice_dma(p.Wq + (size_t)128 * w * HD + wcol, HD, mine, lane);
    asm volatile("s_waitcnt vmcnt(8)" ::: "memory");
    slice_frags<true>(lbase + w * 16384 + 8192, lane, W1[2]);
    WAIT_LGKM(); __builtin_amdgcn_sched_barrier(0);
    DRAIN_VM();
    slice_frags<true>(lbase + w * 16384, lane, W1[0]);
    WAIT_LGKM(); __builtin_amdgcn_sched_barrier(0);
  }
  near_wait(ctrl, nr, dead);
  const unsigned myrel = nr.rel(c);

  LAS float* RED = (LAS float*)L;
  LAS u8* AHF = L + 24576;
  volatile LAS unsigned* FOK = (volatile LAS unsigned*)(L + 26624 + 4096);
  LAS float* INV = (LAS float*)(L + 30976);
  LAS u8* KVL = L + 49152;
  const int sidx = r16 < 8 ? 0x4444 : 0xEEEE;
  const int gg = g & 1, hi = g >> 1, rp = 2 * gg + hi;
  int gone = dead ? 1 : 0;

  auto fetch_kv_issue = [&](int s, int lo, int hi) {
    if ((s & 3) == 0) { bool d = gone != 0; const int blk = s >> 2; wait_flags2(ctrl, W_FKV + blk * 128 + 2 * h, W_FKV + blk * 128 + 64 + 2 * h, 0x20u, d); gone = __builtin_amdgcn_readfirstlane(d ? 1 : 0); }
    CFENCE();
    const unsigned dst = lbase + 49152 + (s & 1) * 16384;
#pragma unroll
    for (int i = lo; i < hi; ++i) dma16<true>(p.ws + nr.frag((i >> 3) ? OFF_VX : OFF_KX, s, h) + (i & 7) * 1024, lane * 16, dst + i * 1024);
  };
  auto kv_ready = [&](int s) {
    if ((s & 3) == 0) { bool d = gone != 0; const int blk = s >> 2; wait_flags2(ctrl, W_FKV + blk * 128 + 2 * h, W_FKV + blk * 128 + 64 + 2 * h, 0x20u, d); gone = __builtin_amdgcn_readfirstlane(d ? 1 : 0); CFENCE(); }
  };
  auto arm_addr = [&](int s, int slot, int q, unsigned& a0, unsigned& a1) {
    a0 = s > 0 ? nr.blk(OFF_PART + (unsigned)(slot == 0 ? 2 : slot - 1) * PART_SZ + (c & (PR - 1)) * 16384u, myrel) + q * 1024 + lane * 16 : 0xFFFFFF00u;
    const unsigned qn = nr.blk(OFF_QKV + (unsigned)((s + ARM_AHEAD) * QH + (h & (QH - 1))) * 16384u, myrel);
    a1 = q == 0 ? qn + j * 1024 + lane * 16
                : lane < 32 ? qn + 4096 + j * 512 + lane * 16
                            : (lane < 48 && s + ARM_AHEAD + 2 < NSEG) ? nr.al + OFF_MEM + (unsigned)(s + ARM_AHEAD + 2) * MEM_ST + (c >> 6) * 16384u + ((lane >> 3) & 1) * 8192u + (c & 63) * 128 + (lane & 7) * 16
                                                                      : 0xFFFFFF00u;
    a1 = s + ARM_AHEAD < NSEG ? a1 : 0xFFFFFF00u;
  };
  if (tid == 0) { FOK[0] = 0u; FOK[1] = 0u; }
  int dA = W_A / 20, dB = W_B / 20, dC = W_C / 20;
  wgbar();

  auto run = [&](auto role) {
    constexpr int ROLE = decltype(role)::v;
    LAS f32x2* redw = (LAS f32x2*)(RED + w * 768) + (rp * 16 + r16) * 2;
    const int R = 32 * j + 4 * w + (r16 >> 2);
    const unsigned rrel = nr.rel(R), part_w = (R & (PR - 1)) * 16384u, part_l = PBF ? h * 128 + rp * 32 + (r16 & 3) * 8 : h * 256 + rp * 64 + (r16 & 3) * 16;
    const unsigned pub_l = ROLE == 0 ? j * 1024 + (8 * w + 2 * g + (r16 & 1) + 16 * (r16 >> 2)) * 16 + (r16 & 2) * 4 : 4096 + (32 * j + r16) * 16 + g * 4;
    unsigned qo, a_part, mo, a_pub;
    float pinf = INFINITY, mi = g < 2 ? -INFINITY : 0.f;
    asm volatile("" : "+v"(pinf), "+v"(mi));
    const f32x4 minit = {mi, mi, mi, mi};
    const unsigned s1 = (r16 & 3) == 0 ? 0x00003F80u : (r16 & 3) == 1 ? 0x3F800000u : 0u;
    const v4u rsel = {(r16 & 4) ? 0u : s1, (r16 & 4) ? s1 : 0u, (r16 & 4) ? 0u : s1, (r16 & 4) ? s1 : 0u};
    int slot = 0, lim = gone ? 0 : RETRY_CAP;
    auto prepare = [&](int s, int sl) {
      qo = nr.blk(OFF_QKV + (unsigned)(s * QH + (h & (QH - 1))) * 16384u, myrel);
      a_part = nr.blk(OFF_PART + (unsigned)sl * PART_SZ + part_w, rrel) + part_l;
      mo = nr.blk(OFF_MEM + (unsigned)s * MEM_ST + (w >> 2) * 16384u, myrel) + (w & 3) * 2048 + lane * 16;
      a_pub = qo + pub_l;
      asm volatile("" : "+v"(a_part), "+v"(mo), "+v"(a_pub));
    };
    prepare(0, 0);
    auto step = [&](auto first, int s) -> bool {
      constexpr bool FIRST = decltype(first)::v != 0;
      const unsigned par = (unsigned)(s / 3) & 1u; (void)par;
      PT(0);
      delay_rt(dA);
      PT(1);
      f32x4 acc[6];
      {
        int tries = 0; bool again;
        do {
          CFENCE();
          const v4u a0 = ld16(rs, mo), a1 = ld16(rs, mo + 1024);
          if (FIRST && tries == 0) slice_dma(p.Wo + (size_t)128 * h * DM + 256 * j + 32 * w, DM, lbase + 98304 + w * 8192, lane);
#pragma unroll
          for (int t = 0; t < 6; ++t) { acc[t] = f32x4{0.f, 0.f, 0.f, 0.f}; asm volatile("" : "+v"(acc[t])); }
          __builtin_amdgcn_sched_barrier(0);
#pragma unroll
          for (int t = 0; t < 6; ++t) acc[t] = smf64(a0, W1[t >> 1][t & 1][0], acc[t], sidx);
          __builtin_amdgcn_sched_barrier(0);
#pragma unroll
          for (int t = 0; t < 6; ++t) acc[t] = smf64(a1, W1[t >> 1][t & 1][1], acc[t], sidx);
          again = (__ballot(max(umax4(a0), umax4(a1)) == SENT) != 0ull) & (tries < lim);
          ++tries;
        } while (__builtin_expect(again, 0));
        lim = tries > lim ? 0 : lim;
        if (w == 0) CTRIES(0, tries - 1);
      }
      CSTAMP(0, 0);
      PT(2);
#pragma unroll
      for (int t = 0; t < 6; ++t) redw[(t >> 1) * 128 + (t & 1)] = fold32(acc[t]);
      PT(3);
      wgbar();
      PT(4);
      CSTAMP(0, 1);
      if (ROLE <= 2) {
        const LAS f32x4* redr = (const LAS f32x4*)RED + w * 64 + lane;
        f32x4 t0 = redr[0], t1 = redr[192];
#pragma unroll
        for (int k = 2; k < 8; k += 2) { t0 += redr[k * 192]; t1 += redr[(k + 1) * 192]; }
        t0 += t1;
        if (ROLE == 0) {
          const unsigned d0 = pk2(t0[0], t0[2]), d1 = pk2(t0[1], t0[3]);
          const unsigned got = __builtin_amdgcn_mov_dpp((r16 & 1) ? d0 : d1, 0xB1, 0xF, 0xF, true);
          st8(rs, a_pub, (r16 & 1) ? v2u{canon(got), canon(d1)} : v2u{canon(d0), canon(got)});
        } else {
          st4(rs, a_pub, canon(pk2(t0[0], t0[1])));
          st4(rs, a_pub + 256, canon(pk2(t0[2], t0[3])));
        }
      }
      CSTAMP(0, 2);
      PT(5);
      if (!FIRST && s == NSEG - 1) return false;
      if (FIRST) {
        if (ROLE >= 4) fetch_kv_issue(0, 4 * (ROLE - 4), 4 * (ROLE - 4) + 4);
        DRAIN_VM();
        slice_frags<false>(lbase + 98304 + w * 8192, lane, WO);
        wgbar();
      }
      acc[0] = f32x4{0.f, 0.f, 0.f, 0.f}; acc[1] = acc[0];
      asm volatile("" : "+v"(acc[0]), "+v"(acc[1]));
      if (ROLE <= 3) {
        v4u KX[2][4], VX[2], F[4]; v2u VM[2];
        const LAS u8* kv = KVL + (s & 1) * 16384 + lane * 16;
#pragma unroll
        for (int i = 0; i < 8; ++i) KX[i >> 2][i & 3] = *(const LAS v4u*)(kv + i * 1024);
        VX[0] = *(const LAS v4u*)(kv + 8192 + 2 * w * 1024); VX[1] = *(const LAS v4u*)(kv + 8192 + (2 * w + 1) * 1024);
        const unsigned fo = qo + lane * 16, vo = qo + 4096 + (32 * w + r16) * 16 + gg * 8;
        __builtin_amdgcn_sched_barrier(0);
        delay_rt((ROLE == 3 ? dB + DB3 : dB));
        PT(6);
        f32x4 sA, sB, sM;
        {
          int tries = 0; bool again;
          do {
            CFENCE();
#pragma unroll
            for (int u = 0; u < 4; ++u) F[u] = ld16(rs, fo + u * 1024);
            VM[0] = ld8(rs, vo); VM[1] = ld8(rs, vo + 256);
            sA = f32x4{0.f, 0.f, 0.f, 0.f}; sB = sA; sM = minit;
#pragma unroll
            for (int u = 0; u < 4; ++u) { sM = mfma32(F[u], F[u], sM); sA = mfma32(KX[0][u], F[u], sA); sB = mfma32(KX[1][u], F[u], sB); }
            const unsigned m = max(max(max(umax4(F[0]), umax4(F[1])), max(umax4(F[2]), umax4(F[3]))), max(max(VM[0].x, VM[0].y), max(VM[1].x, VM[1].y)));
            again = (__ballot(m == SENT) != 0ull) & (tries < lim);
            ++tries;
          } while (__builtin_expect(again, 0));
          lim = tries > lim ? 0 : lim;
          if (w == 0) CTRIES(1, tries - 1);
        }
        if (ROLE == 2) FOK[0] = (unsigned)(s + 1);
        CSTAMP(0, 3);
        PT(7);
        const float sc2 = 0.08838834764831845f * 1.4426950408889634f;
        sA *= sc2; sB *= sc2; sM *= sc2;
        float mx = fmaxf(fmaxf(fmaxf(fmaxf(sA[0], sA[1]), sA[2]), fmaxf(fmaxf(sA[3], sB[0]), sB[1])), fmaxf(fmaxf(fmaxf(sB[2], sB[3]), sM[0]), fmaxf(fmaxf(sM[1], sM[2]), sM[3])));
        { auto r = __builtin_amdgcn_permlane16_swap(f2u(mx), f2u(mx), false, false); mx = __builtin_amdgcn_fmed3f(u2f(r[0]), u2f(r[1]), pinf); }
        { auto r = __builtin_amdgcn_permlane32_swap(f2u(mx), f2u(mx), false, false); mx = __builtin_amdgcn_fmed3f(u2f(r[0]), u2f(r[1]), pinf); }
        sA -= mx; sB -= mx; sM -= mx;
#pragma unroll
        for (int r = 0; r < 4; ++r) { sA[r] = __builtin_amdgcn_exp2f(sA[r]); sB[r] = __builtin_amdgcn_exp2f(sB[r]); sM[r] = __builtin_amdgcn_exp2f(sM[r]); }
        const v4u pb = {pk2(sA[0], sA[1]), pk2(sA[2], sA[3]), pk2(sB[0], sB[1]), pk2(sB[2], sB[3])};
        const v2u pm = {pk2(sM[0], sM[1]), pk2(sM[2], sM[3])};
        f32x4 e0 = mfma32(VX[0], pb, f32x4{0.f, 0.f, 0.f, 0.f}), e1 = mfma32(VX[1], pb, f32x4{0.f, 0.f, 0.f, 0.f});
        e0 = mfma16(VM[0], pm, e0); e1 = mfma16(VM[1], pm, e1);
        if (ROLE == 3) {
          const f32x4 ss = sA + sB + sM;
          INV[lane] = __builtin_amdgcn_rcpf(xadd32(xadd16((ss[0] + ss[1]) + (ss[2] + ss[3]))));
        }
        LAS u8* ah = AHF + (r16 < 8 ? (4 * w + hi) * 128 + r16 * 16 + gg * 8 : 4096 + lane * 8);
        *(LAS v2u*)ah = v2u{pk2(e0[0], e0[1]), pk2(e0[2], e0[3])};
        *(LAS v2u*)(ah + 256) = v2u{pk2(e1[0], e1[1]), pk2(e1[2], e1[3])};
      } else {
        constexpr int Q = ROLE - 4;
        kv_ready(s + 1);
        const u8* kb = p.ws + nr.frag((Q >> 1) ? OFF_VX : OFF_KX, s + 1, h) + (Q & 1) * 4096;
        const unsigned dst = lbase + 49152 + ((s + 1) & 1) * 16384 + Q * 4096;
        unsigned ar0, ar1;
        arm_addr(s, slot, Q, ar0, ar1);
        asm volatile("" : "+v"(ar0), "+v"(ar1));
        __builtin_amdgcn_sched_barrier(0);
        for (unsigned sp = 0; sp < (1u << 20); ++sp)
          if ((unsigned)__builtin_amdgcn_readfirstlane(FOK[0]) == (unsigned)(s + 1)) break;
#pragma unroll
        for (int i = 0; i < 4; ++i) dma16<true>(kb + i * 1024, lane * 16, dst + i * 1024);
        st16(rs, ar0, v4u{SENT, SENT, SENT, SENT});
        if (Q < 2) st16(rs, ar1, v4u{SENT, SENT, SENT, SENT});
      }
      PT(9);
      wgbar();
      PT(10);
      CSTAMP(7, 4);
      {
        const v4u b0 = *(LAS v4u*)(AHF + lane * 16), b1 = *(LAS v4u*)(AHF + 1024 + lane * 16);
        const f32x2 iv = *(LAS f32x2*)(INV + 2 * rp);
        acc[0] = smf64(b0, WO[0][0], acc[0], sidx); acc[1] = smf64(b0, WO[1][0], acc[1], sidx);
        acc[0] = smf64(b1, WO[0][1], acc[0], sidx); acc[1] = smf64(b1, WO[1][1], acc[1], sidx);
        const f32x2 f0 = fold32(acc[0]) * iv, f1 = fold32(acc[1]) * iv;
        const v4u pv = {canon(f2u(f0[0])), canon(f2u(f1[0])), canon(f2u(f0[1])), canon(f2u(f1[1]))};
        if (PBF) st8(rs, a_part, TAGP(v2u{canon(pk2(f0[0], f1[0])), canon(pk2(f0[1], f1[1]))})); else st16(rs, a_part, pv);
      }
      __builtin_amdgcn_sched_barrier(0);
      CSTAMP(7, 5);
      PT(11);
      const int nslot = slot == NSLOT - 1 ? 0 : slot + 1;
      if (ROLE == 7) {
        unsigned po = nr.blk(OFF_PART + (unsigned)slot * PART_SZ + (c & (PR - 1)) * 16384u, myrel) + lane * 16;
        unsigned a_mem = nr.blk(OFF_MEM + (unsigned)(s + 1) * MEM_ST + (c >> 6) * 16384u, myrel) + (c & 63) * 128 + (2 * (r16 >> 2) + g) * 16 + (r16 & 3) * 4;
        a_mem = lane < 32 ? a_mem : 0xFFFFFF00u;
        prepare(s + 1, nslot);
        asm volatile("" : "+v"(a_mem), "+v"(po));
        delay_rt(dC);
        PT(12);
        f32x4 t;
        {
          int tries = 0; bool again;
          const unsigned p8 = po - lane * 8;
          do {
            CFENCE();
            v2u pq[8];
#pragma unroll
            for (int i = 0; i < 8; ++i) pq[i] = ld8(rs, p8 + i * 512);
            t = f32x4{0.f, 0.f, 0.f, 0.f};
#pragma unroll
            for (int i = 0; i < 8; i += 2) t = mfma32(rsel, v4u{pq[i].x, pq[i].y, pq[i + 1].x, pq[i + 1].y}, t);
            unsigned m = 0;
#pragma unroll
            for (int i = 0; i < 8; ++i) m = max(m, max(pq[i].x, pq[i].y));
            again = (__ballot(m == SENT) != 0ull) & (tries < lim);
            ++tries;
          } while (__builtin_expect(again, 0));
          lim = tries > lim ? 0 : lim;
          CTRIES(2, tries - 1);
        }
        CSTAMP(7, 6);
        PT(13);
        st4(rs, a_mem, canon(pk2(t[0], t[1])));
      } else {
        prepare(s + 1, nslot);
        if (ROLE == 2 && j == 0 && lane == 0) __hip_atomic_store(ctrl + W_PROG + LN * h, (unsigned)(s + 1), RLX_AGENT);
        if (ROLE >= 4 || ARM_LATE) DRAIN_VM();
      }
      slot = nslot;
      PT(14);
      wgbar();
      CSTAMP(7, 7);
      return true;
    };
    step(IC<1>{}, 0);
    for (int s = 1; step(IC<0>{}, s); ++s) {}
    if (lim == 0 && lane == 0) __hip_atomic_store(ctrl + W_TMO, 1u, RLX_AGENT);
  };
  switch (w) {
    case 0: case 1: run(IC<0>{}); break;
    case 2: run(IC<2>{}); break;
    case 3: run(IC<3>{}); break;
    case 4: run(IC<4>{}); break;
    case 5: run(IC<5>{}); break;
    case 6: run(IC<6>{}); break;
    default: run(IC<7>{}); break;
  }
}

enum { M_K = 0, M_V = 1, M_O = 2 };

template <int BN, int MODE, int WM = 2>
__device__ __forceinline__ void gemm_tile(LAS u8* L, const u16* A, int lda, const u16* W, int ldw, int nkt, f32x4 (&acc)[8 / WM][BN * WM / 128], const Near* nr = nullptr) {
  constexpr int MT = 8 / WM, NT = BN * WM / 128, NCG = BN / 64, STG = 16384 + BN * 128, NB = BN / 64;
  constexpr int NSTG = LDS_BYTES / STG;
  const int tid = threadIdx.x, lane = tid & 63, w = __builtin_amdgcn_readfirstlane(tid >> 6);
  const int wm = w / (8 / WM), wn = w % (8 / WM), r16 = lane & 15, g = lane >> 4, q = r16 >> 2, pp = lane & 3;
  const int arow = 16 * w + (lane >> 3);
  const unsigned avo0 = (arow * lda + 8 * ((lane & 7) ^ ((arow >> 1) & 7))) * 2;
  const unsigned avo1 = ((arow + 8) * lda + 8 * ((lane & 7) ^ (((arow + 8) >> 1) & 7))) * 2;
  const int kr = 8 * w + (lane >> 3);
  const int krs = MODE == M_O ? ((kr & ~31) + 16 * ((kr >> 2) & 1) + 4 * ((kr >> 3) & 3) + (kr & 3)) : kr;
  const int fsw = ((kr >> 1) & 1) | (((kr >> 3) & 1) << 1);
  const unsigned wvo = (krs * ldw + 16 * (((lane & 7) >> 1) ^ fsw) + 8 * (lane & 1)) * 2;
  const unsigned lb = (unsigned)(size_t)L;
  auto stage = [&](int kt, int buf) {
    const unsigned sb = lb + buf * STG;
    if (MODE == M_O) {
      const u8* ab = (const u8*)A + (unsigned)__builtin_amdgcn_readfirstlane(nr->frag(OFF_AX, lda + (w >> 1), kt >> 1)) + (w & 1) * 4096 + (kt & 1) * 2048;
      dma16<false>(ab, lane * 16, sb + (2 * w) * 1024);
      dma16<false>(ab + 1024, lane * 16, sb + (2 * w + 1) * 1024);
    } else {
      dma16<false>(A + kt * 64, avo0, sb + (2 * w) * 1024);
      dma16<false>(A + kt * 64, avo1, sb + (2 * w + 1) * 1024);
    }
#pragma unroll
    for (int i = 0; i < NB; ++i) dma16<false>(W + (size_t)kt * 64 * ldw + 64 * i, wvo, sb + 16384 + (w * NB + i) * 1024);
  };
  const unsigned aoff = lb + (16 * MT * wm + r16) * 128 + ((g ^ ((r16 >> 1) & 7)) * 16);
  const int fr = ((q >> 1) & 1) | ((g & 1) << 1);
  const int cgw = (wn * NT) >> 2, c0 = (wn * NT) & 3;
  unsigned boff[NT];
#pragma unroll
  for (int n = 0; n < NT; ++n) boff[n] = lb + 16384 + (g * NCG + cgw) * 1024 + q * 128 + (((c0 + n) ^ fr) * 32) + pp * 8;
#pragma unroll
  for (int m = 0; m < MT; ++m)
#pragma unroll
    for (int n = 0; n < NT; ++n) acc[m][n] = f32x4{0.f, 0.f, 0.f, 0.f};

  auto compute = [&](int buf) {
    const unsigned so = buf * STG;
    v4u af[2][MT], bf[2][NT];
#pragma unroll
    for (int ks = 0; ks < 2; ++ks) {
#pragma unroll
      for (int n = 0; n < NT; ++n) {
        const v2u x0 = trread(boff[n] + so + ks * 4 * NCG * 1024), x1 = trread(boff[n] + so + ks * 4 * NCG * 1024 + 512);
        bf[ks][n] = v4u{x0.x, x0.y, x1.x, x1.y};
      }
#pragma unroll
      for (int m = 0; m < MT; ++m)
        af[ks][m] = MODE == M_O ? *(LAS v4u*)(size_t)(lb + so + wm * 8192 + lane * 16 + m * 2048 + ks * 1024) : *(LAS v4u*)(size_t)(((aoff + so) ^ (ks * 64)) + m * 2048);
    }
    __builtin_amdgcn_sched_barrier(0);
#pragma unroll
    for (int ks = 0; ks < 2; ++ks)
#pragma unroll
      for (int m = 0; m < MT; ++m)
#pragma unroll
        for (int n = 0; n < NT; ++n) acc[m][n] = MODE == M_V ? mfma32(af[ks][m], bf[ks][n], acc[m][n]) : mfma32(bf[ks][n], af[ks][m], acc[m][n]);
    WAIT_LGKM();
  };
  wgbar();
#pragma unroll
  for (int i = 0; i < NSTG - 1; ++i) stage(i, i);
  int buf = 0;
  for (int kt = 0; kt < nkt - (NSTG - 1); ++kt) {
    wait_vm<(NSTG - 2) * (2 + NB)>();
    __builtin_amdgcn_s_barrier(); CFENCE();
    stage(kt + NSTG - 1, buf >= 1 ? buf - 1 : NSTG - 1);
    compute(buf);
    buf = buf == NSTG - 1 ? 0 : buf + 1;
  }
  static_for<NSTG - 1>([&](auto ic) {
    wait_vm<(NSTG - 2 - decltype(ic)::v) * (2 + NB)>();
    __builtin_amdgcn_s_barrier(); CFENCE();
    compute(buf);
    buf = buf == NSTG - 1 ? 0 : buf + 1;
  });
}

template <int BN, int MODE, int WM = 2>
__device__ __forceinline__ void proj_task(const Params& p, LAS u8* L, rsrc_t rs, Near& nr, bool& dead, int blk, int mtx, int col0) {
  constexpr int MT = 8 / WM, NT = BN * WM / 128;
  const int tid = threadIdx.x, lane = tid & 63, w = __builtin_amdgcn_readfirstlane(tid >> 6), wm = w / (8 / WM), wn = w % (8 / WM);
  f32x4 acc[MT][NT];
  const u16* W = (mtx == 0 ? p.Wk : mtx == 1 ? p.Wv : p.Wq) + col0;
  gemm_tile<BN, MODE, WM>(L, p.x + (size_t)blk * 128 * DM, DM, W, HD, DM / 64, acc);
  const int cw = col0 + wn * 16 * NT, h = cw >> 7;
  if (BN == 64 && blk == 0) near_wait((gu32*)p.ws, nr, dead);
  const unsigned reg = mtx == 0 ? OFF_KX : mtx == 1 ? OFF_VX : OFF_QX;
  const int s0 = blk * 4 + (MT / 2) * wm;
  if (MODE == M_K) {
#pragma unroll
    for (int m = 0; m < MT; ++m) {
      const unsigned o = nr.frag(reg, s0 + (m >> 1), h) + lane * 16 + ((m & 1) * 4 + ((cw & 127) >> 5)) * 1024;
#pragma unroll
      for (int v = 0; v < NT / 2; ++v)
        st16(rs, o + v * 1024, v4u{pk2(acc[m][2 * v][0], acc[m][2 * v + 1][0]), pk2(acc[m][2 * v][1], acc[m][2 * v + 1][1]),
                                   pk2(acc[m][2 * v][2], acc[m][2 * v + 1][2]), pk2(acc[m][2 * v][3], acc[m][2 * v + 1][3])});
    }
  } else {
#pragma unroll
    for (int sg = 0; sg < MT / 2; ++sg)
#pragma unroll
      for (int n = 0; n < NT; ++n)
        st16(rs, nr.frag(reg, s0 + sg, h) + lane * 16 + (((cw & 127) >> 4) + n) * 1024,
             v4u{pk2(acc[2 * sg][n][0], acc[2 * sg][n][1]), pk2(acc[2 * sg][n][2], acc[2 * sg][n][3]),
                 pk2(acc[2 * sg + 1][n][0], acc[2 * sg + 1][n][1]), pk2(acc[2 * sg + 1][n][2], acc[2 * sg + 1][n][3])});
  }
  DRAIN_VM();
  wgbar();
  gu32* ctrl = (gu32*)p.ws;
  if (tid < BN / 64) {
    const int word = mtx == 2 ? W_FQ + blk * 64 + (col0 >> 6) + tid : W_FKV + blk * 128 + mtx * 64 + (col0 >> 6) + tid;
    __hip_atomic_store(ctrl + word, 1u, RLX_AGENT);
  }
}

__device__ __forceinline__ float sigmoidf_(float v) { return 1.0f / (1.0f + __expf(-v)); }

__device__ __forceinline__ void rg_task(const Params& p, rsrc_t rs, int wk) {
  const int tid = threadIdx.x, lane = tid & 63, w = __builtin_amdgcn_readfirstlane(tid >> 6);
  v4u wr[2], gw[2];
#pragma unroll
  for (int i = 0; i < 2; ++i) { wr[i] = *(const GAS v4u*)(p.wr + i * 512 + lane * 8); gw[i] = *(const GAS v4u*)(p.gw + i * 512 + lane * 8); }
#pragma unroll
  for (int t = 0; t < 2; ++t) {
    const int tok = wk * 16 + 2 * w + t;
    float r = 0.f, gs = 0.f;
#pragma unroll
    for (int i = 0; i < 2; ++i) {
      const v4u xv = *(const GAS v4u*)(p.x + (size_t)tok * DM + i * 512 + lane * 8);
#pragma unroll
      for (int d = 0; d < 4; ++d) {
        r = fmaf(bflo(xv[d]), bflo(wr[i][d]), r); r = fmaf(bfhi(xv[d]), bfhi(wr[i][d]), r);
        gs += bfr(bflo(xv[d]) * bflo(gw[i][d])); gs += bfr(bfhi(xv[d]) * bfhi(gw[i][d]));
      }
    }
    r = bfr(wave_sum(r)); gs = bfr(wave_sum(gs));
    if (lane == 0) { st4(rs, OFF_R + tok * 4, f2u(r)); st4(rs, OFF_G + tok * 4, f2u(bfr(sigmoidf_(gs)))); }
  }
  DRAIN_VM();
  wgbar();
  if (tid == 0) __hip_atomic_fetch_add((gu32*)p.ws + W_RG, 1u, RLX_AGENT);
}

__device__ __forceinline__ unsigned long long top_key(unsigned bits, int idx) {
  const unsigned o = (bits & 0x7FFFFFFFu) > 0x7F800000u ? 0u : ((bits & 0x80000000u) ? ~bits : (bits | 0x80000000u));
  return ((unsigned long long)o << 32) | (unsigned)(0x7FFFFFFF - idx);
}
__device__ __forceinline__ void top_ins(unsigned long long k, unsigned long long& a, unsigned long long& b) {
  const unsigned long long lo = k < a ? k : a;
  a = k < a ? a : k;
  b = lo > b ? lo : b;
}
__device__ __forceinline__ void top2(rsrc_t rs, LAS u8* L, int& i0, int& i1) {
  const int tid = threadIdx.x, lane = tid & 63, w = tid >> 6;
  const v4u rv = ld16(rs, OFF_R + tid * 16);
  unsigned long long a = 0ull, b = 0ull;
#pragma unroll
  for (int d = 0; d < 4; ++d) top_ins(top_key(rv[d], tid * 4 + d), a, b);
#pragma unroll
  for (int o = 1; o < 64; o <<= 1) {
    const unsigned long long c0 = __shfl_xor(a, o), c1 = __shfl_xor(b, o);
    top_ins(c0, a, b); top_ins(c1, a, b);
  }
  LAS unsigned long long* tv = (LAS unsigned long long*)L;
  wgbar();
  if (lane == 0) { tv[2 * w] = a; tv[2 * w + 1] = b; }
  wgbar();
  a = b = 0ull;
#pragma unroll
  for (int k = 0; k < 16; ++k) top_ins(tv[k], a, b);
  wgbar();
  i0 = (0x7FFFFFFF - (int)(unsigned)a) & 2047; i1 = (0x7FFFFFFF - (int)(unsigned)b) & 2047;
}

__device__ __forceinline__ void mlp1_task(const Params& p, LAS u8* L, rsrc_t rs, int t, bool& dead) {
  const int tid = threadIdx.x;
  gu32* ctrl = (gu32*)p.ws;
  wait_ge(ctrl, W_RG, NWORK, 0x40u, dead);
  int i0, i1;
  top2(rs, L, i0, i1);
  if (t == 0 && tid == 0) {
    const float r0 = u2f(ld4(rs, OFF_R + i0 * 4)), r1 = u2f(ld4(rs, OFF_R + i1 * 4));
    st16(rs, W_TOP * 4, v4u{(unsigned)i0, (unsigned)i1, f2u(bfr(sigmoidf_(r0))), f2u(bfr(sigmoidf_(r1)))});
  }
  const int rg = tid >> 3, cq = tid & 7;
  float ag[2][8], au[2][8];
#pragma unroll
  for (int e = 0; e < 8; ++e) { ag[0][e] = ag[1][e] = au[0][e] = au[1][e] = 0.f; }
#pragma unroll 4
  for (int i = 0; i < 16; ++i) {
    const int k = rg + 64 * i;
    const v4u gv = *(const GAS v4u*)(p.wg + (size_t)k * FF + 64 * t + 8 * cq), uv = *(const GAS v4u*)(p.wu + (size_t)k * FF + 64 * t + 8 * cq);
    const float x0 = bf2f(p.x[(size_t)i0 * DM + k]), x1 = bf2f(p.x[(size_t)i1 * DM + k]);
#pragma unroll
    for (int d = 0; d < 4; ++d) {
      ag[0][2 * d] = fmaf(x0, bflo(gv[d]), ag[0][2 * d]); ag[0][2 * d + 1] = fmaf(x0, bfhi(gv[d]), ag[0][2 * d + 1]);
      ag[1][2 * d] = fmaf(x1, bflo(gv[d]), ag[1][2 * d]); ag[1][2 * d + 1] = fmaf(x1, bfhi(gv[d]), ag[1][2 * d + 1]);
      au[0][2 * d] = fmaf(x0, bflo(uv[d]), au[0][2 * d]); au[0][2 * d + 1] = fmaf(x0, bfhi(uv[d]), au[0][2 * d + 1]);
      au[1][2 * d] = fmaf(x1, bflo(uv[d]), au[1][2 * d]); au[1][2 * d + 1] = fmaf(x1, bfhi(uv[d]), au[1][2 * d + 1]);
    }
  }
  LAS float* R = (LAS float*)L;
#pragma unroll
  for (int e = 0; e < 8; ++e) {
    R[(0 * 64 + rg) * 65 + 8 * cq + e] = ag[0][e]; R[(1 * 64 + rg) * 65 + 8 * cq + e] = ag[1][e];
    R[(2 * 64 + rg) * 65 + 8 * cq + e] = au[0][e]; R[(3 * 64 + rg) * 65 + 8 * cq + e] = au[1][e];
  }
  wgbar();
  if (tid < 128) {
    const int tok = tid >> 6, col = tid & 63;
    float gs = 0.f, us = 0.f;
    for (int r = 0; r < 64; ++r) { gs += R[(tok * 64 + r) * 65 + col]; us += R[((2 + tok) * 64 + r) * 65 + col]; }
    gs = bfr(gs); us = bfr(us);
    const float act = bfr(gs * sigmoidf_(gs));
    st4(rs, OFF_HID + (tok * FF + 64 * t + col) * 4, f2u(bfr(act * us)));
  }
  DRAIN_VM();
  wgbar();
  if (tid == 0) __hip_atomic_fetch_add(ctrl + W_M1, 1u, RLX_AGENT);
}

__device__ __forceinline__ void mlp2_task(const Params& p, LAS u8* L, rsrc_t rs, int t, bool& dead) {
  const int tid = threadIdx.x;
  gu32* ctrl = (gu32*)p.ws;
  wait_ge(ctrl, W_M1, 56, 0x41u, dead);
  CFENCE();
  const int kc = t >> 2, cb = t & 3, rg = tid >> 5, cq = tid & 31;
  float a[2][8];
#pragma unroll
  for (int e = 0; e < 8; ++e) a[0][e] = a[1][e] = 0.f;
#pragma unroll 4
  for (int i = 0; i < 16; ++i) {
    const int k = 256 * kc + rg + 16 * i;
    const v4u dv = *(const GAS v4u*)(p.wd + (size_t)k * DM + 256 * cb + 8 * cq);
    const float h0 = u2f(ld4(rs, OFF_HID + k * 4)), h1 = u2f(ld4(rs, OFF_HID + (FF + k) * 4));
#pragma unroll
    for (int d = 0; d < 4; ++d) {
      a[0][2 * d] = fmaf(h0, bflo(dv[d]), a[0][2 * d]); a[0][2 * d + 1] = fmaf(h0, bfhi(dv[d]), a[0][2 * d + 1]);
      a[1][2 * d] = fmaf(h1, bflo(dv[d]), a[1][2 * d]); a[1][2 * d + 1] = fmaf(h1, bfhi(dv[d]), a[1][2 * d + 1]);
    }
  }
  LAS float* R = (LAS float*)L;
  wgbar();
#pragma unroll
  for (int e = 0; e < 8; ++e) { R[(0 * 16 + rg) * 257 + 8 * cq + e] = a[0][e]; R[(1 * 16 + rg) * 257 + 8 * cq + e] = a[1][e]; }
  wgbar();
  {
    const int tok = tid >> 8, col = tid & 255;
    float sacc = 0.f;
#pragma unroll
    for (int r = 0; r < 16; ++r) sacc += R[(tok * 16 + r) * 257 + col];
    st4(rs, OFF_YP + ((kc * 2 + tok) * DM + 256 * cb + col) * 4, f2u(sacc));
  }
  DRAIN_VM();
  wgbar();
  if (tid == 0) __hip_atomic_store(ctrl + W_M2 + t * LN, 1u, RLX_AGENT);
}

__device__ __forceinline__ void seg_softmax(const v4u (&F)[4], const v4u (&KX)[2][4], const v4u (&QX)[4], int tq, int g, v4u& pb, v2u& pm) {
  const float scale = 0.08838834764831845f, l2e = 1.4426950408889634f, neg = bfr(-1000000000.0f);
  f32x4 sA = {0.f, 0.f, 0.f, 0.f}, sB = sA, sM = sA;
#pragma unroll
  for (int u = 0; u < 4; ++u) { sM = mfma32(F[u], QX[u], sM); sA = mfma32(KX[0][u], QX[u], sA); sB = mfma32(KX[1][u], QX[u], sB); }
#pragma unroll
  for (int r = 0; r < 4; ++r) {
    sA[r] = 4 * g + r > tq ? neg : bfr(bfr(sA[r]) * scale);
    sB[r] = 16 + 4 * g + r > tq ? neg : bfr(bfr(sB[r]) * scale);
    sM[r] = g < 2 ? -INFINITY : bfr(bfr(sM[r]) * scale);
  }
  float mx = fmaxf(fmaxf(fmaxf(sA[0], sA[1]), fmaxf(sA[2], sA[3])), fmaxf(fmaxf(sB[0], sB[1]), fmaxf(sB[2], sB[3])));
  mx = fmaxf(mx, fmaxf(fmaxf(sM[0], sM[1]), fmaxf(sM[2], sM[3])));
  mx = xmax32(xmax16(mx));
  float sum = 0.f;
#pragma unroll
  for (int r = 0; r < 4; ++r) {
    sA[r] = __builtin_amdgcn_exp2f((sA[r] - mx) * l2e); sB[r] = __builtin_amdgcn_exp2f((sB[r] - mx) * l2e); sM[r] = __builtin_amdgcn_exp2f((sM[r] - mx) * l2e);
    sum += sA[r] + sB[r] + sM[r];
  }
  const float inv = 1.0f / xadd32(xadd16(sum));
  pb = v4u{pk2(sA[0] * inv, sA[1] * inv), pk2(sA[2] * inv, sA[3] * inv), pk2(sB[0] * inv, sB[1] * inv), pk2(sB[2] * inv, sB[3] * inv)};
  pm = v2u{pk2(sM[0] * inv, sM[1] * inv), pk2(sM[2] * inv, sM[3] * inv)};
}

__device__ __forceinline__ void attn_task(const Params& p, rsrc_t rs, const Near& nr, int s, int h, bool& dead) {
  const int lane = threadIdx.x & 63, r16 = lane & 15, g = lane >> 4, gg = g & 1;
  gu32* ctrl = (gu32*)p.ws;
  const int blk = s >> 2;
  wait_flags2(ctrl, W_FKV + blk * 128 + 2 * h, W_FKV + blk * 128 + 64 + 2 * h, 0x50u, dead);
  wait_flags2(ctrl, W_FQ + blk * 64 + 2 * h, W_FQ + blk * 64 + 2 * h, 0x51u, dead);
  CFENCE();
  const unsigned fk = nr.frag(OFF_KX, s, h) + lane * 16, fq = nr.frag(OFF_QX, s, h) + lane * 16, fv = nr.frag(OFF_VX, s, h) + lane * 16;
  v4u KX[2][4], QX[2][4], VX[8], F[4]; v2u VM[8];
#pragma unroll
  for (int i = 0; i < 8; ++i) { KX[i >> 2][i & 3] = ld16(rs, fk + i * 1024); QX[i >> 2][i & 3] = ld16(rs, fq + i * 1024); VX[i] = ld16(rs, fv + i * 1024); }
  wait_ge(ctrl, W_PROG + LN * h, (unsigned)(s + 1), 0x52u, dead);
  const unsigned qo = nr.blk(OFF_QKV + (unsigned)(s * QH + (h & (QH - 1))) * 16384u, nr.rel(h * 4));
  for (unsigned sp = 0;;) {
    CFENCE();
    unsigned m = 0;
#pragma unroll
    for (int u = 0; u < 4; ++u) { F[u] = ld16(rs, qo + u * 1024 + lane * 16); m = max(m, umax4(F[u])); }
#pragma unroll
    for (int e = 0; e < 8; ++e) { VM[e] = ld8(rs, qo + 4096 + (16 * e + r16) * 16 + gg * 8); m = max(m, max(VM[e].x, VM[e].y)); }
    if (__all(m != SENT) || dead) break;
    if (spin_fail(sp, ctrl, 0x53u)) { dead = true; break; }
  }
#pragma unroll
  for (int qt = 0; qt < 2; ++qt) {
    v4u pb; v2u pm;
    seg_softmax(F, KX, QX[qt], 16 * qt + r16, g, pb, pm);
    const unsigned ao = nr.frag(OFF_AX, s, h) + lane * 16 + qt * 4096;
#pragma unroll
    for (int v = 0; v < 4; ++v) {
      f32x4 o0 = mfma32(VX[2 * v], pb, f32x4{0.f, 0.f, 0.f, 0.f}), o1 = mfma32(VX[2 * v + 1], pb, f32x4{0.f, 0.f, 0.f, 0.f});
      o0 = mfma16(VM[2 * v], pm, o0); o1 = mfma16(VM[2 * v + 1], pm, o1);
      st16(rs, ao + v * 1024, v4u{pk2(o0[0], o0[1]), pk2(o0[2], o0[3]), pk2(o1[0], o1[1]), pk2(o1[2], o1[3])});
    }
  }
  DRAIN_VM();
}

__device__ __forceinline__ void mix_store(const Params& p, rsrc_t rs, const v4u& top, int tok, int col, f32x4 a) {
  const float gt = u2f(ld4(rs, OFF_G + tok * 4)), og = bfr(1.0f - gt);
  const int sel = tok == (int)top.x ? 0 : tok == (int)top.y ? 1 : -1;
  const v2u xv = *(const GAS v2u*)(p.x + (size_t)tok * DM + col);
  float bv[4] = {bflo(xv.x), bfhi(xv.x), bflo(xv.y), bfhi(xv.y)};
  if (sel >= 0) {
    f32x4 y = {0.f, 0.f, 0.f, 0.f};
    for (int kc = 0; kc < 14; ++kc) y += __builtin_bit_cast(f32x4, ld16(rs, OFF_YP + ((kc * 2 + sel) * DM + col) * 4));
    const float rsel = u2f(sel == 0 ? top.z : top.w);
#pragma unroll
    for (int r = 0; r < 4; ++r) bv[r] = bfr(bv[r] + bfr(bfr(y[r]) * rsel));
  }
  float o[4];
#pragma unroll
  for (int r = 0; r < 4; ++r) o[r] = bfr(gt * bfr(a[r])) + bfr(og * bv[r]);
  *(GAS v2u*)(p.out + (size_t)tok * DM + col) = v2u{pk2(o[0], o[1]), pk2(o[2], o[3])};
}

__device__ __forceinline__ void oproj_task(const Params& p, LAS u8* L, rsrc_t rs, const Near& nr, int blk, int ct, bool& dead) {
  const int tid = threadIdx.x, lane = tid & 63, w = __builtin_amdgcn_readfirstlane(tid >> 6), wm = w >> 2, wn = w & 3, r16 = lane & 15, g = lane >> 4;
  gu32* ctrl = (gu32*)p.ws;
  if (w == 0) {
    wait_lines(ctrl, W_M2, LN, 56, 1u, lane, 0x60u, dead);
    wait_lines(ctrl, W_T2 + 8 * blk * LN, LN, 8, 1u, lane, 0x61u, dead);
    __builtin_amdgcn_fence(__ATOMIC_ACQUIRE, "agent");
    DRAIN_VM();
  }
  f32x4 acc[4][2];
  gemm_tile<128, M_O>(L, (const u16*)p.ws, blk * 4, p.Wo + ct * 128, DM, HD / 64, acc, &nr);
  const v4u top = ld16(rs, W_TOP * 4);
#pragma unroll
  for (int m = 0; m < 4; ++m)
#pragma unroll
    for (int n = 0; n < 2; ++n) mix_store(p, rs, top, blk * 128 + 64 * wm + 16 * m + r16, ct * 128 + 32 * wn + 16 * n + 4 * g, acc[m][n]);
}

__device__ __forceinline__ void stream_role(const Params& p, LAS u8* L, rsrc_t rs, const Near& nr, int set, int q, bool& dead) {
  int wst = 100; const int wk = set * 32 + q; (void)wst; (void)wk;
  const int tid = threadIdx.x, lane = tid & 63, w = __builtin_amdgcn_readfirstlane(tid >> 6), r16 = lane & 15, g = lane >> 4, gg = g & 1;
  const int ks = q >> 3, ct = q & 7, np = w & 3, kh = w >> 2;
  gu32* ctrl = (gu32*)p.ws;
  const unsigned lb = (unsigned)(size_t)L;
  v4u Wf[16][2];
  LAS u8* Wl = L + 65536 + w * 8192 + lane * 16;
  {
    const int kr = 8 * w + (lane >> 3);
    const int krs = (kr & ~31) + 16 * ((kr >> 2) & 1) + 4 * ((kr >> 3) & 3) + (kr & 3);
    const int fsw = ((kr >> 1) & 1) | (((kr >> 3) & 1) << 1);
    const unsigned wvo = (krs * DM + 16 * (((lane & 7) >> 1) ^ fsw) + 8 * (lane & 1)) * 2;
    const int qq = r16 >> 2, fr = ((qq >> 1) & 1) | ((g & 1) << 1);
    const unsigned boff = lb + kh * 65536 + (g * 2 + (np >> 1)) * 1024 + qq * 128 + (lane & 3) * 8;
    const unsigned b0 = boff + (((2 * (np & 1)) ^ fr) * 32), b1 = boff + (((2 * (np & 1) + 1) ^ fr) * 32);
    static_for<2>([&](auto rc) {
      constexpr int RD = decltype(rc)::v;
      wgbar();
#pragma unroll
      for (int kt = 0; kt < 8; ++kt) {
        const u16* wb = p.Wo + (size_t)(1024 * ks + 512 * (kt >> 2) + 64 * (4 * RD + (kt & 3))) * DM + 128 * ct;
        dma16<false>(wb, wvo, lb + kt * 16384 + (w * 2) * 1024);
        dma16<false>(wb + 64, wvo, lb + kt * 16384 + (w * 2 + 1) * 1024);
      }
      DRAIN_VM();
      wgbar();
#pragma unroll
      for (int i = 0; i < 8; ++i) {
        const unsigned o = (i >> 1) * 16384 + (i & 1) * 8192;
        const v2u x0 = trread(b0 + o), x1 = trread(b0 + o + 512), y0 = trread(b1 + o), y1 = trread(b1 + o + 512);
        Wf[8 * RD + i][0] = v4u{x0.x, x0.y, x1.x, x1.y}; Wf[8 * RD + i][1] = v4u{y0.x, y0.y, y1.x, y1.y};
      }
    });
  }
  wgbar();
#pragma unroll
  for (int i = 0; i < 8; ++i) *(LAS v4u*)(Wl + i * 1024) = Wf[12 + (i >> 1)][i & 1];
  wait_lines(ctrl, W_M2, LN, 56, 1u, lane, 0x70u, dead);
  {
    const int fb = NBULK + (lane & 7), kind = (lane >> 3) % 3, hf = lane >= 24 ? 1 : 0;
    gu32* f = ctrl + (kind == 2 ? W_FQ + fb * 64 : W_FKV + fb * 128 + kind * 64) + 2 * q + hf;
    for (unsigned sp = 0; !dead;) {
      if (__all(__hip_atomic_load(f, RLX_AGENT) != 0u)) break;
      __builtin_amdgcn_s_sleep(8);
      if (spin_fail(sp, ctrl, 0x73u)) dead = true;
    }
  }
  CFENCE();
  const v4u top = ld16(rs, W_TOP * 4);

  auto run = [&](auto role) {
    constexpr bool ATT = decltype(role)::v == 0;
    const int qt = w & 1, eh = w >> 1;
    v4u KX[2][4], QX[4], VX[4];
    auto prefetch = [&](int s) {
      const unsigned fk = nr.frag(OFF_KX, s, q) + lane * 16, fq = nr.frag(OFF_QX, s, q) + lane * 16, fv = nr.frag(OFF_VX, s, q) + lane * 16;
#pragma unroll
      for (int i = 0; i < 8; ++i) KX[i >> 2][i & 3] = ld16(rs, fk + i * 1024);
#pragma unroll
      for (int i = 0; i < 4; ++i) { QX[i] = ld16(rs, fq + qt * 4096 + i * 1024); VX[i] = ld16(rs, fv + (4 * eh + i) * 1024); }
    };
    auto finish = [&](int s) {
      const int f = 4 * ks + (w - 4);
      wait_lines(ctrl, W_SP + (set * 32 + ct) * LN, 8 * LN, 4, (unsigned)(s + 1), lane, 0x72u, dead);
      CFENCE();
      f32x4 sum = {0.f, 0.f, 0.f, 0.f};
#pragma unroll
      for (int k = 0; k < 8; ++k) sum += __builtin_bit_cast(f32x4, ld16(rs, nr.frag(OFF_PS, 0, (((s & 3) * 8 + ct) * 8 + k) * 2 + (f >> 3)) + (f & 7) * 1024 + lane * 16));
      mix_store(p, rs, top, s * 32 + 16 * (f & 1) + r16, ct * 128 + 16 * (f >> 1) + 4 * g, sum);
    };
    unsigned prog = 0;
    if (ATT) prefetch(NBULK * 4 + set);
    for (int s = NBULK * 4 + set; s < NSEG; s += 2) {
      const int si = s - NBULK * 4;
      if (ATT) {
        const unsigned need = (unsigned)(s + 1 < NSEG - 1 ? s + 1 : NSEG - 1);
        if (prog < need) {
          for (unsigned sp = 0; !dead;) {
            prog = __builtin_amdgcn_readfirstlane(__hip_atomic_load(ctrl + W_PROG + LN * q, RLX_AGENT));
            if (prog >= need) break;
            __builtin_amdgcn_s_sleep(2);
            if (spin_fail(sp, ctrl, 0x52u)) dead = true;
          }
        }
        const unsigned qo = nr.blk(OFF_QKV + (unsigned)(s * QH + (q & (QH - 1))) * 16384u, nr.rel(q * 4));
        v4u F[4]; v2u VM[4];
        for (unsigned sp = 0;;) {
          CFENCE();
          unsigned m = 0;
#pragma unroll
          for (int u = 0; u < 4; ++u) { F[u] = ld16(rs, qo + u * 1024 + lane * 16); m = max(m, umax4(F[u])); }
#pragma unroll
          for (int e = 0; e < 4; ++e) { VM[e] = ld8(rs, qo + 4096 + (16 * (4 * eh + e) + r16) * 16 + gg * 8); m = max(m, max(VM[e].x, VM[e].y)); }
          if (__all(m != SENT) || dead) break;
          if (spin_fail(sp, ctrl, 0x53u)) { dead = true; break; }
        }
        v4u pb; v2u pm;
        seg_softmax(F, KX, QX, 16 * qt + r16, g, pb, pm);
        const unsigned ao = nr.frag(OFF_AX, s, q) + qt * 4096 + eh * 2048 + lane * 16;
#pragma unroll
        for (int v = 0; v < 2; ++v) {
          f32x4 o0 = mfma32(VX[2 * v], pb, f32x4{0.f, 0.f, 0.f, 0.f}), o1 = mfma32(VX[2 * v + 1], pb, f32x4{0.f, 0.f, 0.f, 0.f});
          o0 = mfma16(VM[2 * v], pm, o0); o1 = mfma16(VM[2 * v + 1], pm, o1);
          st16(rs, ao + v * 1024, v4u{pk2(o0[0], o0[1]), pk2(o0[2], o0[3]), pk2(o1[0], o1[1]), pk2(o1[2], o1[3])});
        }
        DRAIN_VM();
      } else if (si >= 2) {
        finish(s - 2);
      }
      wgbar();
      WSTAMP();
      if (tid == 0) __hip_atomic_store(ctrl + W_SA + (set * 32 + q) * LN, (unsigned)(s + 1), RLX_AGENT);
      if (w == 4) wait_lines(ctrl, W_SA + (set * 32 + ks * 8) * LN, LN, 8, (unsigned)(s + 1), lane, 0x71u, dead);
      wgbar();
      WSTAMP();
      {
        const unsigned ao = nr.frag(OFF_AX, s, 8 * ks + w) + lane * 16;
        v4u t[8];
#pragma unroll
        for (int i = 0; i < 8; ++i) t[i] = ld16(rs, ao + i * 1024);
#pragma unroll
        for (int i = 0; i < 8; ++i) *(LAS v4u*)(L + w * 8192 + i * 1024 + lane * 16) = t[i];
      }
      if (ATT && s + 2 < NSEG) prefetch(s + 2);
      wgbar();
      f32x4 a[2][2];
#pragma unroll
      for (int i = 0; i < 4; ++i) a[i >> 1][i & 1] = f32x4{0.f, 0.f, 0.f, 0.f};
#pragma unroll
      for (int k = 0; k < 16; ++k) {
        const LAS u8* fp = L + kh * 32768 + (k >> 2) * 8192 + (k & 3) * 1024 + lane * 16;
        const v4u x0 = *(const LAS v4u*)fp, x1 = *(const LAS v4u*)(fp + 4096);
#pragma unroll
        for (int n = 0; n < 2; ++n) {
          const v4u wf = k < 12 ? Wf[k][n] : *(const LAS v4u*)(Wl + ((k - 12) * 2 + n) * 1024);
          a[n][0] = mfma32(wf, x0, a[n][0]); a[n][1] = mfma32(wf, x1, a[n][1]);
        }
      }
      WSTAMP();
      const unsigned pb = nr.frag(OFF_PS, 0, (((s & 3) * 8 + ct) * 8 + 2 * ks + kh) * 2 + (np >> 1)) + 4 * (np & 1) * 1024 + lane * 16;
#pragma unroll
      for (int i = 0; i < 4; ++i) st16(rs, pb + i * 1024, __builtin_bit_cast(v4u, a[i >> 1][i & 1]));
      DRAIN_VM();
      wgbar();
      if (tid == 0) __hip_atomic_store(ctrl + W_SP + (set * 32 + q) * LN, (unsigned)(s + 1), RLX_AGENT);
      WSTAMP();
    }
    if (!ATT) finish(NSEG - 2 + set);
  };
  if (w < 4) run(IC<0>{}); else run(IC<1>{});
}

__device__ __forceinline__ void worker_role(const Params& p, LAS u8* L, rsrc_t rs, int b) {
  const int tid = threadIdx.x, lane = tid & 63, w = __builtin_amdgcn_readfirstlane(tid >> 6);
  const int x8 = 2 * (b & 3) + (b >> 7), r = (b >> 3) & 15, wk = r * 8 + x8;
  gu32* ctrl = (gu32*)p.ws;
  bool dead = false;
  int wst = 0; (void)wst;
  Near nr = near_init(p.ws);
  WSTAMP();
  {
    const int cg = x8 * 16 + r;
    for (int blk = 0; blk < 2; ++blk) {
      if (cg < 64) proj_task<64, M_K, 4>(p, L, rs, nr, dead, blk, 0, cg * 64); else proj_task<64, M_V, 4>(p, L, rs, nr, dead, blk, 1, (cg - 64) * 64);
      WSTAMP();
      if (blk == 0) WSTAMP();
    }
  }
  rg_task(p, rs, wk);
  WSTAMP();
  for (int rnd = 0; rnd < 6; ++rnd) {
    const int ct = x8 * 4 + (r & 3), bq = r >> 2;
    if (rnd < 4) {
      const int blk = 2 + rnd * 4 + bq;
      if (blk < 16) { if (ct < 16) proj_task<256, M_K>(p, L, rs, nr, dead, blk, 0, ct * 256); else proj_task<256, M_V>(p, L, rs, nr, dead, blk, 1, (ct - 16) * 256); }
      else { const int t = (blk - 16) * 32 + ct; if (t < 56) mlp1_task(p, L, rs, t, dead); }
    } else {
      const int qct = x8 * 2 + (r & 1), blk = (rnd - 4) * 8 + (r >> 1);
      proj_task<256, M_K>(p, L, rs, nr, dead, blk, 2, qct * 256);
    }
    WSTAMP();
  }
  if (wk < 56) mlp2_task(p, L, rs, wk, dead);
  WSTAMP();
  if (r < 8) {
    const int blk = x8, m = r;
    for (int i = 0; i < 2; ++i) { const int t = (i * 8 + m) * 8 + w; attn_task(p, rs, nr, blk * 4 + (t >> 5), t & 31, dead); WSTAMP(); }
    wgbar();
    if (tid == 0) __hip_atomic_store(ctrl + W_T2 + (8 * blk + m) * LN, 1u, RLX_AGENT);
    oproj_task(p, L, rs, nr, blk, m, dead);
    WSTAMP();
  } else {
    const int sid = x8 * 8 + (r - 8);
    stream_role(p, L, rs, nr, sid >> 5, sid & 31, dead);
    WSTAMP();
  }
}

__global__ void __launch_bounds__(512) rmt_init_kernel(u8* ws, const u16* mem0) {
  const int tid = threadIdx.x;
  if (blockIdx.x >= 4) {
    const Near n = near_init(ws);
    arm_share(__builtin_amdgcn_make_buffer_rsrc(ws, 0, WS_END, 0x00020000), n.al, blockIdx.x - 4, tid);
    return;
  }
  if (blockIdx.x >= 2) {
    const Near n = near_init(ws);
    const int i = (blockIdx.x - 2) * 512 + tid, cb = i >> 3, row = i & 7;
    const u16* m0 = mem0 + row * DM + (cb >> 2) * 32 + (cb & 3) * 4;
    const v2u lo = *(const GAS v2u*)m0, hi = *(const GAS v2u*)(m0 + 16);
    const v4u v = {canon(__builtin_amdgcn_perm(hi.x, lo.x, 0x05040100u)), canon(__builtin_amdgcn_perm(hi.x, lo.x, 0x07060302u)),
                   canon(__builtin_amdgcn_perm(hi.y, lo.y, 0x05040100u)), canon(__builtin_amdgcn_perm(hi.y, lo.y, 0x07060302u))};
    u8* d = ws + n.al + OFF_MEM + (cb >> 6) * 16384 + (cb & 63) * 128 + row * 16;
    *(GAS v4u*)d = v; *(GAS v4u*)(d + 8192) = v;
    return;
  }
  if (blockIdx.x == 0) {
    for (int i = tid; i < (int)(CTRL_BYTES / 16); i += 512)
      if (i != W_CFG / 4) ((GAS v4u*)ws)[i] = v4u{0u, 0u, 0u, 0u};
  } else if (tid == 0) {
    const Near n = near_init(ws);
    gu32* a0 = (gu32*)(ws + n.al + OFF_CAL); gu32* a1 = a0 + 2048;
    unsigned long long b0 = ~0ull, b1 = ~0ull;
    for (int i = 0; i < 3; ++i) {
      const unsigned long long t0 = __builtin_amdgcn_s_memtime();
      __hip_atomic_store(a0, 0u, RLX_AGENT); DRAIN_VM();
      const unsigned long long t1 = __builtin_amdgcn_s_memtime();
      __hip_atomic_store(a1, 0u, RLX_AGENT); DRAIN_VM();
      const unsigned long long t2 = __builtin_amdgcn_s_memtime();
      b0 = t1 - t0 < b0 ? t1 - t0 : b0; b1 = t2 - t1 < b1 ? t2 - t1 : b1;
    }
    const unsigned nz = ((b1 < b0 ? 1u : 0u) ^ ((n.va21 + OFF_CAL) >> 21) ^ (xcc_id() >> 2)) & 1u;
    ((GAS v4u*)ws)[W_CFG / 4] = v4u{nz, 0u, 0u, 0u};
  }
}

__global__ void __launch_bounds__(NTHR, 2) rmt_mod_kernel(Params p) {
  extern __shared__ __attribute__((aligned(16))) u8 smem[];
  LAS u8* L = (LAS u8*)smem;
  const rsrc_t rs = __builtin_amdgcn_make_buffer_rsrc(p.ws, 0, WS_END, 0x00020000);
  const int b = blockIdx.x;
  if ((b & 4) == 0) chain_role(p, L, rs, b); else worker_role(p, L, rs, b);
}

}

extern "C" void kernel_launch(void* const* d_in, const int* in_sizes, int n_in, void* d_out, int out_size, void* d_ws, size_t ws_size,
                              hipStream_t stream) {
  (void)in_sizes; (void)n_in; (void)out_size; (void)ws_size;
  Params p;
  p.x = (const u16*)d_in[0]; p.mem0 = (const u16*)d_in[1]; p.Wq = (const u16*)d_in[2]; p.Wk = (const u16*)d_in[3];
  p.Wv = (const u16*)d_in[4]; p.Wo = (const u16*)d_in[5]; p.wr = (const u16*)d_in[8]; p.wg = (const u16*)d_in[9];
  p.wu = (const u16*)d_in[10]; p.wd = (const u16*)d_in[11]; p.gw = (const u16*)d_in[12];
  p.out = (u16*)d_out; p.ws = (u8*)d_ws;
  (void)hipFuncSetAttribute((const void*)rmt_mod_kernel, hipFuncAttributeMaxDynamicSharedMemorySize, LDS_BYTES);
  rmt_init_kernel<<<4 + NCHAIN, 512, 0, stream>>>(p.ws, p.mem0);
  rmt_mod_kernel<<<NWG, NTHR, LDS_BYTES, stream>>>(p);
}
```

```cpp
#include <hip/hip_runtime.h>
#include <hip/hip_bf16.h>
#include <stdint.h>

#define GAS __attribute__((address_space(1)))
#define LAS __attribute__((address_space(3)))
typedef unsigned v4u __attribute__((ext_vector_type(4)));
typedef unsigned v2u __attribute__((ext_vector_type(2)));
typedef unsigned v8u __attribute__((ext_vector_type(8)));
typedef float f32x4 __attribute__((ext_vector_type(4)));
typedef __bf16 bfx8 __attribute__((ext_vector_type(8)));
typedef __bf16 bfx16 __attribute__((ext_vector_type(16)));
typedef short s16x4 __attribute__((ext_vector_type(4)));
typedef GAS unsigned gu32;
typedef unsigned short u16;
typedef unsigned char u8;
#define RLX_AGENT __ATOMIC_RELAXED, __HIP_MEMORY_SCOPE_AGENT

#define PBF 1
#define SPLIT_XCD 2
namespace {

constexpr int SEQ = 2048, DM = 1024, NH = 32, HD = 4096, NSEG = 64, FF = 3584;
constexpr int NCHAIN = 128, NWORK = 128, NWG = 256, NTHR = 512;
constexpr unsigned SENT = 0xFFFFFFFFu;
constexpr int AUX_SC1 = 16;

constexpr unsigned CTRL_BYTES = 73728;
constexpr int LN = 32;
constexpr int W_TMO = 0, W_RG = 1 * LN, W_M1 = 2 * LN, W_TOP = 3 * LN  , W_CFG = 4 * LN  ,
              W_XCC = 5 * LN, W_PROG = 8 * LN  , W_FKV = 64 * LN  , W_FQ = 128 * LN  ,
              W_ARM = 160 * LN  , W_M2 = 288 * LN  , W_T2 = 344 * LN  ,
              W_SA = 408 * LN  , W_SP = 472 * LN  ;
constexpr unsigned OFF_MEM = 98304;
constexpr unsigned MEM_ST = 32768;
constexpr unsigned OFF_QKV = OFF_MEM + NSEG * MEM_ST;
constexpr unsigned QH = 32, PR = 128;
constexpr unsigned OFF_PART = OFF_QKV + NSEG * QH * 16384;
constexpr unsigned PART_SZ = PR * 16384;
#define NSLOT 3
constexpr unsigned OFF_ARM_END = OFF_PART + NSLOT * PART_SZ;
constexpr unsigned OFF_CAL = OFF_ARM_END;
constexpr unsigned OFF_KX = OFF_CAL + 16384 + 16384;
#define WFAR 1
constexpr unsigned FB = WFAR ? 16384 : 8192;
constexpr unsigned OFF_VX = OFF_KX + NSEG * NH * FB;
constexpr unsigned OFF_QX = OFF_VX + NSEG * NH * FB;
constexpr unsigned OFF_AX = OFF_QX + NSEG * NH * FB;
constexpr unsigned OFF_R = OFF_AX + NSEG * NH * FB + 512 * FB + 16384;
constexpr unsigned OFF_G = OFF_R + SEQ * 4;
constexpr unsigned OFF_HID = OFF_G + SEQ * 4;
constexpr unsigned OFF_YP = OFF_HID + 2 * FF * 4;
constexpr unsigned OFF_PS = OFF_AX + NSEG * NH * FB;
constexpr unsigned WS_END = OFF_YP + 14 * 2 * DM * 4;
constexpr int NBULK = 8;
constexpr int LDS_BYTES = 160 * 1024;
constexpr unsigned SPIN_CAP = 1u << 18;

#define TAGP(...) (__VA_ARGS__)
#define BG_DELAY 16
#define ARM_LATE 0
#define TLB_WARM 16384
#define W_A 360
#define W_B 80
#define W_C 260

struct Params {
  const u16 *x, *mem0, *Wq, *Wk, *Wv, *Wo, *wr, *wg, *wu, *wd, *gw;
  u16* out;
  u8* ws;
};

__device__ __forceinline__ float u2f(unsigned u) { return __builtin_bit_cast(float, u); }
__device__ __forceinline__ unsigned f2u(float f) { return __builtin_bit_cast(unsigned, f); }
__device__ __forceinline__ float bf2f(unsigned b) { return u2f(b << 16); }
__device__ __forceinline__ float bflo(unsigned d) { return u2f(d << 16); }
__device__ __forceinline__ float bfhi(unsigned d) { return u2f(d & 0xFFFF0000u); }
__device__ __forceinline__ unsigned pk2(float lo, float hi) {
  typedef __bf16 bfx2 __attribute__((ext_vector_type(2)));
  bfx2 v; v.x = (__bf16)lo; v.y = (__bf16)hi;
  return __builtin_bit_cast(unsigned, v);
}
__device__ __forceinline__ float bfr(float f) { return (float)(__bf16)f; }
__device__ __forceinline__ unsigned canon(unsigned d) { return min(d, 0xFFFFFFFEu); }
__device__ __forceinline__ f32x4 mfma32(v4u a, v4u b, f32x4 c) {
  return __builtin_amdgcn_mfma_f32_16x16x32_bf16(__builtin_bit_cast(bfx8, a), __builtin_bit_cast(bfx8, b), c, 0, 0, 0);
}
__device__ __forceinline__ f32x4 mfma16(v2u a, v2u b, f32x4 c) { return mfma32(v4u{a.x, a.y, 0u, 0u}, v4u{b.x, b.y, 0u, 0u}, c); }
__device__ __forceinline__ f32x4 smf64(v4u a, v8u b, f32x4 c, int idx) {
  return __builtin_amdgcn_smfmac_f32_16x16x64_bf16(__builtin_bit_cast(bfx8, a), __builtin_bit_cast(bfx16, b), c, idx, 0, 0);
}
__device__ __forceinline__ v2u trread(unsigned lds_addr) {
  return __builtin_bit_cast(v2u, __builtin_amdgcn_ds_read_tr16_b64_v4i16((LAS s16x4*)(size_t)lds_addr));
}
typedef float f32x2 __attribute__((ext_vector_type(2)));
__device__ __forceinline__ f32x2 fold32(f32x4 v) {
  const auto r0 = __builtin_amdgcn_permlane32_swap(f2u(v[0]), f2u(v[2]), false, false);
  const auto r1 = __builtin_amdgcn_permlane32_swap(f2u(v[1]), f2u(v[3]), false, false);
  return f32x2{u2f(r0[0]), u2f(r1[0])} + f32x2{u2f(r0[1]), u2f(r1[1])};
}
__device__ __forceinline__ float xmax16(float a) { auto r = __builtin_amdgcn_permlane16_swap(f2u(a), f2u(a), false, false); return __builtin_amdgcn_fmed3f(u2f(r[0]), u2f(r[1]), INFINITY); }
__device__ __forceinline__ float xmax32(float a) { auto r = __builtin_amdgcn_permlane32_swap(f2u(a), f2u(a), false, false); return __builtin_amdgcn_fmed3f(u2f(r[0]), u2f(r[1]), INFINITY); }
__device__ __forceinline__ float xadd16(float a) { auto r = __builtin_amdgcn_permlane16_swap(f2u(a), f2u(a), false, false); return u2f(r[0]) + u2f(r[1]); }
__device__ __forceinline__ float xadd32(float a) { auto r = __builtin_amdgcn_permlane32_swap(f2u(a), f2u(a), false, false); return u2f(r[0]) + u2f(r[1]); }

__device__ __forceinline__ float wave_sum(float v) {
  v += __shfl_xor(v, 1); v += __shfl_xor(v, 2); v += __shfl_xor(v, 4); v += __shfl_xor(v, 8);
  return xadd32(xadd16(v));
}
#define CFENCE() asm volatile("" ::: "memory")
#define DRAIN_VM() asm volatile("s_waitcnt vmcnt(0)" ::: "memory")
#define WAIT_LGKM() asm volatile("s_waitcnt lgkmcnt(0)" ::: "memory")
__device__ __forceinline__ void wgbar() { WAIT_LGKM(); __builtin_amdgcn_s_barrier(); CFENCE(); }

typedef __amdgpu_buffer_rsrc_t rsrc_t;
__device__ __forceinline__ v4u ld16(rsrc_t rs, unsigned off) { return __builtin_amdgcn_raw_buffer_load_b128(rs, off, 0, AUX_SC1); }
__device__ __forceinline__ v2u ld8(rsrc_t rs, unsigned off) { return __builtin_amdgcn_raw_buffer_load_b64(rs, off, 0, AUX_SC1); }
__device__ __forceinline__ unsigned ld4(rsrc_t rs, unsigned off) { return __builtin_amdgcn_raw_buffer_load_b32(rs, off, 0, AUX_SC1); }
__device__ __forceinline__ void st16(rsrc_t rs, unsigned off, v4u v) { __builtin_amdgcn_raw_buffer_store_b128(v, rs, off, 0, AUX_SC1); }
__device__ __forceinline__ void st8(rsrc_t rs, unsigned off, v2u v) { __builtin_amdgcn_raw_buffer_store_b64(v, rs, off, 0, AUX_SC1); }
__device__ __forceinline__ void st4(rsrc_t rs, unsigned off, unsigned v) { __builtin_amdgcn_raw_buffer_store_b32(v, rs, off, 0, AUX_SC1); }
__device__ __forceinline__ unsigned umax4(v4u v) { return max(max(v.x, v.y), max(v.z, v.w)); }

template <int N> __device__ __forceinline__ void delay_cyc() {
#pragma unroll
  for (int i = 0; i < N / 64; ++i) asm volatile("s_nop 15");
  if ((N % 64) >= 4) asm volatile("s_nop %0" :: "i"((N % 64) / 4 - 1));
}

#define PACE 0
#define PACE_GAIN 0.3f
#define PACE_CLAMP 2.0f
#define GAP_A 6
#define DB3 5
#define EXTRA_B 0
#define EXTRA_C 0
#define GAP_C 6
#define AD_DEC 0
#define AD_INC 0
__device__ __forceinline__ void delay_rt(int n) {
  asm volatile("s_cmp_lt_i32 %0, 1\n\ts_cbranch_scc1 2f\n\ts_mov_b32 vcc_lo, %0\n1:\n\ts_nop 1\n\ts_sub_i32 vcc_lo, vcc_lo, 1\n\ts_cmp_gt_i32 vcc_lo, 0\n\ts_cbranch_scc1 1b\n2:" :: "s"(n) : "vcc", "scc");
}
__device__ __forceinline__ void adapt(int& n, int misses) { n = misses ? min(n + AD_INC, 120) : max(n - AD_DEC, 0); }

__device__ __forceinline__ bool spin_fail(unsigned& spins, gu32* ctrl, unsigned code) {
  ++spins;
  if (spins > SPIN_CAP) { __hip_atomic_store(ctrl + W_TMO, 1u, RLX_AGENT); __hip_atomic_fetch_max(ctrl + W_TMO + 1, code, RLX_AGENT); return true; }
  return (spins & 255u) == 0u && __builtin_amdgcn_readfirstlane(__hip_atomic_load(ctrl + W_TMO, RLX_AGENT)) != 0u;
}
constexpr int RETRY_CAP = 1 << 15;
__device__ __forceinline__ void wait_ge(gu32* ctrl, int word, unsigned want, unsigned code, bool& dead) {
  if (dead) return;
  for (unsigned sp = 0;;) {
    if ((unsigned)__builtin_amdgcn_readfirstlane(__hip_atomic_load(ctrl + word, RLX_AGENT)) >= want) return;
    __builtin_amdgcn_s_sleep(4);
    if (spin_fail(sp, ctrl, code)) { dead = true; return; }
  }
}
__device__ __forceinline__ void wait_flags2(gu32* ctrl, int w0, int w1, unsigned code, bool& dead) {
  if (dead) return;
  for (unsigned sp = 0;;) {
    const unsigned long long a = __hip_atomic_load((GAS unsigned long long*)(ctrl + w0), RLX_AGENT);
    const unsigned long long b = __hip_atomic_load((GAS unsigned long long*)(ctrl + w1), RLX_AGENT);
    const unsigned long long m = a & b;
    if (__builtin_amdgcn_readfirstlane((unsigned)m & (unsigned)(m >> 32)) != 0u) return;
    __builtin_amdgcn_s_sleep(4);
    if (spin_fail(sp, ctrl, code)) { dead = true; return; }
  }
}
__device__ __forceinline__ void wait_lines(gu32* ctrl, int word, int stride, int n, unsigned want, int lane, unsigned code, bool& dead) {
  if (dead) return;
  gu32* mine = ctrl + word + (lane < n ? lane : 0) * stride;
  for (unsigned sp = 0;;) {
    if (__all(__hip_atomic_load(mine, RLX_AGENT) >= want)) return;
    __builtin_amdgcn_s_sleep(2);
    if (spin_fail(sp, ctrl, code)) { dead = true; return; }
  }
}
constexpr int ARM_AHEAD = 4;
__device__ __forceinline__ void arm_share(rsrc_t rs, unsigned al, int c, int tid) {
  const v4u s = {SENT, SENT, SENT, SENT};
  const unsigned lo = (tid & 63) * 16, w = tid >> 6;
  constexpr int NB = PR / 64;
  for (int sl = 0; sl < NSLOT; ++sl)
#pragma unroll
    for (int k = 0; k < NB; ++k) if (!PBF || w < 4) st16(rs, al + OFF_PART + sl * PART_SZ + (c * NB + k) * 8192 + w * 1024 + lo, s);
  if (w < 6) {
#pragma unroll
    for (int k = 0; k < NB; ++k) st16(rs, al + OFF_QKV + (c * NB + k) * 8192 + w * 1024 + lo, s);
  } else if (w == 6) st16(rs, al + OFF_MEM + MEM_ST + c * 1024 + lo, s);
  else if (c < 32) st16(rs, al + OFF_MEM + 5 * MEM_ST + c * 1024 + lo, s);
}
struct Near {
  unsigned al, va21, nz, k;
  __device__ __forceinline__ unsigned rel(int c) const { return k >> 2; }
  __device__ __forceinline__ unsigned frag(unsigned reg, int s, int h) const { return WFAR ? blk(reg + (unsigned)(s * NH + h) * FB, (k >> 2) ^ 1u) : reg + (unsigned)(s * NH + h) * FB; }
  __device__ __forceinline__ unsigned blk(unsigned pair_off, unsigned iod) const { return al + pair_off + ((((va21 + pair_off) >> 21) ^ nz ^ iod) & 1u) * 8192u; }
};
__device__ __forceinline__ Near near_init(const u8* ws) {
  Near n; const unsigned lo = (unsigned)(size_t)ws;
  n.al = (0u - lo) & 16383u; n.va21 = lo + n.al; n.nz = 0; n.k = 0; return n;
}
__device__ __forceinline__ unsigned xcc_id() { return (unsigned)__builtin_amdgcn_s_getreg(20 | (3 << 11)) & 7u; }
__device__ __forceinline__ void near_wait(gu32* ctrl, Near& n, bool& dead) {
  wait_ge(ctrl, W_XCC, 16u, 0x11u, dead);
  n.k = __builtin_amdgcn_readfirstlane(__hip_atomic_load(ctrl + W_XCC, RLX_AGENT)) & 7u;
  n.nz = __builtin_amdgcn_readfirstlane(__hip_atomic_load(ctrl + W_CFG, RLX_AGENT)) & 1u;
}

#define CSTAMP(wv, slot)
#define PT(id)
#define CTRIES(slot, n)
#define WSTAMP()
template <bool SC1>
__device__ __forceinline__ void dma16(const void* base, unsigned voff, unsigned lds_addr) {
  unsigned keep;
  if (SC1) asm volatile("s_mov_b32 %0, m0\n\ts_mov_b32 m0, %3\n\ts_nop 0\n\tglobal_load_lds_dwordx4 %1, %2 sc1\n\ts_mov_b32 m0, %0" : "=&s"(keep) : "v"(voff), "s"(base), "s"(lds_addr) : "memory");
  else asm volatile("s_mov_b32 %0, m0\n\ts_mov_b32 m0, %3\n\ts_nop 0\n\tglobal_load_lds_dwordx4 %1, %2\n\ts_mov_b32 m0, %0" : "=&s"(keep) : "v"(voff), "s"(base), "s"(lds_addr) : "memory");
}

template <int N> __device__ __forceinline__ void wait_vm() { static_assert(N >= 0 && N < 64, "vmcnt"); asm volatile("s_waitcnt vmcnt(%0)" :: "i"(N) : "memory"); }
template <int V> struct IC { static constexpr int v = V; };
template <int N, int I = 0, class F> __device__ __forceinline__ void static_for(F&& f) { if constexpr (I < N) { f(IC<I>{}); static_for<N, I + 1>(f); } }
__device__ __forceinline__ void slice_dma(const u16* src, int ld, unsigned dst, int lane) {
  const unsigned vo = ((lane >> 2) * ld + 8 * (lane & 3)) * 2;
#pragma unroll
  for (int p = 0; p < 8; ++p) dma16<false>(src + (size_t)16 * p * ld, vo, dst + p * 1024);
}
template <bool PERM>
__device__ __forceinline__ void slice_frags(unsigned base, int lane, v8u (&f)[2][2]) {
  const int gB = lane >> 4, q = (lane & 15) >> 2, p = lane & 3;
#pragma unroll
  for (int cc = 0; cc < 2; ++cc)
#pragma unroll
    for (int rr = 0; rr < 4; ++rr) {
      const int kp = 64 * cc + 32 * (rr >> 1) + 16 * (gB >> 1) + 4 * (gB & 1) + 2 * (rr & 1) + (q & 1) + 8 * (q >> 1);
      const int row = PERM ? ((kp & ~31) + 16 * (kp & 1) + ((kp & 31) >> 1)) : kp;
#pragma unroll
      for (int tb = 0; tb < 2; ++tb) {
        const v2u v = trread(base + row * 64 + tb * 32 + p * 8);
        f[tb][cc][2 * rr] = v.x; f[tb][cc][2 * rr + 1] = v.y;
      }
    }
}

__device__ __forceinline__ void chain_role(const Params& p, LAS u8* L, rsrc_t rs, int b) {
  const int tid = threadIdx.x, lane = tid & 63, w = __builtin_amdgcn_readfirstlane(tid >> 6);
  const int c = (b & 3) * 32 + (b >> 3);
  const int h = c >> 2, j = c & 3;
  const int dbgi = (c >> 4) + 8 * (c & 15); (void)dbgi;
  const int r16 = lane & 15, g = lane >> 4;
  gu32* ctrl = (gu32*)p.ws;
  bool dead = false;
  const unsigned lbase = (unsigned)(size_t)L;
  Near nr = near_init(p.ws);
  if (b == 0 && tid == 0) __hip_atomic_store(ctrl + W_XCC, 16u | xcc_id(), RLX_AGENT);

  v8u W1[3][2][2];
  v8u WO[2][2];
  {
    const unsigned mine = lbase + w * 16384;
    const size_t wcol = (size_t)h * 128 + 32 * j;
    slice_dma(p.Wk + (size_t)128 * w * HD + wcol, HD, mine, lane);
    slice_dma(p.Wv + (size_t)128 * w * HD + wcol, HD, mine + 8192, lane);
    {
      const unsigned idx = ((c & 31) * 8 + w) * 64 + lane;
      const unsigned t = ld4(rs, idx < (OFF_QX - OFF_MEM) / TLB_WARM ? nr.al + OFF_MEM + idx * TLB_WARM + (TLB_WARM - 1024) : 0xFFFFFF00u);
      asm volatile("" :: "v"(t));
    }
    asm volatile("s_waitcnt vmcnt(8)" ::: "memory");
    slice_frags<true>(lbase + w * 16384, lane, W1[1]);
    WAIT_LGKM(); __builtin_amdgcn_sched_barrier(0);
    slice_dma(p.Wq + (size_t)128 * w * HD + wcol, HD, mine, lane);
    asm volatile("s_waitcnt vmcnt(8)" ::: "memory");
    slice_frags<true>(lbase + w * 16384 + 8192, lane, W1[2]);
    WAIT_LGKM(); __builtin_amdgcn_sched_barrier(0);
    DRAIN_VM();
    slice_frags<true>(lbase + w * 16384, lane, W1[0]);
    WAIT_LGKM(); __builtin_amdgcn_sched_barrier(0);
  }
  near_wait(ctrl, nr, dead);
  const unsigned myrel = nr.rel(c);

  LAS float* RED = (LAS float*)L;
  LAS u8* AHF = L + 24576;
  volatile LAS unsigned* FOK = (volatile LAS unsigned*)(L + 26624 + 4096);
  LAS float* INV = (LAS float*)(L + 30976);
  LAS u8* KVL = L + 49152;
  const int sidx = r16 < 8 ? 0x4444 : 0xEEEE;
  const int gg = g & 1, hi = g >> 1, rp = 2 * gg + hi;
  int gone = dead ? 1 : 0;

  auto fetch_kv_issue = [&](int s, int lo, int hi) {
    if ((s & 3) == 0) { bool d = gone != 0; const int blk = s >> 2; wait_flags2(ctrl, W_FKV + blk * 128 + 2 * h, W_FKV + blk * 128 + 64 + 2 * h, 0x20u, d); gone = __builtin_amdgcn_readfirstlane(d ? 1 : 0); }
    CFENCE();
    const unsigned dst = lbase + 49152 + (s & 1) * 16384;
#pragma unroll
    for (int i = lo; i < hi; ++i) dma16<true>(p.ws + nr.frag((i >> 3) ? OFF_VX : OFF_KX, s, h) + (i & 7) * 1024, lane * 16, dst + i * 1024);
  };
  auto kv_ready = [&](int s) {
    if ((s & 3) == 0) { bool d = gone != 0; const int blk = s >> 2; wait_flags2(ctrl, W_FKV + blk * 128 + 2 * h, W_FKV + blk * 128 + 64 + 2 * h, 0x20u, d); gone = __builtin_amdgcn_readfirstlane(d ? 1 : 0); CFENCE(); }
  };
  auto arm_addr = [&](int s, int slot, int q, unsigned& a0, unsigned& a1) {
    a0 = s > 0 ? nr.blk(OFF_PART + (unsigned)(slot == 0 ? 2 : slot - 1) * PART_SZ + (c & (PR - 1)) * 16384u, myrel) + q * 1024 + lane * 16 : 0xFFFFFF00u;
    const unsigned qn = nr.blk(OFF_QKV + (unsigned)((s + ARM_AHEAD) * QH + (h & (QH - 1))) * 16384u, myrel);
    a1 = q == 0 ? qn + j * 1024 + lane * 16
                : lane < 32 ? qn + 4096 + j * 512 + lane * 16
                            : (lane < 48 && s + ARM_AHEAD + 2 < NSEG) ? nr.al + OFF_MEM + (unsigned)(s + ARM_AHEAD + 2) * MEM_ST + (c >> 6) * 16384u + ((lane >> 3) & 1) * 8192u + (c & 63) * 128 + (lane & 7) * 16
                                                                      : 0xFFFFFF00u;
    a1 = s + ARM_AHEAD < NSEG ? a1 : 0xFFFFFF00u;
  };
  if (tid == 0) { FOK[0] = 0u; FOK[1] = 0u; }
  int dA = W_A / 20, dB = W_B / 20, dC = W_C / 20;
  wgbar();

  auto run = [&](auto role) {
    constexpr int ROLE = decltype(role)::v;
    LAS f32x2* redw = (LAS f32x2*)(RED + w * 768) + (rp * 16 + r16) * 2;
    const int R = 32 * j + 4 * w + (r16 >> 2);
    const unsigned rrel = nr.rel(R), part_w = (R & (PR - 1)) * 16384u, part_l = PBF ? h * 128 + rp * 32 + (r16 & 3) * 8 : h * 256 + rp * 64 + (r16 & 3) * 16;
    const unsigned pub_l = ROLE == 0 ? j * 1024 + (8 * w + 2 * g + (r16 & 1) + 16 * (r16 >> 2)) * 16 + (r16 & 2) * 4 : 4096 + (32 * j + r16) * 16 + g * 4;
    unsigned qo, a_part, mo, a_pub;
    float pinf = INFINITY, mi = g < 2 ? -INFINITY : 0.f;
    asm volatile("" : "+v"(pinf), "+v"(mi));
    const f32x4 minit = {mi, mi, mi, mi};
    const unsigned s1 = (r16 & 3) == 0 ? 0x00003F80u : (r16 & 3) == 1 ? 0x3F800000u : 0u;
    const v4u rsel = {(r16 & 4) ? 0u : s1, (r16 & 4) ? s1 : 0u, (r16 & 4) ? 0u : s1, (r16 & 4) ? s1 : 0u};
    int slot = 0, lim = gone ? 0 : RETRY_CAP;
    auto prepare = [&](int s, int sl) {
      qo = nr.blk(OFF_QKV + (unsigned)(s * QH + (h & (QH - 1))) * 16384u, myrel);
      a_part = nr.blk(OFF_PART + (unsigned)sl * PART_SZ + part_w, rrel) + part_l;
      mo = nr.blk(OFF_MEM + (unsigned)s * MEM_ST + (w >> 2) * 16384u, myrel) + (w & 3) * 2048 + lane * 16;
      a_pub = qo + pub_l;
      asm volatile("" : "+v"(a_part), "+v"(mo), "+v"(a_pub));
    };
    prepare(0, 0);
    auto step = [&](auto first, int s) -> bool {
      constexpr bool FIRST = decltype(first)::v != 0;
      const unsigned par = (unsigned)(s / 3) & 1u; (void)par;
      PT(0);
      delay_rt(dA);
      PT(1);
      f32x4 acc[6];
      {
        int tries = 0; bool again;
        do {
          CFENCE();
          const v4u a0 = ld16(rs, mo), a1 = ld16(rs, mo + 1024);
          if (FIRST && tries == 0) slice_dma(p.Wo + (size_t)128 * h * DM + 256 * j + 32 * w, DM, lbase + 98304 + w * 8192, lane);
#pragma unroll
          for (int t = 0; t < 6; ++t) { acc[t] = f32x4{0.f, 0.f, 0.f, 0.f}; asm volatile("" : "+v"(acc[t])); }
          __builtin_amdgcn_sched_barrier(0);
#pragma unroll
          for (int t = 0; t < 6; ++t) acc[t] = smf64(a0, W1[t >> 1][t & 1][0], acc[t], sidx);
          __builtin_amdgcn_sched_barrier(0);
#pragma unroll
          for (int t = 0; t < 6; ++t) acc[t] = smf64(a1, W1[t >> 1][t & 1][1], acc[t], sidx);
          again = (__ballot(max(umax4(a0), umax4(a1)) == SENT) != 0ull) & (tries < lim);
          ++tries;
        } while (__builtin_expect(again, 0));
        lim = tries > lim ? 0 : lim;
        if (w == 0) CTRIES(0, tries - 1);
      }
      CSTAMP(0, 0);
      PT(2);
#pragma unroll
      for (int t = 0; t < 6; ++t) redw[(t >> 1) * 128 + (t & 1)] = fold32(acc[t]);
      PT(3);
      wgbar();
      PT(4);
      CSTAMP(0, 1);
      if (ROLE <= 2) {
        const LAS f32x4* redr = (const LAS f32x4*)RED + w * 64 + lane;
        f32x4 t0 = redr[0], t1 = redr[192];
#pragma unroll
        for (int k = 2; k < 8; k += 2) { t0 += redr[k * 192]; t1 += redr[(k + 1) * 192]; }
        t0 += t1;
        if (ROLE == 0) {
          const unsigned d0 = pk2(t0[0], t0[2]), d1 = pk2(t0[1], t0[3]);
          const unsigned got = __builtin_amdgcn_mov_dpp((r16 & 1) ? d0 : d1, 0xB1, 0xF, 0xF, true);
          st8(rs, a_pub, (r16 & 1) ? v2u{canon(got), canon(d1)} : v2u{canon(d0), canon(got)});
        } else {
          st4(rs, a_pub, canon(pk2(t0[0], t0[1])));
          st4(rs, a_pub + 256, canon(pk2(t0[2], t0[3])));
        }
      }
      CSTAMP(0, 2);
      PT(5);
      if (!FIRST && s == NSEG - 1) return false;
      if (FIRST) {
        if (ROLE >= 4) fetch_kv_issue(0, 4 * (ROLE - 4), 4 * (ROLE - 4) + 4);
        DRAIN_VM();
        slice_frags<false>(lbase + 98304 + w * 8192, lane, WO);
        wgbar();
      }
      acc[0] = f32x4{0.f, 0.f, 0.f, 0.f}; acc[1] = acc[0];
      asm volatile("" : "+v"(acc[0]), "+v"(acc[1]));
      if (ROLE <= 3) {
        v4u KX[2][4], VX[2], F[4]; v2u VM[2];
        const LAS u8* kv = KVL + (s & 1) * 16384 + lane * 16;
#pragma unroll
        for (int i = 0; i < 8; ++i) KX[i >> 2][i & 3] = *(const LAS v4u*)(kv + i * 1024);
        VX[0] = *(const LAS v4u*)(kv + 8192 + 2 * w * 1024); VX[1] = *(const LAS v4u*)(kv + 8192 + (2 * w + 1) * 1024);
        const unsigned fo = qo + lane * 16, vo = qo + 4096 + (32 * w + r16) * 16 + gg * 8;
        __builtin_amdgcn_sched_barrier(0);
        delay_rt((ROLE == 3 ? dB + DB3 : dB));
        PT(6);
        f32x4 sA, sB, sM;
        {
          int tries = 0; bool again;
          do {
            CFENCE();
#pragma unroll
            for (int u = 0; u < 4; ++u) F[u] = ld16(rs, fo + u * 1024);
            VM[0] = ld8(rs, vo); VM[1] = ld8(rs, vo + 256);
            sA = f32x4{0.f, 0.f, 0.f, 0.f}; sB = sA; sM = minit;
#pragma unroll
            for (int u = 0; u < 4; ++u) { sM = mfma32(F[u], F[u], sM); sA = mfma32(KX[0][u], F[u], sA); sB = mfma32(KX[1][u], F[u], sB); }
            const unsigned m = max(max(max(umax4(F[0]), umax4(F[1])), max(umax4(F[2]), umax4(F[3]))), max(max(VM[0].x, VM[0].y), max(VM[1].x, VM[1].y)));
            again = (__ballot(m == SENT) != 0ull) & (tries < lim);
            ++tries;
          } while (__builtin_expect(again, 0));
          lim = tries > lim ? 0 : lim;
          if (w == 0) CTRIES(1, tries - 1);
        }
        if (ROLE == 2) FOK[0] = (unsigned)(s + 1);
        CSTAMP(0, 3);
        PT(7);
        const float sc2 = 0.08838834764831845f * 1.4426950408889634f;
        sA *= sc2; sB *= sc2; sM *= sc2;
        float mx = fmaxf(fmaxf(fmaxf(fmaxf(sA[0], sA[1]), sA[2]), fmaxf(fmaxf(sA[3], sB[0]), sB[1])), fmaxf(fmaxf(fmaxf(sB[2], sB[3]), sM[0]), fmaxf(fmaxf(sM[1], sM[2]), sM[3])));
        { auto r = __builtin_amdgcn_permlane16_swap(f2u(mx), f2u(mx), false, false); mx = __builtin_amdgcn_fmed3f(u2f(r[0]), u2f(r[1]), pinf); }
        { auto r = __builtin_amdgcn_permlane32_swap(f2u(mx), f2u(mx), false, false); mx = __builtin_amdgcn_fmed3f(u2f(r[0]), u2f(r[1]), pinf); }
        sA -= mx; sB -= mx; sM -= mx;
#pragma unroll
        for (int r = 0; r < 4; ++r) { sA[r] = __builtin_amdgcn_exp2f(sA[r]); sB[r] = __builtin_amdgcn_exp2f(sB[r]); sM[r] = __builtin_amdgcn_exp2f(sM[r]); }
        const v4u pb = {pk2(sA[0], sA[1]), pk2(sA[2], sA[3]), pk2(sB[0], sB[1]), pk2(sB[2], sB[3])};
        const v2u pm = {pk2(sM[0], sM[1]), pk2(sM[2], sM[3])};
        f32x4 e0 = mfma32(VX[0], pb, f32x4{0.f, 0.f, 0.f, 0.f}), e1 = mfma32(VX[1], pb, f32x4{0.f, 0.f, 0.f, 0.f});
        e0 = mfma16(VM[0], pm, e0); e1 = mfma16(VM[1], pm, e1);
        if (ROLE == 3) {
          const f32x4 ss = sA + sB + sM;
          INV[lane] = __builtin_amdgcn_rcpf(xadd32(xadd16((ss[0] + ss[1]) + (ss[2] + ss[3]))));
        }
        LAS u8* ah = AHF + (r16 < 8 ? (4 * w + hi) * 128 + r16 * 16 + gg * 8 : 4096 + lane * 8);
        *(LAS v2u*)ah = v2u{pk2(e0[0], e0[1]), pk2(e0[2], e0[3])};
        *(LAS v2u*)(ah + 256) = v2u{pk2(e1[0], e1[1]), pk2(e1[2], e1[3])};
      } else {
        constexpr int Q = ROLE - 4;
        kv_ready(s + 1);
        const u8* kb = p.ws + nr.frag((Q >> 1) ? OFF_VX : OFF_KX, s + 1, h) + (Q & 1) * 4096;
        const unsigned dst = lbase + 49152 + ((s + 1) & 1) * 16384 + Q * 4096;
        unsigned ar0, ar1;
        arm_addr(s, slot, Q, ar0, ar1);
        asm volatile("" : "+v"(ar0), "+v"(ar1));
        __builtin_amdgcn_sched_barrier(0);
        for (unsigned sp = 0; sp < (1u << 20); ++sp)
          if ((unsigned)__builtin_amdgcn_readfirstlane(FOK[0]) == (unsigned)(s + 1)) break;
#pragma unroll
        for (int i = 0; i < 4; ++i) dma16<true>(kb + i * 1024, lane * 16, dst + i * 1024);
        st16(rs, ar0, v4u{SENT, SENT, SENT, SENT});
        if (Q < 2) st16(rs, ar1, v4u{SENT, SENT, SENT, SENT});
      }
      PT(9);
      wgbar();
      PT(10);
      CSTAMP(7, 4);
      {
        const v4u b0 = *(LAS v4u*)(AHF + lane * 16), b1 = *(LAS v4u*)(AHF + 1024 + lane * 16);
        const f32x2 iv = *(LAS f32x2*)(INV + 2 * rp);
        acc[0] = smf64(b0, WO[0][0], acc[0], sidx); acc[1] = smf64(b0, WO[1][0], acc[1], sidx);
        acc[0] = smf64(b1, WO[0][1], acc[0], sidx); acc[1] = smf64(b1, WO[1][1], acc[1], sidx);
        const f32x2 f0 = fold32(acc[0]) * iv, f1 = fold32(acc[1]) * iv;
        const v4u pv = {canon(f2u(f0[0])), canon(f2u(f1[0])), canon(f2u(f0[1])), canon(f2u(f1[1]))};
        if (PBF) st8(rs, a_part, TAGP(v2u{canon(pk2(f0[0], f1[0])), canon(pk2(f0[1], f1[1]))})); else st16(rs, a_part, pv);
      }
      __builtin_amdgcn_sched_barrier(0);
      CSTAMP(7, 5);
      PT(11);
      const int nslot = slot == NSLOT - 1 ? 0 : slot + 1;
      if (ROLE == 7) {
        unsigned po = nr.blk(OFF_PART + (unsigned)slot * PART_SZ + (c & (PR - 1)) * 16384u, myrel) + lane * 16;
        unsigned a_mem = nr.blk(OFF_MEM + (unsigned)(s + 1) * MEM_ST + (c >> 6) * 16384u, myrel) + (c & 63) * 128 + (2 * (r16 >> 2) + g) * 16 + (r16 & 3) * 4;
        a_mem = lane < 32 ? a_mem : 0xFFFFFF00u;
        prepare(s + 1, nslot);
        asm volatile("" : "+v"(a_mem), "+v"(po));
        delay_rt(dC);
        PT(12);
        f32x4 t;
        {
          int tries = 0; bool again;
          const unsigned p8 = po - lane * 8;
          do {
            CFENCE();
            v2u pq[8];
#pragma unroll
            for (int i = 0; i < 8; ++i) pq[i] = ld8(rs, p8 + i * 512);
            t = f32x4{0.f, 0.f, 0.f, 0.f};
#pragma unroll
            for (int i = 0; i < 8; i += 2) t = mfma32(rsel, v4u{pq[i].x, pq[i].y, pq[i + 1].x, pq[i + 1].y}, t);
            unsigned m = 0;
#pragma unroll
            for (int i = 0; i < 8; ++i) m = max(m, max(pq[i].x, pq[i].y));
            again = (__ballot(m == SENT) != 0ull) & (tries < lim);
            ++tries;
          } while (__builtin_expect(again, 0));
          lim = tries > lim ? 0 : lim;
          CTRIES(2, tries - 1);
        }
        CSTAMP(7, 6);
        PT(13);
        st4(rs, a_mem, canon(pk2(t[0], t[1])));
      } else {
        prepare(s + 1, nslot);
        if (ROLE == 2 && j == 0 && lane == 0) __hip_atomic_store(ctrl + W_PROG + LN * h, (unsigned)(s + 1), RLX_AGENT);
        if (ROLE >= 4 || ARM_LATE) DRAIN_VM();
      }
      slot = nslot;
      PT(14);
      wgbar();
      CSTAMP(7, 7);
      return true;
    };
    step(IC<1>{}, 0);
    for (int s = 1; step(IC<0>{}, s); ++s) {}
    if (lim == 0 && lane == 0) __hip_atomic_store(ctrl + W_TMO, 1u, RLX_AGENT);
  };
  switch (w) {
    case 0: case 1: run(IC<0>{}); break;
    case 2: run(IC<2>{}); break;
    case 3: run(IC<3>{}); break;
    case 4: run(IC<4>{}); break;
    case 5: run(IC<5>{}); break;
    case 6: run(IC<6>{}); break;
    default: run(IC<7>{}); break;
  }
}

enum { M_K = 0, M_V = 1, M_O = 2 };

template <int BN, int MODE, int WM = 2>
__device__ __forceinline__ void gemm_tile(LAS u8* L, const u16* A, int lda, const u16* W, int ldw, int nkt, f32x4 (&acc)[8 / WM][BN * WM / 128], const Near* nr = nullptr) {
  constexpr int MT = 8 / WM, NT = BN * WM / 128, NCG = BN / 64, STG = 16384 + BN * 128, NB = BN / 64;
  constexpr int NSTG = LDS_BYTES / STG;
  const int tid = threadIdx.x, lane = tid & 63, w = __builtin_amdgcn_readfirstlane(tid >> 6);
  const int wm = w / (8 / WM), wn = w % (8 / WM), r16 = lane & 15, g = lane >> 4, q = r16 >> 2, pp = lane & 3;
  const int arow = 16 * w + (lane >> 3);
  const unsigned avo0 = (arow * lda + 8 * ((lane & 7) ^ ((arow >> 1) & 7))) * 2;
  const unsigned avo1 = ((arow + 8) * lda + 8 * ((lane & 7) ^ (((arow + 8) >> 1) & 7))) * 2;
  const int kr = 8 * w + (lane >> 3);
  const int krs = MODE == M_O ? ((kr & ~31) + 16 * ((kr >> 2) & 1) + 4 * ((kr >> 3) & 3) + (kr & 3)) : kr;
  const int fsw = ((kr >> 1) & 1) | (((kr >> 3) & 1) << 1);
  const unsigned wvo = (krs * ldw + 16 * (((lane & 7) >> 1) ^ fsw) + 8 * (lane & 1)) * 2;
  const unsigned lb = (unsigned)(size_t)L;
  auto stage = [&](int kt, int buf) {
    const unsigned sb = lb + buf * STG;
    if (MODE == M_O) {
      const u8* ab = (const u8*)A + (unsigned)__builtin_amdgcn_readfirstlane(nr->frag(OFF_AX, lda + (w >> 1), kt >> 1)) + (w & 1) * 4096 + (kt & 1) * 2048;
      dma16<false>(ab, lane * 16, sb + (2 * w) * 1024);
      dma16<false>(ab + 1024, lane * 16, sb + (2 * w + 1) * 1024);
    } else {
      dma16<false>(A + kt * 64, avo0, sb + (2 * w) * 1024);
      dma16<false>(A + kt * 64, avo1, sb + (2 * w + 1) * 1024);
    }
#pragma unroll
    for (int i = 0; i < NB; ++i) dma16<false>(W + (size_t)kt * 64 * ldw + 64 * i, wvo, sb + 16384 + (w * NB + i) * 1024);
  };
  const unsigned aoff = lb + (16 * MT * wm + r16) * 128 + ((g ^ ((r16 >> 1) & 7)) * 16);
  const int fr = ((q >> 1) & 1) | ((g & 1) << 1);
  const int cgw = (wn * NT) >> 2, c0 = (wn * NT) & 3;
  unsigned boff[NT];
#pragma unroll
  for (int n = 0; n < NT; ++n) boff[n] = lb + 16384 + (g * NCG + cgw) * 1024 + q * 128 + (((c0 + n) ^ fr) * 32) + pp * 8;
#pragma unroll
  for (int m = 0; m < MT; ++m)
#pragma unroll
    for (int n = 0; n < NT; ++n) acc[m][n] = f32x4{0.f, 0.f, 0.f, 0.f};

  auto compute = [&](int buf) {
    const unsigned so = buf * STG;
    v4u af[2][MT], bf[2][NT];
#pragma unroll
    for (int ks = 0; ks < 2; ++ks) {
#pragma unroll
      for (int n = 0; n < NT; ++n) {
        const v2u x0 = trread(boff[n] + so + ks * 4 * NCG * 1024), x1 = trread(boff[n] + so + ks * 4 * NCG * 1024 + 512);
        bf[ks][n] = v4u{x0.x, x0.y, x1.x, x1.y};
      }
#pragma unroll
      for (int m = 0; m < MT; ++m)
        af[ks][m] = MODE == M_O ? *(LAS v4u*)(size_t)(lb + so + wm * 8192 + lane * 16 + m * 2048 + ks * 1024) : *(LAS v4u*)(size_t)(((aoff + so) ^ (ks * 64)) + m * 2048);
    }
    __builtin_amdgcn_sched_barrier(0);
#pragma unroll
    for (int ks = 0; ks < 2; ++ks)
#pragma unroll
      for (int m = 0; m < MT; ++m)
#pragma unroll
        for (int n = 0; n < NT; ++n) acc[m][n] = MODE == M_V ? mfma32(af[ks][m], bf[ks][n], acc[m][n]) : mfma32(bf[ks][n], af[ks][m], acc[m][n]);
    WAIT_LGKM();
  };
  wgbar();
#pragma unroll
  for (int i = 0; i < NSTG - 1; ++i) stage(i, i);
  int buf = 0;
  for (int kt = 0; kt < nkt - (NSTG - 1); ++kt) {
    wait_vm<(NSTG - 2) * (2 + NB)>();
    __builtin_amdgcn_s_barrier(); CFENCE();
    stage(kt + NSTG - 1, buf >= 1 ? buf - 1 : NSTG - 1);
    compute(buf);
    buf = buf == NSTG - 1 ? 0 : buf + 1;
  }
  static_for<NSTG - 1>([&](auto ic) {
    wait_vm<(NSTG - 2 - decltype(ic)::v) * (2 + NB)>();
    __builtin_amdgcn_s_barrier(); CFENCE();
    compute(buf);
    buf = buf == NSTG - 1 ? 0 : buf + 1;
  });
}

template <int BN, int MODE, int WM = 2>
__device__ __forceinline__ void proj_task(const Params& p, LAS u8* L, rsrc_t rs, Near& nr, bool& dead, int blk, int mtx, int col0) {
  constexpr int MT = 8 / WM, NT = BN * WM / 128;
  const int tid = threadIdx.x, lane = tid & 63, w = __builtin_amdgcn_readfirstlane(tid >> 6), wm = w / (8 / WM), wn = w % (8 / WM);
  f32x4 acc[MT][NT];
  const u16* W = (mtx == 0 ? p.Wk : mtx == 1 ? p.Wv : p.Wq) + col0;
  gemm_tile<BN, MODE, WM>(L, p.x + (size_t)blk * 128 * DM, DM, W, HD, DM / 64, acc);
  const int cw = col0 + wn * 16 * NT, h = cw >> 7;
  if (BN == 64 && blk == 0) near_wait((gu32*)p.ws, nr, dead);
  const unsigned reg = mtx == 0 ? OFF_KX : mtx == 1 ? OFF_VX : OFF_QX;
  const int s0 = blk * 4 + (MT / 2) * wm;
  if (MODE == M_K) {
#pragma unroll
    for (int m = 0; m < MT; ++m) {
      const unsigned o = nr.frag(reg, s0 + (m >> 1), h) + lane * 16 + ((m & 1) * 4 + ((cw & 127) >> 5)) * 1024;
#pragma unroll
      for (int v = 0; v < NT / 2; ++v)
        st16(rs, o + v * 1024, v4u{pk2(acc[m][2 * v][0], acc[m][2 * v + 1][0]), pk2(acc[m][2 * v][1], acc[m][2 * v + 1][1]),
                                   pk2(acc[m][2 * v][2], acc[m][2 * v + 1][2]), pk2(acc[m][2 * v][3], acc[m][2 * v + 1][3])});
    }
  } else {
#pragma unroll
    for (int sg = 0; sg < MT / 2; ++sg)
#pragma unroll
      for (int n = 0; n < NT; ++n)
        st16(rs, nr.frag(reg, s0 + sg, h) + lane * 16 + (((cw & 127) >> 4) + n) * 1024,
             v4u{pk2(acc[2 * sg][n][0], acc[2 * sg][n][1]), pk2(acc[2 * sg][n][2], acc[2 * sg][n][3]),
                 pk2(acc[2 * sg + 1][n][0], acc[2 * sg + 1][n][1]), pk2(acc[2 * sg + 1][n][2], acc[2 * sg + 1][n][3])});
  }
  DRAIN_VM();
  wgbar();
  gu32* ctrl = (gu32*)p.ws;
  if (tid < BN / 64) {
    const int word = mtx == 2 ? W_FQ + blk * 64 + (col0 >> 6) + tid : W_FKV + blk * 128 + mtx * 64 + (col0 >> 6) + tid;
    __hip_atomic_store(ctrl + word, 1u, RLX_AGENT);
  }
}

__device__ __forceinline__ float sigmoidf_(float v) { return 1.0f / (1.0f + __expf(-v)); }

__device__ __forceinline__ void rg_task(const Params& p, rsrc_t rs, int wk) {
  const int tid = threadIdx.x, lane = tid & 63, w = __builtin_amdgcn_readfirstlane(tid >> 6);
  v4u wr[2], gw[2];
#pragma unroll
  for (int i = 0; i < 2; ++i) { wr[i] = *(const GAS v4u*)(p.wr + i * 512 + lane * 8); gw[i] = *(const GAS v4u*)(p.gw + i * 512 + lane * 8); }
#pragma unroll
  for (int t = 0; t < 2; ++t) {
    const int tok = wk * 16 + 2 * w + t;
    float r = 0.f, gs = 0.f;
#pragma unroll
    for (int i = 0; i < 2; ++i) {
      const v4u xv = *(const GAS v4u*)(p.x + (size_t)tok * DM + i * 512 + lane * 8);
#pragma unroll
      for (int d = 0; d < 4; ++d) {
        r = fmaf(bflo(xv[d]), bflo(wr[i][d]), r); r = fmaf(bfhi(xv[d]), bfhi(wr[i][d]), r);
        gs += bfr(bflo(xv[d]) * bflo(gw[i][d])); gs += bfr(bfhi(xv[d]) * bfhi(gw[i][d]));
      }
    }
    r = bfr(wave_sum(r)); gs = bfr(wave_sum(gs));
    if (lane == 0) { st4(rs, OFF_R + tok * 4, f2u(r)); st4(rs, OFF_G + tok * 4, f2u(bfr(sigmoidf_(gs)))); }
  }
  DRAIN_VM();
  wgbar();
  if (tid == 0) __hip_atomic_fetch_add((gu32*)p.ws + W_RG, 1u, RLX_AGENT);
}

__device__ __forceinline__ unsigned long long top_key(unsigned bits, int idx) {
  const unsigned o = (bits & 0x7FFFFFFFu) > 0x7F800000u ? 0u : ((bits & 0x80000000u) ? ~bits : (bits | 0x80000000u));
  return ((unsigned long long)o << 32) | (unsigned)(0x7FFFFFFF - idx);
}
__device__ __forceinline__ void top_ins(unsigned long long k, unsigned long long& a, unsigned long long& b) {
  const unsigned long long lo = k < a ? k : a;
  a = k < a ? a : k;
  b = lo > b ? lo : b;
}
__device__ __forceinline__ void top2(rsrc_t rs, LAS u8* L, int& i0, int& i1) {
  const int tid = threadIdx.x, lane = tid & 63, w = tid >> 6;
  const v4u rv = ld16(rs, OFF_R + tid * 16);
  unsigned long long a = 0ull, b = 0ull;
#pragma unroll
  for (int d = 0; d < 4; ++d) top_ins(top_key(rv[d], tid * 4 + d), a, b);
#pragma unroll
  for (int o = 1; o < 64; o <<= 1) {
    const unsigned long long c0 = __shfl_xor(a, o), c1 = __shfl_xor(b, o);
    top_ins(c0, a, b); top_ins(c1, a, b);
  }
  LAS unsigned long long* tv = (LAS unsigned long long*)L;
  wgbar();
  if (lane == 0) { tv[2 * w] = a; tv[2 * w + 1] = b; }
  wgbar();
  a = b = 0ull;
#pragma unroll
  for (int k = 0; k < 16; ++k) top_ins(tv[k], a, b);
  wgbar();
  i0 = (0x7FFFFFFF - (int)(unsigned)a) & 2047; i1 = (0x7FFFFFFF - (int)(unsigned)b) & 2047;
}

__device__ __forceinline__ void mlp1_task(const Params& p, LAS u8* L, rsrc_t rs, int t, bool& dead) {
  const int tid = threadIdx.x;
  gu32* ctrl = (gu32*)p.ws;
  wait_ge(ctrl, W_RG, NWORK, 0x40u, dead);
  int i0, i1;
  top2(rs, L, i0, i1);
  if (t == 0 && tid == 0) {
    const float r0 = u2f(ld4(rs, OFF_R + i0 * 4)), r1 = u2f(ld4(rs, OFF_R + i1 * 4));
    st16(rs, W_TOP * 4, v4u{(unsigned)i0, (unsigned)i1, f2u(bfr(sigmoidf_(r0))), f2u(bfr(sigmoidf_(r1)))});
  }
  const int rg = tid >> 3, cq = tid & 7;
  float ag[2][8], au[2][8];
#pragma unroll
  for (int e = 0; e < 8; ++e) { ag[0][e] = ag[1][e] = au[0][e] = au[1][e] = 0.f; }
#pragma unroll 4
  for (int i = 0; i < 16; ++i) {
    const int k = rg + 64 * i;
    const v4u gv = *(const GAS v4u*)(p.wg + (size_t)k * FF + 64 * t + 8 * cq), uv = *(const GAS v4u*)(p.wu + (size_t)k * FF + 64 * t + 8 * cq);
    const float x0 = bf2f(p.x[(size_t)i0 * DM + k]), x1 = bf2f(p.x[(size_t)i1 * DM + k]);
#pragma unroll
    for (int d = 0; d < 4; ++d) {
      ag[0][2 * d] = fmaf(x0, bflo(gv[d]), ag[0][2 * d]); ag[0][2 * d + 1] = fmaf(x0, bfhi(gv[d]), ag[0][2 * d + 1]);
      ag[1][2 * d] = fmaf(x1, bflo(gv[d]), ag[1][2 * d]); ag[1][2 * d + 1] = fmaf(x1, bfhi(gv[d]), ag[1][2 * d + 1]);
      au[0][2 * d] = fmaf(x0, bflo(uv[d]), au[0][2 * d]); au[0][2 * d + 1] = fmaf(x0, bfhi(uv[d]), au[0][2 * d + 1]);
      au[1][2 * d] = fmaf(x1, bflo(uv[d]), au[1][2 * d]); au[1][2 * d + 1] = fmaf(x1, bfhi(uv[d]), au[1][2 * d + 1]);
    }
  }
  LAS float* R = (LAS float*)L;
#pragma unroll
  for (int e = 0; e < 8; ++e) {
    R[(0 * 64 + rg) * 65 + 8 * cq + e] = ag[0][e]; R[(1 * 64 + rg) * 65 + 8 * cq + e] = ag[1][e];
    R[(2 * 64 + rg) * 65 + 8 * cq + e] = au[0][e]; R[(3 * 64 + rg) * 65 + 8 * cq + e] = au[1][e];
  }
  wgbar();
  if (tid < 128) {
    const int tok = tid >> 6, col = tid & 63;
    float gs = 0.f, us = 0.f;
    for (int r = 0; r < 64; ++r) { gs += R[(tok * 64 + r) * 65 + col]; us += R[((2 + tok) * 64 + r) * 65 + col]; }
    gs = bfr(gs); us = bfr(us);
    const float act = bfr(gs * sigmoidf_(gs));
    st4(rs, OFF_HID + (tok * FF + 64 * t + col) * 4, f2u(bfr(act * us)));
  }
  DRAIN_VM();
  wgbar();
  if (tid == 0) __hip_atomic_fetch_add(ctrl + W_M1, 1u, RLX_AGENT);
}

__device__ __forceinline__ void mlp2_task(const Params& p, LAS u8* L, rsrc_t rs, int t, bool& dead) {
  const int tid = threadIdx.x;
  gu32* ctrl = (gu32*)p.ws;
  wait_ge(ctrl, W_M1, 56, 0x41u, dead);
  CFENCE();
  const int kc = t >> 2, cb = t & 3, rg = tid >> 5, cq = tid & 31;
  float a[2][8];
#pragma unroll
  for (int e = 0; e < 8; ++e) a[0][e] = a[1][e] = 0.f;
#pragma unroll 4
  for (int i = 0; i < 16; ++i) {
    const int k = 256 * kc + rg + 16 * i;
    const v4u dv = *(const GAS v4u*)(p.wd + (size_t)k * DM + 256 * cb + 8 * cq);
    const float h0 = u2f(ld4(rs, OFF_HID + k * 4)), h1 = u2f(ld4(rs, OFF_HID + (FF + k) * 4));
#pragma unroll
    for (int d = 0; d < 4; ++d) {
      a[0][2 * d] = fmaf(h0, bflo(dv[d]), a[0][2 * d]); a[0][2 * d + 1] = fmaf(h0, bfhi(dv[d]), a[0][2 * d + 1]);
      a[1][2 * d] = fmaf(h1, bflo(dv[d]), a[1][2 * d]); a[1][2 * d + 1] = fmaf(h1, bfhi(dv[d]), a[1][2 * d + 1]);
    }
  }
  LAS float* R = (LAS float*)L;
  wgbar();
#pragma unroll
  for (int e = 0; e < 8; ++e) { R[(0 * 16 + rg) * 257 + 8 * cq + e] = a[0][e]; R[(1 * 16 + rg) * 257 + 8 * cq + e] = a[1][e]; }
  wgbar();
  {
    const int tok = tid >> 8, col = tid & 255;
    float sacc = 0.f;
#pragma unroll
    for (int r = 0; r < 16; ++r) sacc += R[(tok * 16 + r) * 257 + col];
    st4(rs, OFF_YP + ((kc * 2 + tok) * DM + 256 * cb + col) * 4, f2u(sacc));
  }
  DRAIN_VM();
  wgbar();
  if (tid == 0) __hip_atomic_store(ctrl + W_M2 + t * LN, 1u, RLX_AGENT);
}

__device__ __forceinline__ void seg_softmax(const v4u (&F)[4], const v4u (&KX)[2][4], const v4u (&QX)[4], int tq, int g, v4u& pb, v2u& pm) {
  const float scale = 0.08838834764831845f, l2e = 1.4426950408889634f, neg = bfr(-1000000000.0f);
  f32x4 sA = {0.f, 0.f, 0.f, 0.f}, sB = sA, sM = sA;
#pragma unroll
  for (int u = 0; u < 4; ++u) { sM = mfma32(F[u], QX[u], sM); sA = mfma32(KX[0][u], QX[u], sA); sB = mfma32(KX[1][u], QX[u], sB); }
#pragma unroll
  for (int r = 0; r < 4; ++r) {
    sA[r] = 4 * g + r > tq ? neg : bfr(bfr(sA[r]) * scale);
    sB[r] = 16 + 4 * g + r > tq ? neg : bfr(bfr(sB[r]) * scale);
    sM[r] = g < 2 ? -INFINITY : bfr(bfr(sM[r]) * scale);
  }
  float mx = fmaxf(fmaxf(fmaxf(sA[0], sA[1]), fmaxf(sA[2], sA[3])), fmaxf(fmaxf(sB[0], sB[1]), fmaxf(sB[2], sB[3])));
  mx = fmaxf(mx, fmaxf(fmaxf(sM[0], sM[1]), fmaxf(sM[2], sM[3])));
  mx = xmax32(xmax16(mx));
  float sum = 0.f;
#pragma unroll
  for (int r = 0; r < 4; ++r) {
    sA[r] = __builtin_amdgcn_exp2f((sA[r] - mx) * l2e); sB[r] = __builtin_amdgcn_exp2f((sB[r] - mx) * l2e); sM[r] = __builtin_amdgcn_exp2f((sM[r] - mx) * l2e);
    sum += sA[r] + sB[r] + sM[r];
  }
  const float inv = 1.0f / xadd32(xadd16(sum));
  pb = v4u{pk2(sA[0] * inv, sA[1] * inv), pk2(sA[2] * inv, sA[3] * inv), pk2(sB[0] * inv, sB[1] * inv), pk2(sB[2] * inv, sB[3] * inv)};
  pm = v2u{pk2(sM[0] * inv, sM[1] * inv), pk2(sM[2] * inv, sM[3] * inv)};
}

__device__ __forceinline__ void attn_task(const Params& p, rsrc_t rs, const Near& nr, int s, int h, bool& dead) {
  const int lane = threadIdx.x & 63, r16 = lane & 15, g = lane >> 4, gg = g & 1;
  gu32* ctrl = (gu32*)p.ws;
  const int blk = s >> 2;
  wait_flags2(ctrl, W_FKV + blk * 128 + 2 * h, W_FKV + blk * 128 + 64 + 2 * h, 0x50u, dead);
  wait_flags2(ctrl, W_FQ + blk * 64 + 2 * h, W_FQ + blk * 64 + 2 * h, 0x51u, dead);
  CFENCE();
  const unsigned fk = nr.frag(OFF_KX, s, h) + lane * 16, fq = nr.frag(OFF_QX, s, h) + lane * 16, fv = nr.frag(OFF_VX, s, h) + lane * 16;
  v4u KX[2][4], QX[2][4], VX[8], F[4]; v2u VM[8];
#pragma unroll
  for (int i = 0; i < 8; ++i) { KX[i >> 2][i & 3] = ld16(rs, fk + i * 1024); QX[i >> 2][i & 3] = ld16(rs, fq + i * 1024); VX[i] = ld16(rs, fv + i * 1024); }
  wait_ge(ctrl, W_PROG + LN * h, (unsigned)(s + 1), 0x52u, dead);
  const unsigned qo = nr.blk(OFF_QKV + (unsigned)(s * QH + (h & (QH - 1))) * 16384u, nr.rel(h * 4));
  for (unsigned sp = 0;;) {
    CFENCE();
    unsigned m = 0;
#pragma unroll
    for (int u = 0; u < 4; ++u) { F[u] = ld16(rs, qo + u * 1024 + lane * 16); m = max(m, umax4(F[u])); }
#pragma unroll
    for (int e = 0; e < 8; ++e) { VM[e] = ld8(rs, qo + 4096 + (16 * e + r16) * 16 + gg * 8); m = max(m, max(VM[e].x, VM[e].y)); }
    if (__all(m != SENT) || dead) break;
    if (spin_fail(sp, ctrl, 0x53u)) { dead = true; break; }
  }
#pragma unroll
  for (int qt = 0; qt < 2; ++qt) {
    v4u pb; v2u pm;
    seg_softmax(F, KX, QX[qt], 16 * qt + r16, g, pb, pm);
    const unsigned ao = nr.frag(OFF_AX, s, h) + lane * 16 + qt * 4096;
#pragma unroll
    for (int v = 0; v < 4; ++v) {
      f32x4 o0 = mfma32(VX[2 * v], pb, f32x4{0.f, 0.f, 0.f, 0.f}), o1 = mfma32(VX[2 * v + 1], pb, f32x4{0.f, 0.f, 0.f, 0.f});
      o0 = mfma16(VM[2 * v], pm, o0); o1 = mfma16(VM[2 * v + 1], pm, o1);
      st16(rs, ao + v * 1024, v4u{pk2(o0[0], o0[1]), pk2(o0[2], o0[3]), pk2(o1[0], o1[1]), pk2(o1[2], o1[3])});
    }
  }
  DRAIN_VM();
}

__device__ __forceinline__ void mix_store(const Params& p, rsrc_t rs, const v4u& top, int tok, int col, f32x4 a) {
  const float gt = u2f(ld4(rs, OFF_G + tok * 4)), og = bfr(1.0f - gt);
  const int sel = tok == (int)top.x ? 0 : tok == (int)top.y ? 1 : -1;
  const v2u xv = *(const GAS v2u*)(p.x + (size_t)tok * DM + col);
  float bv[4] = {bflo(xv.x), bfhi(xv.x), bflo(xv.y), bfhi(xv.y)};
  if (sel >= 0) {
    f32x4 y = {0.f, 0.f, 0.f, 0.f};
    for (int kc = 0; kc < 14; ++kc) y += __builtin_bit_cast(f32x4, ld16(rs, OFF_YP + ((kc * 2 + sel) * DM + col) * 4));
    const float rsel = u2f(sel == 0 ? top.z : top.w);
#pragma unroll
    for (int r = 0; r < 4; ++r) bv[r] = bfr(bv[r] + bfr(bfr(y[r]) * rsel));
  }
  float o[4];
#pragma unroll
  for (int r = 0; r < 4; ++r) o[r] = bfr(gt * bfr(a[r])) + bfr(og * bv[r]);
  *(GAS v2u*)(p.out + (size_t)tok * DM + col) = v2u{pk2(o[0], o[1]), pk2(o[2], o[3])};
}

__device__ __forceinline__ void oproj_task(const Params& p, LAS u8* L, rsrc_t rs, const Near& nr, int blk, int ct, bool& dead) {
  const int tid = threadIdx.x, lane = tid & 63, w = __builtin_amdgcn_readfirstlane(tid >> 6), wm = w >> 2, wn = w & 3, r16 = lane & 15, g = lane >> 4;
  gu32* ctrl = (gu32*)p.ws;
  if (w == 0) {
    wait_lines(ctrl, W_M2, LN, 56, 1u, lane, 0x60u, dead);
    wait_lines(ctrl, W_T2 + 8 * blk * LN, LN, 8, 1u, lane, 0x61u, dead);
    __builtin_amdgcn_fence(__ATOMIC_ACQUIRE, "agent");
    DRAIN_VM();
  }
  f32x4 acc[4][2];
  gemm_tile<128, M_O>(L, (const u16*)p.ws, blk * 4, p.Wo + ct * 128, DM, HD / 64, acc, &nr);
  const v4u top = ld16(rs, W_TOP * 4);
#pragma unroll
  for (int m = 0; m < 4; ++m)
#pragma unroll
    for (int n = 0; n < 2; ++n) mix_store(p, rs, top, blk * 128 + 64 * wm + 16 * m + r16, ct * 128 + 32 * wn + 16 * n + 4 * g, acc[m][n]);
}

__device__ __forceinline__ void stream_role(const Params& p, LAS u8* L, rsrc_t rs, const Near& nr, int set, int q, bool& dead) {
  int wst = 100; const int wk = set * 32 + q; (void)wst; (void)wk;
  const int tid = threadIdx.x, lane = tid & 63, w = __builtin_amdgcn_readfirstlane(tid >> 6), r16 = lane & 15, g = lane >> 4, gg = g & 1;
  const int ks = q >> 3, ct = q & 7, np = w & 3, kh = w >> 2;
  gu32* ctrl = (gu32*)p.ws;
  const unsigned lb = (unsigned)(size_t)L;
  v4u Wf[16][2];
  LAS u8* Wl = L + 65536 + w * 8192 + lane * 16;
  {
    const int kr = 8 * w + (lane >> 3);
    const int krs = (kr & ~31) + 16 * ((kr >> 2) & 1) + 4 * ((kr >> 3) & 3) + (kr & 3);
    const int fsw = ((kr >> 1) & 1) | (((kr >> 3) & 1) << 1);
    const unsigned wvo = (krs * DM + 16 * (((lane & 7) >> 1) ^ fsw) + 8 * (lane & 1)) * 2;
    const int qq = r16 >> 2, fr = ((qq >> 1) & 1) | ((g & 1) << 1);
    const unsigned boff = lb + kh * 65536 + (g * 2 + (np >> 1)) * 1024 + qq * 128 + (lane & 3) * 8;
    const unsigned b0 = boff + (((2 * (np & 1)) ^ fr) * 32), b1 = boff + (((2 * (np & 1) + 1) ^ fr) * 32);
    static_for<2>([&](auto rc) {
      constexpr int RD = decltype(rc)::v;
      wgbar();
#pragma unroll
      for (int kt = 0; kt < 8; ++kt) {
        const u16* wb = p.Wo + (size_t)(1024 * ks + 512 * (kt >> 2) + 64 * (4 * RD + (kt & 3))) * DM + 128 * ct;
        dma16<false>(wb, wvo, lb + kt * 16384 + (w * 2) * 1024);
        dma16<false>(wb + 64, wvo, lb + kt * 16384 + (w * 2 + 1) * 1024);
      }
      DRAIN_VM();
      wgbar();
#pragma unroll
      for (int i = 0; i < 8; ++i) {
        const unsigned o = (i >> 1) * 16384 + (i & 1) * 8192;
        const v2u x0 = trread(b0 + o), x1 = trread(b0 + o + 512), y0 = trread(b1 + o), y1 = trread(b1 + o + 512);
        Wf[8 * RD + i][0] = v4u{x0.x, x0.y, x1.x, x1.y}; Wf[8 * RD + i][1] = v4u{y0.x, y0.y, y1.x, y1.y};
      }
    });
  }
  wgbar();
#pragma unroll
  for (int i = 0; i < 8; ++i) *(LAS v4u*)(Wl + i * 1024) = Wf[12 + (i >> 1)][i & 1];
  wait_lines(ctrl, W_M2, LN, 56, 1u, lane, 0x70u, dead);
  {
    const int fb = NBULK + (lane & 7), kind = (lane >> 3) % 3, hf = lane >= 24 ? 1 : 0;
    gu32* f = ctrl + (kind == 2 ? W_FQ + fb * 64 : W_FKV + fb * 128 + kind * 64) + 2 * q + hf;
    for (unsigned sp = 0; !dead;) {
      if (__all(__hip_atomic_load(f, RLX_AGENT) != 0u)) break;
      __builtin_amdgcn_s_sleep(8);
      if (spin_fail(sp, ctrl, 0x73u)) dead = true;
    }
  }
  CFENCE();
  const v4u top = ld16(rs, W_TOP * 4);

  auto run = [&](auto role) {
    constexpr bool ATT = decltype(role)::v == 0;
    const int qt = w & 1, eh = w >> 1;
    v4u KX[2][4], QX[4], VX[4];
    auto prefetch = [&](int s) {
      const unsigned fk = nr.frag(OFF_KX, s, q) + lane * 16, fq = nr.frag(OFF_QX, s, q) + lane * 16, fv = nr.frag(OFF_VX, s, q) + lane * 16;
#pragma unroll
      for (int i = 0; i < 8; ++i) KX[i >> 2][i & 3] = ld16(rs, fk + i * 1024);
#pragma unroll
      for (int i = 0; i < 4; ++i) { QX[i] = ld16(rs, fq + qt * 4096 + i * 1024); VX[i] = ld16(rs, fv + (4 * eh + i) * 1024); }
    };
    auto finish = [&](int s) {
      const int f = 4 * ks + (w - 4);
      wait_lines(ctrl, W_SP + (set * 32 + ct) * LN, 8 * LN, 4, (unsigned)(s + 1), lane, 0x72u, dead);
      CFENCE();
      f32x4 sum = {0.f, 0.f, 0.f, 0.f};
#pragma unroll
      for (int k = 0; k < 8; ++k) sum += __builtin_bit_cast(f32x4, ld16(rs, nr.frag(OFF_PS, 0, (((s & 3) * 8 + ct) * 8 + k) * 2 + (f >> 3)) + (f & 7) * 1024 + lane * 16));
      mix_store(p, rs, top, s * 32 + 16 * (f & 1) + r16, ct * 128 + 16 * (f >> 1) + 4 * g, sum);
    };
    unsigned prog = 0;
    if (ATT) prefetch(NBULK * 4 + set);
    for (int s = NBULK * 4 + set; s < NSEG; s += 2) {
      const int si = s - NBULK * 4;
      if (ATT) {
        const unsigned need = (unsigned)(s + 1 < NSEG - 1 ? s + 1 : NSEG - 1);
        if (prog < need) {
          for (unsigned sp = 0; !dead;) {
            prog = __builtin_amdgcn_readfirstlane(__hip_atomic_load(ctrl + W_PROG + LN * q, RLX_AGENT));
            if (prog >= need) break;
            __builtin_amdgcn_s_sleep(2);
            if (spin_fail(sp, ctrl, 0x52u)) dead = true;
          }
        }
        const unsigned qo = nr.blk(OFF_QKV + (unsigned)(s * QH + (q & (QH - 1))) * 16384u, nr.rel(q * 4));
        v4u F[4]; v2u VM[4];
        for (unsigned sp = 0;;) {
          CFENCE();
          unsigned m = 0;
#pragma unroll
          for (int u = 0; u < 4; ++u) { F[u] = ld16(rs, qo + u * 1024 + lane * 16); m = max(m, umax4(F[u])); }
#pragma unroll
          for (int e = 0; e < 4; ++e) { VM[e] = ld8(rs, qo + 4096 + (16 * (4 * eh + e) + r16) * 16 + gg * 8); m = max(m, max(VM[e].x, VM[e].y)); }
          if (__all(m != SENT) || dead) break;
          if (spin_fail(sp, ctrl, 0x53u)) { dead = true; break; }
        }
        v4u pb; v2u pm;
        seg_softmax(F, KX, QX, 16 * qt + r16, g, pb, pm);
        const unsigned ao = nr.frag(OFF_AX, s, q) + qt * 4096 + eh * 2048 + lane * 16;
#pragma unroll
        for (int v = 0; v < 2; ++v) {
          f32x4 o0 = mfma32(VX[2 * v], pb, f32x4{0.f, 0.f, 0.f, 0.f}), o1 = mfma32(VX[2 * v + 1], pb, f32x4{0.f, 0.f, 0.f, 0.f});
          o0 = mfma16(VM[2 * v], pm, o0); o1 = mfma16(VM[2 * v + 1], pm, o1);
          st16(rs, ao + v * 1024, v4u{pk2(o0[0], o0[1]), pk2(o0[2], o0[3]), pk2(o1[0], o1[1]), pk2(o1[2], o1[3])});
        }
        DRAIN_VM();
      } else if (si >= 2) {
        finish(s - 2);
      }
      wgbar();
      WSTAMP();
      if (tid == 0) __hip_atomic_store(ctrl + W_SA + (set * 32 + q) * LN, (unsigned)(s + 1), RLX_AGENT);
      if (w == 4) wait_lines(ctrl, W_SA + (set * 32 + ks * 8) * LN, LN, 8, (unsigned)(s + 1), lane, 0x71u, dead);
      wgbar();
      WSTAMP();
      {
        const unsigned ao = nr.frag(OFF_AX, s, 8 * ks + w) + lane * 16;
        v4u t[8];
#pragma unroll
        for (int i = 0; i < 8; ++i) t[i] = ld16(rs, ao + i * 1024);
#pragma unroll
        for (int i = 0; i < 8; ++i) *(LAS v4u*)(L + w * 8192 + i * 1024 + lane * 16) = t[i];
      }
      if (ATT && s + 2 < NSEG) prefetch(s + 2);
      wgbar();
      f32x4 a[2][2];
#pragma unroll
      for (int i = 0; i < 4; ++i) a[i >> 1][i & 1] = f32x4{0.f, 0.f, 0.f, 0.f};
#pragma unroll
      for (int k = 0; k < 16; ++k) {
        const LAS u8* fp = L + kh * 32768 + (k >> 2) * 8192 + (k & 3) * 1024 + lane * 16;
        const v4u x0 = *(const LAS v4u*)fp, x1 = *(const LAS v4u*)(fp + 4096);
#pragma unroll
        for (int n = 0; n < 2; ++n) {
          const v4u wf = k < 12 ? Wf[k][n] : *(const LAS v4u*)(Wl + ((k - 12) * 2 + n) * 1024);
          a[n][0] = mfma32(wf, x0, a[n][0]); a[n][1] = mfma32(wf, x1, a[n][1]);
        }
      }
      WSTAMP();
      const unsigned pb = nr.frag(OFF_PS, 0, (((s & 3) * 8 + ct) * 8 + 2 * ks + kh) * 2 + (np >> 1)) + 4 * (np & 1) * 1024 + lane * 16;
#pragma unroll
      for (int i = 0; i < 4; ++i) st16(rs, pb + i * 1024, __builtin_bit_cast(v4u, a[i >> 1][i & 1]));
      DRAIN_VM();
      wgbar();
      if (tid == 0) __hip_atomic_store(ctrl + W_SP + (set * 32 + q) * LN, (unsigned)(s + 1), RLX_AGENT);
      WSTAMP();
    }
    if (!ATT) finish(NSEG - 2 + set);
  };
  if (w < 4) run(IC<0>{}); else run(IC<1>{});
}

__device__ __forceinline__ void worker_role(const Params& p, LAS u8* L, rsrc_t rs, int b) {
  const int tid = threadIdx.x, lane = tid & 63, w = __builtin_amdgcn_readfirstlane(tid >> 6);
  const int x8 = 2 * (b & 3) + (b >> 7), r = (b >> 3) & 15, wk = r * 8 + x8;
  gu32* ctrl = (gu32*)p.ws;
  bool dead = false;
  int wst = 0; (void)wst;
  Near nr = near_init(p.ws);
  WSTAMP();
  {
    const int cg = x8 * 16 + r;
    for (int blk = 0; blk < 2; ++blk) {
      if (cg < 64) proj_task<64, M_K, 4>(p, L, rs, nr, dead, blk, 0, cg * 64); else proj_task<64, M_V, 4>(p, L, rs, nr, dead, blk, 1, (cg - 64) * 64);
      WSTAMP();
      if (blk == 0) WSTAMP();
    }
  }
  rg_task(p, rs, wk);
  WSTAMP();
  for (int rnd = 0; rnd < 6; ++rnd) {
    const int ct = x8 * 4 + (r & 3), bq = r >> 2;
    if (rnd < 4) {
      const int blk = 2 + rnd * 4 + bq;
      if (blk < 16) { if (ct < 16) proj_task<256, M_K>(p, L, rs, nr, dead, blk, 0, ct * 256); else proj_task<256, M_V>(p, L, rs, nr, dead, blk, 1, (ct - 16) * 256); }
      else { const int t = (blk - 16) * 32 + ct; if (t < 56) mlp1_task(p, L, rs, t, dead); }
    } else {
      const int qct = x8 * 2 + (r & 1), blk = (rnd - 4) * 8 + (r >> 1);
      proj_task<256, M_K>(p, L, rs, nr, dead, blk, 2, qct * 256);
    }
    WSTAMP();
  }
  if (wk < 56) mlp2_task(p, L, rs, wk, dead);
  WSTAMP();
  if (r < 8) {
    const int blk = x8, m = r;
    for (int i = 0; i < 2; ++i) { const int t = (i * 8 + m) * 8 + w; attn_task(p, rs, nr, blk * 4 + (t >> 5), t & 31, dead); WSTAMP(); }
    wgbar();
    if (tid == 0) __hip_atomic_store(ctrl + W_T2 + (8 * blk + m) * LN, 1u, RLX_AGENT);
    oproj_task(p, L, rs, nr, blk, m, dead);
    WSTAMP();
  } else {
    const int sid = x8 * 8 + (r - 8);
    stream_role(p, L, rs, nr, sid >> 5, sid & 31, dead);
    WSTAMP();
  }
}

__global__ void __launch_bounds__(512) rmt_init_kernel(u8* ws, const u16* mem0) {
  const int tid = threadIdx.x;
  if (blockIdx.x >= 4) {
    const Near n = near_init(ws);
    arm_share(__builtin_amdgcn_make_buffer_rsrc(ws, 0, WS_END, 0x00020000), n.al, blockIdx.x - 4, tid);
    return;
  }
  if (blockIdx.x >= 2) {
    const Near n = near_init(ws);
    const int i = (blockIdx.x - 2) * 512 + tid, cb = i >> 3, row = i & 7;
    const u16* m0 = mem0 + row * DM + (cb >> 2) * 32 + (cb & 3) * 4;
    const v2u lo = *(const GAS v2u*)m0, hi = *(const GAS v2u*)(m0 + 16);
    const v4u v = {canon(__builtin_amdgcn_perm(hi.x, lo.x, 0x05040100u)), canon(__builtin_amdgcn_perm(hi.x, lo.x, 0x07060302u)),
                   canon(__builtin_amdgcn_perm(hi.y, lo.y, 0x05040100u)), canon(__builtin_amdgcn_perm(hi.y, lo.y, 0x07060302u))};
    u8* d = ws + n.al + OFF_MEM + (cb >> 6) * 16384 + (cb & 63) * 128 + row * 16;
    *(GAS v4u*)d = v; *(GAS v4u*)(d + 8192) = v;
    return;
  }
  if (blockIdx.x == 0) {
    for (int i = tid; i < (int)(CTRL_BYTES / 16); i += 512)
      if (i != W_CFG / 4) ((GAS v4u*)ws)[i] = v4u{0u, 0u, 0u, 0u};
  } else if (tid == 0) {
    const Near n = near_init(ws);
    gu32* a0 = (gu32*)(ws + n.al + OFF_CAL); gu32* a1 = a0 + 2048;
    unsigned long long b0 = ~0ull, b1 = ~0ull;
    for (int i = 0; i < 3; ++i) {
      const unsigned long long t0 = __builtin_amdgcn_s_memtime();
      __hip_atomic_store(a0, 0u, RLX_AGENT); DRAIN_VM();
      const unsigned long long t1 = __builtin_amdgcn_s_memtime();
      __hip_atomic_store(a1, 0u, RLX_AGENT); DRAIN_VM();
      const unsigned long long t2 = __builtin_amdgcn_s_memtime();
      b0 = t1 - t0 < b0 ? t1 - t0 : b0; b1 = t2 - t1 < b1 ? t2 - t1 : b1;
    }
    const unsigned nz = ((b1 < b0 ? 1u : 0u) ^ ((n.va21 + OFF_CAL) >> 21) ^ (xcc_id() >> 2)) & 1u;
    ((GAS v4u*)ws)[W_CFG / 4] = v4u{nz, 0u, 0u, 0u};
  }
}

__global__ void __launch_bounds__(NTHR, 2) rmt_mod_kernel(Params p) {
  extern __shared__ __attribute__((aligned(16))) u8 smem[];
  LAS u8* L = (LAS u8*)smem;
  const rsrc_t rs = __builtin_amdgcn_make_buffer_rsrc(p.ws, 0, WS_END, 0x00020000);
  const int b = blockIdx.x;
  if ((b & 4) == 0) chain_role(p, L, rs, b); else worker_role(p, L, rs, b);
}

}

extern "C" void kernel_launch(void* const* d_in, const int* in_sizes, int n_in, void* d_out, int out_size, void* d_ws, size_t ws_size,
                              hipStream_t stream) {
  (void)in_sizes; (void)n_in; (void)out_size; (void)ws_size;
  Params p;
  p.x = (const u16*)d_in[0]; p.mem0 = (const u16*)d_in[1]; p.Wq = (const u16*)d_in[2]; p.Wk = (const u16*)d_in[3];
  p.Wv = (const u16*)d_in[4]; p.Wo = (const u16*)d_in[5]; p.wr = (const u16*)d_in[8]; p.wg = (const u16*)d_in[9];
  p.wu = (const u16*)d_in[10]; p.wd = (const u16*)d_in[11]; p.gw = (const u16*)d_in[12];
  p.out = (u16*)d_out; p.ws = (u8*)d_ws;
  (void)hipFuncSetAttribute((const void*)rmt_mod_kernel, hipFuncAttributeMaxDynamicSharedMemorySize, LDS_BYTES);
  rmt_init_kernel<<<4 + NCHAIN, 512, 0, stream>>>(p.ws, p.mem0);
  rmt_mod_kernel<<<NWG, NTHR, LDS_BYTES, stream>>>(p);
}
```

```cpp
#include <hip/hip_runtime.h>
#include <hip/hip_bf16.h>
#include <stdint.h>

#define GAS __attribute__((address_space(1)))
#define LAS __attribute__((address_space(3)))
typedef unsigned v4u __attribute__((ext_vector_type(4)));
typedef unsigned v2u __attribute__((ext_vector_type(2)));
typedef unsigned v8u __attribute__((ext_vector_type(8)));
typedef float f32x4 __attribute__((ext_vector_type(4)));
typedef __bf16 bfx8 __attribute__((ext_vector_type(8)));
typedef __bf16 bfx16 __attribute__((ext_vector_type(16)));
typedef short s16x4 __attribute__((ext_vector_type(4)));
typedef GAS unsigned gu32;
typedef unsigned short u16;
typedef unsigned char u8;
#define RLX_AGENT __ATOMIC_RELAXED, __HIP_MEMORY_SCOPE_AGENT

#define PBF 1
#define SPLIT_XCD 2
namespace {

constexpr int SEQ = 2048, DM = 1024, NH = 32, HD = 4096, NSEG = 64, FF = 3584;
constexpr int NCHAIN = 128, NWORK = 128, NWG = 256, NTHR = 512;
constexpr unsigned SENT = 0xFFFFFFFFu;
constexpr int AUX_SC1 = 16;

constexpr unsigned CTRL_BYTES = 81920;
constexpr int LN = 32;
constexpr int W_TMO = 0, W_RG = 1 * LN, W_TOP = 3 * LN  , W_CFG = 4 * LN  ,
              W_XCC = 5 * LN, W_PROG = 8 * LN  , W_FKV = 64 * LN  , W_FQ = 128 * LN  ,
              W_ARM = 160 * LN  , W_M2 = 288 * LN  , W_T2 = 344 * LN  ,
              W_SA = 408 * LN  , W_SP = 472 * LN  , W_M1 = 536 * LN  ;
constexpr unsigned OFF_MEM = 98304;
constexpr unsigned MEM_ST = 32768;
constexpr unsigned OFF_QKV = OFF_MEM + NSEG * MEM_ST;
constexpr unsigned QH = 32, PR = 128;
constexpr unsigned OFF_PART = OFF_QKV + NSEG * QH * 16384;
constexpr unsigned PART_SZ = PR * 16384;
#define NSLOT 3
constexpr unsigned OFF_ARM_END = OFF_PART + NSLOT * PART_SZ;
constexpr unsigned OFF_CAL = OFF_ARM_END;
constexpr unsigned OFF_KX = OFF_CAL + 16384 + 16384;
#define WFAR 1
constexpr unsigned FB = WFAR ? 16384 : 8192;
constexpr unsigned OFF_VX = OFF_KX + NSEG * NH * FB;
constexpr unsigned OFF_QX = OFF_VX + NSEG * NH * FB;
constexpr unsigned OFF_AX = OFF_QX + NSEG * NH * FB;
constexpr unsigned OFF_R = OFF_AX + NSEG * NH * FB + 1024 * FB + 16384;
constexpr unsigned OFF_G = OFF_R + SEQ * 4;
constexpr unsigned OFF_HID = OFF_G + SEQ * 4;
constexpr unsigned OFF_YP = OFF_HID + 2 * FF * 4;
constexpr unsigned OFF_PS = OFF_AX + NSEG * NH * FB;
constexpr unsigned WS_END = OFF_YP + 14 * 2 * DM * 4;
constexpr int NBULK = 8;
constexpr int LDS_BYTES = 160 * 1024;
constexpr unsigned SPIN_CAP = 1u << 18;

#define TAGP(...) (__VA_ARGS__)
#define BG_DELAY 16
#define ARM_LATE 0
#define TLB_WARM 0
#define NQK 4
#define FOK_AT 4
#define W_A 320
#define W_B 120
#define W_C 260

struct Params {
  const u16 *x, *mem0, *Wq, *Wk, *Wv, *Wo, *wr, *wg, *wu, *wd, *gw;
  u16* out;
  u8* ws;
};

__device__ __forceinline__ float u2f(unsigned u) { return __builtin_bit_cast(float, u); }
__device__ __forceinline__ unsigned f2u(float f) { return __builtin_bit_cast(unsigned, f); }
__device__ __forceinline__ float bf2f(unsigned b) { return u2f(b << 16); }
__device__ __forceinline__ float bflo(unsigned d) { return u2f(d << 16); }
__device__ __forceinline__ float bfhi(unsigned d) { return u2f(d & 0xFFFF0000u); }
__device__ __forceinline__ unsigned pk2(float lo, float hi) {
  typedef __bf16 bfx2 __attribute__((ext_vector_type(2)));
  bfx2 v; v.x = (__bf16)lo; v.y = (__bf16)hi;
  return __builtin_bit_cast(unsigned, v);
}
__device__ __forceinline__ float bfr(float f) { return (float)(__bf16)f; }
__device__ __forceinline__ unsigned canon(unsigned d) { return min(d, 0xFFFFFFFEu); }
__device__ __forceinline__ f32x4 mfma32(v4u a, v4u b, f32x4 c) {
  return __builtin_amdgcn_mfma_f32_16x16x32_bf16(__builtin_bit_cast(bfx8, a), __builtin_bit_cast(bfx8, b), c, 0, 0, 0);
}
__device__ __forceinline__ f32x4 mfma16(v2u a, v2u b, f32x4 c) { return mfma32(v4u{a.x, a.y, 0u, 0u}, v4u{b.x, b.y, 0u, 0u}, c); }
__device__ __forceinline__ f32x4 smf64(v4u a, v8u b, f32x4 c, int idx) {
  return __builtin_amdgcn_smfmac_f32_16x16x64_bf16(__builtin_bit_cast(bfx8, a), __builtin_bit_cast(bfx16, b), c, idx, 0, 0);
}
__device__ __forceinline__ v2u trread(unsigned lds_addr) {
  return __builtin_bit_cast(v2u, __builtin_amdgcn_ds_read_tr16_b64_v4i16((LAS s16x4*)(size_t)lds_addr));
}
typedef float f32x2 __attribute__((ext_vector_type(2)));
__device__ __forceinline__ f32x2 fold32(f32x4 v) {
  const auto r0 = __builtin_amdgcn_permlane32_swap(f2u(v[0]), f2u(v[2]), false, false);
  const auto r1 = __builtin_amdgcn_permlane32_swap(f2u(v[1]), f2u(v[3]), false, false);
  return f32x2{u2f(r0[0]), u2f(r1[0])} + f32x2{u2f(r0[1]), u2f(r1[1])};
}
__device__ __forceinline__ float xmax16(float a) { auto r = __builtin_amdgcn_permlane16_swap(f2u(a), f2u(a), false, false); return __builtin_amdgcn_fmed3f(u2f(r[0]), u2f(r[1]), INFINITY); }
__device__ __forceinline__ float xmax32(float a) { auto r = __builtin_amdgcn_permlane32_swap(f2u(a), f2u(a), false, false); return __builtin_amdgcn_fmed3f(u2f(r[0]), u2f(r[1]), INFINITY); }
__device__ __forceinline__ float xadd16(float a) { auto r = __builtin_amdgcn_permlane16_swap(f2u(a), f2u(a), false, false); return u2f(r[0]) + u2f(r[1]); }
__device__ __forceinline__ float xadd32(float a) { auto r = __builtin_amdgcn_permlane32_swap(f2u(a), f2u(a), false, false); return u2f(r[0]) + u2f(r[1]); }

__device__ __forceinline__ float wave_sum(float v) {
  v += __shfl_xor(v, 1); v += __shfl_xor(v, 2); v += __shfl_xor(v, 4); v += __shfl_xor(v, 8);
  return xadd32(xadd16(v));
}
#define CFENCE() asm volatile("" ::: "memory")
#define DRAIN_VM() asm volatile("s_waitcnt vmcnt(0)" ::: "memory")
#define WAIT_LGKM() asm volatile("s_waitcnt lgkmcnt(0)" ::: "memory")
__device__ __forceinline__ void wgbar() { WAIT_LGKM(); __builtin_amdgcn_s_barrier(); CFENCE(); }

typedef __amdgpu_buffer_rsrc_t rsrc_t;
__device__ __forceinline__ v4u ld16(rsrc_t rs, unsigned off) { return __builtin_amdgcn_raw_buffer_load_b128(rs, off, 0, AUX_SC1); }
__device__ __forceinline__ v2u ld8(rsrc_t rs, unsigned off) { return __builtin_amdgcn_raw_buffer_load_b64(rs, off, 0, AUX_SC1); }
__device__ __forceinline__ unsigned ld4(rsrc_t rs, unsigned off) { return __builtin_amdgcn_raw_buffer_load_b32(rs, off, 0, AUX_SC1); }
__device__ __forceinline__ void st16(rsrc_t rs, unsigned off, v4u v) { __builtin_amdgcn_raw_buffer_store_b128(v, rs, off, 0, AUX_SC1); }
__device__ __forceinline__ void st8(rsrc_t rs, unsigned off, v2u v) { __builtin_amdgcn_raw_buffer_store_b64(v, rs, off, 0, AUX_SC1); }
__device__ __forceinline__ void st4(rsrc_t rs, unsigned off, unsigned v) { __builtin_amdgcn_raw_buffer_store_b32(v, rs, off, 0, AUX_SC1); }
__device__ __forceinline__ unsigned umax4(v4u v) { return max(max(v.x, v.y), max(v.z, v.w)); }

template <int N> __device__ __forceinline__ void delay_cyc() {
#pragma unroll
  for (int i = 0; i < N / 64; ++i) asm volatile("s_nop 15");
  if ((N % 64) >= 4) asm volatile("s_nop %0" :: "i"((N % 64) / 4 - 1));
}

#define PACE 0
#define PACE_GAIN 0.3f
#define PACE_CLAMP 2.0f
#define GAP_A 6
#define DB3 5
#define EXTRA_B 0
#define EXTRA_C 0
#define GAP_C 6
#define AD_DEC 0
#define AD_INC 0
__device__ __forceinline__ void delay_rt(int n) {
  asm volatile("s_cmp_lt_i32 %0, 1\n\ts_cbranch_scc1 2f\n\ts_mov_b32 vcc_lo, %0\n1:\n\ts_nop 1\n\ts_sub_i32 vcc_lo, vcc_lo, 1\n\ts_cmp_gt_i32 vcc_lo, 0\n\ts_cbranch_scc1 1b\n2:" :: "s"(n) : "vcc", "scc");
}
__device__ __forceinline__ void adapt(int& n, int misses) { n = misses ? min(n + AD_INC, 120) : max(n - AD_DEC, 0); }

__device__ __forceinline__ bool spin_fail(unsigned& spins, gu32* ctrl, unsigned code) {
  ++spins;
  if (spins > SPIN_CAP) { __hip_atomic_store(ctrl + W_TMO, 1u, RLX_AGENT); __hip_atomic_fetch_max(ctrl + W_TMO + 1, code, RLX_AGENT); return true; }
  return (spins & 255u) == 0u && __builtin_amdgcn_readfirstlane(__hip_atomic_load(ctrl + W_TMO, RLX_AGENT)) != 0u;
}
constexpr int RETRY_CAP = 1 << 15;
__device__ __forceinline__ void wait_ge(gu32* ctrl, int word, unsigned want, unsigned code, bool& dead) {
  if (dead) return;
  for (unsigned sp = 0;;) {
    if ((unsigned)__builtin_amdgcn_readfirstlane(__hip_atomic_load(ctrl + word, RLX_AGENT)) >= want) return;
    __builtin_amdgcn_s_sleep(4);
    if (spin_fail(sp, ctrl, code)) { dead = true; return; }
  }
}
__device__ __forceinline__ void wait_flags2(gu32* ctrl, int w0, int w1, unsigned code, bool& dead) {
  if (dead) return;
  for (unsigned sp = 0;;) {
    const unsigned long long a = __hip_atomic_load((GAS unsigned long long*)(ctrl + w0), RLX_AGENT);
    const unsigned long long b = __hip_atomic_load((GAS unsigned long long*)(ctrl + w1), RLX_AGENT);
    const unsigned long long m = a & b;
    if (__builtin_amdgcn_readfirstlane((unsigned)m & (unsigned)(m >> 32)) != 0u) return;
    __builtin_amdgcn_s_sleep(4);
    if (spin_fail(sp, ctrl, code)) { dead = true; return; }
  }
}
__device__ __forceinline__ void wait_lines(gu32* ctrl, int word, int stride, int n, unsigned want, int lane, unsigned code, bool& dead) {
  if (dead) return;
  gu32* mine = ctrl + word + (lane < n ? lane : 0) * stride;
  for (unsigned sp = 0;;) {
    if (__all(__hip_atomic_load(mine, RLX_AGENT) >= want)) return;
    __builtin_amdgcn_s_sleep(2);
    if (spin_fail(sp, ctrl, code)) { dead = true; return; }
  }
}
constexpr int ARM_AHEAD = 4;
__device__ __forceinline__ void arm_share(rsrc_t rs, unsigned al, int c, int tid) {
  const v4u s = {SENT, SENT, SENT, SENT};
  const unsigned lo = (tid & 63) * 16, w = tid >> 6;
  constexpr int NB = PR / 64;
  for (int sl = 0; sl < NSLOT; ++sl)
#pragma unroll
    for (int k = 0; k < NB; ++k) if (!PBF || w < 4) st16(rs, al + OFF_PART + sl * PART_SZ + (c * NB + k) * 8192 + w * 1024 + lo, s);
  if (w < 6) {
#pragma unroll
    for (int k = 0; k < NB; ++k) st16(rs, al + OFF_QKV + (c * NB + k) * 8192 + w * 1024 + lo, s);
  } else if (w == 6) st16(rs, al + OFF_MEM + MEM_ST + c * 1024 + lo, s);
  else if (c < 32) st16(rs, al + OFF_MEM + 5 * MEM_ST + c * 1024 + lo, s);
}
struct Near {
  unsigned al, va21, nz, k;
  __device__ __forceinline__ unsigned rel(int c) const { return k >> 2; }
  __device__ __forceinline__ unsigned frag(unsigned reg, int s, int h) const { return WFAR ? blk(reg + (unsigned)(s * NH + h) * FB, (k >> 2) ^ 1u) : reg + (unsigned)(s * NH + h) * FB; }
  __device__ __forceinline__ unsigned blk(unsigned pair_off, unsigned iod) const { return al + pair_off + ((((va21 + pair_off) >> 21) ^ nz ^ iod) & 1u) * 8192u; }
};
__device__ __forceinline__ Near near_init(const u8* ws) {
  Near n; const unsigned lo = (unsigned)(size_t)ws;
  n.al = (0u - lo) & 16383u; n.va21 = lo + n.al; n.nz = 0; n.k = 0; return n;
}
__device__ __forceinline__ unsigned xcc_id() { return (unsigned)__builtin_amdgcn_s_getreg(20 | (3 << 11)) & 7u; }
__device__ __forceinline__ void near_wait(gu32* ctrl, Near& n, bool& dead) {
  wait_ge(ctrl, W_XCC, 16u, 0x11u, dead);
  n.k = __builtin_amdgcn_readfirstlane(__hip_atomic_load(ctrl + W_XCC, RLX_AGENT)) & 7u;
  n.nz = __builtin_amdgcn_readfirstlane(__hip_atomic_load(ctrl + W_CFG, RLX_AGENT)) & 1u;
}

#define CSTAMP(wv, slot)
#define PT(id)
#define CTRIES(slot, n)
#define WSTAMP()
template <bool SC1>
__device__ __forceinline__ void dma16(const void* base, unsigned voff, unsigned lds_addr) {
  unsigned keep;
  if (SC1) asm volatile("s_mov_b32 %0, m0\n\ts_mov_b32 m0, %3\n\ts_nop 0\n\tglobal_load_lds_dwordx4 %1, %2 sc1\n\ts_mov_b32 m0, %0" : "=&s"(keep) : "v"(voff), "s"(base), "s"(lds_addr) : "memory");
  else asm volatile("s_mov_b32 %0, m0\n\ts_mov_b32 m0, %3\n\ts_nop 0\n\tglobal_load_lds_dwordx4 %1, %2\n\ts_mov_b32 m0, %0" : "=&s"(keep) : "v"(voff), "s"(base), "s"(lds_addr) : "memory");
}

template <int N> __device__ __forceinline__ void wait_vm() { static_assert(N >= 0 && N < 64, "vmcnt"); asm volatile("s_waitcnt vmcnt(%0)" :: "i"(N) : "memory"); }
template <int V> struct IC { static constexpr int v = V; };
template <int N, int I = 0, class F> __device__ __forceinline__ void static_for(F&& f) { if constexpr (I < N) { f(IC<I>{}); static_for<N, I + 1>(f); } }
__device__ __forceinline__ void slice_dma(const u16* src, int ld, unsigned dst, int lane) {
  const unsigned vo = ((lane >> 2) * ld + 8 * (lane & 3)) * 2;
#pragma unroll
  for (int p = 0; p < 8; ++p) dma16<false>(src + (size_t)16 * p * ld, vo, dst + p * 1024);
}
template <bool PERM>
__device__ __forceinline__ void slice_frags(unsigned base, int lane, v8u (&f)[2][2]) {
  const int gB = lane >> 4, q = (lane & 15) >> 2, p = lane & 3;
#pragma unroll
  for (int cc = 0; cc < 2; ++cc)
#pragma unroll
    for (int rr = 0; rr < 4; ++rr) {
      const int kp = 64 * cc + 32 * (rr >> 1) + 16 * (gB >> 1) + 4 * (gB & 1) + 2 * (rr & 1) + (q & 1) + 8 * (q >> 1);
      const int row = PERM ? ((kp & ~31) + 16 * (kp & 1) + ((kp & 31) >> 1)) : kp;
#pragma unroll
      for (int tb = 0; tb < 2; ++tb) {
        const v2u v = trread(base + row * 64 + tb * 32 + p * 8);
        f[tb][cc][2 * rr] = v.x; f[tb][cc][2 * rr + 1] = v.y;
      }
    }
}

__device__ __forceinline__ void chain_role(const Params& p, LAS u8* L, rsrc_t rs, int b) {
  const int tid = threadIdx.x, lane = tid & 63, w = __builtin_amdgcn_readfirstlane(tid >> 6);
  const int c = (b & 3) * 32 + (b >> 3);
  const int h = c >> 2, j = c & 3;
  const int dbgi = (c >> 4) + 8 * (c & 15); (void)dbgi;
  const int r16 = lane & 15, g = lane >> 4;
  gu32* ctrl = (gu32*)p.ws;
  bool dead = false;
  const unsigned lbase = (unsigned)(size_t)L;
  Near nr = near_init(p.ws);
  if (b == 0 && tid == 0) __hip_atomic_store(ctrl + W_XCC, 16u | xcc_id(), RLX_AGENT);

  v8u W1[3][2][2];
  v8u WO[2][2];
  {
    const unsigned mine = lbase + w * 16384;
    const size_t wcol = (size_t)h * 128 + 32 * j;
    slice_dma(p.Wk + (size_t)128 * w * HD + wcol, HD, mine, lane);
    slice_dma(p.Wv + (size_t)128 * w * HD + wcol, HD, mine + 8192, lane);
    asm volatile("s_waitcnt vmcnt(8)" ::: "memory");
    slice_frags<true>(lbase + w * 16384, lane, W1[1]);
    WAIT_LGKM(); __builtin_amdgcn_sched_barrier(0);
    slice_dma(p.Wq + (size_t)128 * w * HD + wcol, HD, mine, lane);
    asm volatile("s_waitcnt vmcnt(8)" ::: "memory");
    slice_frags<true>(lbase + w * 16384 + 8192, lane, W1[2]);
    WAIT_LGKM(); __builtin_amdgcn_sched_barrier(0);
    DRAIN_VM();
    slice_frags<true>(lbase + w * 16384, lane, W1[0]);
    WAIT_LGKM(); __builtin_amdgcn_sched_barrier(0);
  }
  near_wait(ctrl, nr, dead);
  const unsigned myrel = nr.rel(c);

  LAS float* RED = (LAS float*)L;
  LAS u8* AHF = L + 24576;
  volatile LAS unsigned* FOK = (volatile LAS unsigned*)(L + 31744);
  LAS float* INV = (LAS float*)(L + 30976);
  LAS u8* KVL = L + 49152;
  const int sidx = r16 < 8 ? 0x4444 : 0xEEEE;
  const int gg = g & 1, hi = g >> 1, rp = 2 * gg + hi;
  int gone = dead ? 1 : 0;

  auto fetch_kv_issue = [&](int s, int lo, int hi) {
    if ((s & 3) == 0) { bool d = gone != 0; const int blk = s >> 2; wait_flags2(ctrl, W_FKV + blk * 128 + 2 * h, W_FKV + blk * 128 + 64 + 2 * h, 0x20u, d); gone = __builtin_amdgcn_readfirstlane(d ? 1 : 0); }
    CFENCE();
    const unsigned dst = lbase + 49152 + (s & 1) * 16384;
#pragma unroll
    for (int i = lo; i < hi; ++i) dma16<true>(p.ws + nr.frag((i >> 3) ? OFF_VX : OFF_KX, s, h) + (i & 7) * 1024, lane * 16, dst + i * 1024);
  };
  auto kv_ready = [&](int s) {
    if ((s & 3) == 0) { bool d = gone != 0; const int blk = s >> 2; wait_flags2(ctrl, W_FKV + blk * 128 + 2 * h, W_FKV + blk * 128 + 64 + 2 * h, 0x20u, d); gone = __builtin_amdgcn_readfirstlane(d ? 1 : 0); CFENCE(); }
  };
  auto arm_addr = [&](int s, int slot, int q, unsigned& a0, unsigned& a1) {
    a0 = s > 0 ? nr.blk(OFF_PART + (unsigned)(slot == 0 ? 2 : slot - 1) * PART_SZ + (c & (PR - 1)) * 16384u, myrel) + q * 1024 + lane * 16 : 0xFFFFFF00u;
    const unsigned qn = nr.blk(OFF_QKV + (unsigned)((s + ARM_AHEAD) * QH + (h & (QH - 1))) * 16384u, myrel);
    a1 = q == 0 ? qn + j * 1024 + lane * 16
                : lane < 32 ? qn + 4096 + j * 512 + lane * 16
                            : (lane < 48 && s + ARM_AHEAD + 2 < NSEG) ? nr.al + OFF_MEM + (unsigned)(s + ARM_AHEAD + 2) * MEM_ST + (c >> 6) * 16384u + ((lane >> 3) & 1) * 8192u + (c & 63) * 128 + (lane & 7) * 16
                                                                      : 0xFFFFFF00u;
    a1 = s + ARM_AHEAD < NSEG ? a1 : 0xFFFFFF00u;
  };
  if (tid == 0) { FOK[0] = 0u; FOK[1] = 0u; }
  int dA = W_A / 20, dB = W_B / 20, dC = W_C / 20;
  wgbar();

  auto run = [&](auto role) {
    constexpr int ROLE = decltype(role)::v;
    LAS f32x2* redw = (LAS f32x2*)(RED + w * 768) + (rp * 16 + r16) * 2;
    const int R = 32 * j + 4 * w + (r16 >> 2);
    const unsigned rrel = nr.rel(R), part_w = (R & (PR - 1)) * 16384u, part_l = PBF ? h * 128 + rp * 32 + (r16 & 3) * 8 : h * 256 + rp * 64 + (r16 & 3) * 16;
    const unsigned pub_l = ROLE == 0 ? j * 1024 + (8 * w + 2 * g + (r16 & 1) + 16 * (r16 >> 2)) * 16 + (r16 & 2) * 4 : 4096 + (32 * j + r16) * 16 + g * 4;
    unsigned qo, a_part, mo, a_pub;
    float pinf = INFINITY, mi = g < 2 ? -INFINITY : 0.f;
    asm volatile("" : "+v"(pinf), "+v"(mi));
    const f32x4 minit = {mi, mi, mi, mi};
    const unsigned s1 = (r16 & 3) == 0 ? 0x00003F80u : (r16 & 3) == 1 ? 0x3F800000u : 0u;
    const v4u rsel = {(r16 & 4) ? 0u : s1, (r16 & 4) ? s1 : 0u, (r16 & 4) ? 0u : s1, (r16 & 4) ? s1 : 0u};
    int slot = 0, lim = gone ? 0 : RETRY_CAP;
    auto prepare = [&](int s, int sl) {
      qo = nr.blk(OFF_QKV + (unsigned)(s * QH + (h & (QH - 1))) * 16384u, myrel);
      a_part = nr.blk(OFF_PART + (unsigned)sl * PART_SZ + part_w, rrel) + part_l;
      mo = nr.blk(OFF_MEM + (unsigned)s * MEM_ST + (w >> 2) * 16384u, myrel) + (w & 3) * 2048 + lane * 16;
      a_pub = qo + pub_l;
      asm volatile("" : "+v"(a_part), "+v"(mo), "+v"(a_pub));
    };
    prepare(0, 0);
    auto step = [&](auto first, int s) -> bool {
      constexpr bool FIRST = decltype(first)::v != 0;
      const unsigned par = (unsigned)(s / 3) & 1u; (void)par;
      PT(0);
      delay_rt(dA);
      PT(1);
      f32x4 acc[6];
      {
        int tries = 0; bool again;
        do {
          CFENCE();
          const v4u a0 = ld16(rs, mo), a1 = ld16(rs, mo + 1024);
          if (FIRST && tries == 0) slice_dma(p.Wo + (size_t)128 * h * DM + 256 * j + 32 * w, DM, lbase + 98304 + w * 8192, lane);
#pragma unroll
          for (int t = 0; t < 6; ++t) { acc[t] = f32x4{0.f, 0.f, 0.f, 0.f}; asm volatile("" : "+v"(acc[t])); }
          __builtin_amdgcn_sched_barrier(0);
#pragma unroll
          for (int t = 0; t < 6; ++t) acc[t] = smf64(a0, W1[t >> 1][t & 1][0], acc[t], sidx);
          __builtin_amdgcn_sched_barrier(0);
#pragma unroll
          for (int t = 0; t < 6; ++t) acc[t] = smf64(a1, W1[t >> 1][t & 1][1], acc[t], sidx);
          again = (__ballot(max(umax4(a0), umax4(a1)) == SENT) != 0ull) & (tries < lim);
          ++tries;
        } while (__builtin_expect(again, 0));
        lim = tries > lim ? 0 : lim;
        if (w == 0) CTRIES(0, tries - 1);
      }
      CSTAMP(0, 0);
      PT(2);
#pragma unroll
      for (int t = 0; t < 6; ++t) redw[(t >> 1) * 128 + (t & 1)] = fold32(acc[t]);
      PT(3);
      wgbar();
      PT(4);
      CSTAMP(0, 1);
      if (ROLE <= 2) {
        const LAS f32x4* redr = (const LAS f32x4*)RED + w * 64 + lane;
        f32x4 t0 = redr[0], t1 = redr[192];
#pragma unroll
        for (int k = 2; k < 8; k += 2) { t0 += redr[k * 192]; t1 += redr[(k + 1) * 192]; }
        t0 += t1;
        if (ROLE == 0) {
          const unsigned d0 = pk2(t0[0], t0[2]), d1 = pk2(t0[1], t0[3]);
          const unsigned got = __builtin_amdgcn_mov_dpp((r16 & 1) ? d0 : d1, 0xB1, 0xF, 0xF, true);
          st8(rs, a_pub, (r16 & 1) ? v2u{canon(got), canon(d1)} : v2u{canon(d0), canon(got)});
        } else {
          st4(rs, a_pub, canon(pk2(t0[0], t0[1])));
          st4(rs, a_pub + 256, canon(pk2(t0[2], t0[3])));
        }
      }
      CSTAMP(0, 2);
      PT(5);
      if (!FIRST && s == NSEG - 1) return false;
      if (FIRST) {
        if (ROLE >= 4) fetch_kv_issue(0, 4 * (ROLE - 4), 4 * (ROLE - 4) + 4);
        DRAIN_VM();
        slice_frags<false>(lbase + 98304 + w * 8192, lane, WO);
        wgbar();
      }
      acc[0] = f32x4{0.f, 0.f, 0.f, 0.f}; acc[1] = acc[0];
      asm volatile("" : "+v"(acc[0]), "+v"(acc[1]));
      if (ROLE < NQK) {
        constexpr int NE = 8 / NQK;
        v4u KX[2][4], VX[NE], F[4]; v2u VM[NE];
        const LAS u8* kv = KVL + (s & 1) * 16384 + lane * 16;
#pragma unroll
        for (int i = 0; i < 8; ++i) KX[i >> 2][i & 3] = *(const LAS v4u*)(kv + i * 1024);
#pragma unroll
        for (int e = 0; e < NE; ++e) VX[e] = *(const LAS v4u*)(kv + 8192 + (NE * w + e) * 1024);
        const unsigned fo = qo + lane * 16, vo = qo + 4096 + (16 * NE * w + r16) * 16 + gg * 8;
        __builtin_amdgcn_sched_barrier(0);
        delay_rt((ROLE == 3 ? dB + DB3 : dB));
        PT(6);
        f32x4 sA, sB, sM;
        {
          int tries = 0; bool again;
          do {
            CFENCE();
#pragma unroll
            for (int u = 0; u < 4; ++u) F[u] = ld16(rs, fo + u * 1024);
#pragma unroll
            for (int e = 0; e < NE; ++e) VM[e] = ld8(rs, vo + e * 256);
            sA = f32x4{0.f, 0.f, 0.f, 0.f}; sB = sA; sM = minit;
#pragma unroll
            for (int u = 0; u < 4; ++u) {
              sM = mfma32(F[u], F[u], sM); sA = mfma32(KX[0][u], F[u], sA); sB = mfma32(KX[1][u], F[u], sB);
              if (u == FOK_AT) { __builtin_amdgcn_sched_barrier(0); FOK[0] = (unsigned)(s + 1); __builtin_amdgcn_sched_barrier(0); }
            }
            unsigned m = max(max(umax4(F[0]), umax4(F[1])), max(umax4(F[2]), umax4(F[3])));
#pragma unroll
            for (int e = 0; e < NE; ++e) m = max(m, max(VM[e].x, VM[e].y));
            again = (__ballot(m == SENT) != 0ull) & (tries < lim);
            ++tries;
          } while (__builtin_expect(again, 0));
          lim = tries > lim ? 0 : lim;
          if (w == 0) CTRIES(1, tries - 1);
        }
        if (FOK_AT > 3) FOK[0] = (unsigned)(s + 1);
        CSTAMP(0, 3);
        PT(7);
        const float sc2 = 0.08838834764831845f * 1.4426950408889634f;
        sA *= sc2; sB *= sc2; sM *= sc2;
        float mx = fmaxf(fmaxf(fmaxf(fmaxf(sA[0], sA[1]), sA[2]), fmaxf(fmaxf(sA[3], sB[0]), sB[1])), fmaxf(fmaxf(fmaxf(sB[2], sB[3]), sM[0]), fmaxf(fmaxf(sM[1], sM[2]), sM[3])));
        { auto r = __builtin_amdgcn_permlane16_swap(f2u(mx), f2u(mx), false, false); mx = __builtin_amdgcn_fmed3f(u2f(r[0]), u2f(r[1]), pinf); }
        { auto r = __builtin_amdgcn_permlane32_swap(f2u(mx), f2u(mx), false, false); mx = __builtin_amdgcn_fmed3f(u2f(r[0]), u2f(r[1]), pinf); }
        sA -= mx; sB -= mx; sM -= mx;
#pragma unroll
        for (int r = 0; r < 4; ++r) { sA[r] = __builtin_amdgcn_exp2f(sA[r]); sB[r] = __builtin_amdgcn_exp2f(sB[r]); sM[r] = __builtin_amdgcn_exp2f(sM[r]); }
        const v4u pb = {pk2(sA[0], sA[1]), pk2(sA[2], sA[3]), pk2(sB[0], sB[1]), pk2(sB[2], sB[3])};
        const v2u pm = {pk2(sM[0], sM[1]), pk2(sM[2], sM[3])};
        f32x4 eo[NE];
#pragma unroll
        for (int e = 0; e < NE; ++e) eo[e] = mfma32(VX[e], pb, f32x4{0.f, 0.f, 0.f, 0.f});
#pragma unroll
        for (int e = 0; e < NE; ++e) eo[e] = mfma16(VM[e], pm, eo[e]);
        if (ROLE == 3 || NQK == 2) {
          const f32x4 ss = sA + sB + sM;
          INV[lane] = __builtin_amdgcn_rcpf(xadd32(xadd16((ss[0] + ss[1]) + (ss[2] + ss[3]))));
        }
        LAS u8* ah = AHF + (r16 < 8 ? (2 * NE * w + hi) * 128 + r16 * 16 + gg * 8 : 4096 + lane * 8);
#pragma unroll
        for (int e = 0; e < NE; ++e) *(LAS v2u*)(ah + e * 256) = v2u{pk2(eo[e][0], eo[e][1]), pk2(eo[e][2], eo[e][3])};
      } else if (ROLE >= 4) {
        constexpr int Q = ROLE - 4;
        kv_ready(s + 1);
        const u8* kb = p.ws + nr.frag((Q >> 1) ? OFF_VX : OFF_KX, s + 1, h) + (Q & 1) * 4096;
        const unsigned dst = lbase + 49152 + ((s + 1) & 1) * 16384 + Q * 4096;
        unsigned ar0, ar1;
        arm_addr(s, slot, Q, ar0, ar1);
        asm volatile("" : "+v"(ar0), "+v"(ar1));
        __builtin_amdgcn_sched_barrier(0);
        for (unsigned sp = 0; sp < (1u << 20); ++sp)
          if ((unsigned)__builtin_amdgcn_readfirstlane(FOK[0]) == (unsigned)(s + 1)) break;
#pragma unroll
        for (int i = 0; i < 4; ++i) dma16<true>(kb + i * 1024, lane * 16, dst + i * 1024);
        st16(rs, ar0, v4u{SENT, SENT, SENT, SENT});
        if (Q < 2) st16(rs, ar1, v4u{SENT, SENT, SENT, SENT});
      }
      PT(9);
      wgbar();
      PT(10);
      CSTAMP(7, 4);
      {
        const v4u b0 = *(LAS v4u*)(AHF + lane * 16), b1 = *(LAS v4u*)(AHF + 1024 + lane * 16);
        const f32x2 iv = *(LAS f32x2*)(INV + 2 * rp);
        acc[0] = smf64(b0, WO[0][0], acc[0], sidx); acc[1] = smf64(b0, WO[1][0], acc[1], sidx);
        acc[0] = smf64(b1, WO[0][1], acc[0], sidx); acc[1] = smf64(b1, WO[1][1], acc[1], sidx);
        const f32x2 f0 = fold32(acc[0]) * iv, f1 = fold32(acc[1]) * iv;
        const v4u pv = {canon(f2u(f0[0])), canon(f2u(f1[0])), canon(f2u(f0[1])), canon(f2u(f1[1]))};
        if (PBF) st8(rs, a_part, TAGP(v2u{canon(pk2(f0[0], f1[0])), canon(pk2(f0[1], f1[1]))})); else st16(rs, a_part, pv);
      }
      __builtin_amdgcn_sched_barrier(0);
      CSTAMP(7, 5);
      PT(11);
      const int nslot = slot == NSLOT - 1 ? 0 : slot + 1;
      if (ROLE == 7) {
        unsigned po = nr.blk(OFF_PART + (unsigned)slot * PART_SZ + (c & (PR - 1)) * 16384u, myrel) + lane * 16;
        unsigned a_mem = nr.blk(OFF_MEM + (unsigned)(s + 1) * MEM_ST + (c >> 6) * 16384u, myrel) + (c & 63) * 128 + (2 * (r16 >> 2) + g) * 16 + (r16 & 3) * 4;
        a_mem = lane < 32 ? a_mem : 0xFFFFFF00u;
        prepare(s + 1, nslot);
        asm volatile("" : "+v"(a_mem), "+v"(po));
        delay_rt(dC);
        PT(12);
        f32x4 t;
        {
          int tries = 0; bool again;
          const unsigned p8 = po - lane * 8;
          do {
            CFENCE();
            v2u pq[8];
#pragma unroll
            for (int i = 0; i < 8; ++i) pq[i] = ld8(rs, p8 + i * 512);
            t = f32x4{0.f, 0.f, 0.f, 0.f};
#pragma unroll
            for (int i = 0; i < 8; i += 2) t = mfma32(rsel, v4u{pq[i].x, pq[i].y, pq[i + 1].x, pq[i + 1].y}, t);
            unsigned m = 0;
#pragma unroll
            for (int i = 0; i < 8; ++i) m = max(m, max(pq[i].x, pq[i].y));
            again = (__ballot(m == SENT) != 0ull) & (tries < lim);
            ++tries;
          } while (__builtin_expect(again, 0));
          lim = tries > lim ? 0 : lim;
          CTRIES(2, tries - 1);
        }
        CSTAMP(7, 6);
        PT(13);
        st4(rs, a_mem, canon(pk2(t[0], t[1])));
      } else {
        prepare(s + 1, nslot);
        if (ROLE == 2 && j == 0 && lane == 0) __hip_atomic_store(ctrl + W_PROG + LN * h, (unsigned)(s + 1), RLX_AGENT);
        if (ROLE >= 4 || ARM_LATE) DRAIN_VM();
      }
      slot = nslot;
      PT(14);
      wgbar();
      CSTAMP(7, 7);
      return true;
    };
    step(IC<1>{}, 0);
    for (int s = 1; step(IC<0>{}, s); ++s) {}
    if (lim == 0 && lane == 0) __hip_atomic_store(ctrl + W_TMO, 1u, RLX_AGENT);
  };
  switch (w) {
    case 0: case 1: run(IC<0>{}); break;
    case 2: run(IC<2>{}); break;
    case 3: run(IC<3>{}); break;
    case 4: run(IC<4>{}); break;
    case 5: run(IC<5>{}); break;
    case 6: run(IC<6>{}); break;
    default: run(IC<7>{}); break;
  }
}

enum { M_K = 0, M_V = 1, M_O = 2 };

template <int BN, int MODE, int WM = 2>
__device__ __forceinline__ void gemm_tile(LAS u8* L, const u16* A, int lda, const u16* W, int ldw, int nkt, f32x4 (&acc)[8 / WM][BN * WM / 128], const Near* nr = nullptr) {
  constexpr int MT = 8 / WM, NT = BN * WM / 128, NCG = BN / 64, STG = 16384 + BN * 128, NB = BN / 64;
  constexpr int NSTG = LDS_BYTES / STG;
  const int tid = threadIdx.x, lane = tid & 63, w = __builtin_amdgcn_readfirstlane(tid >> 6);
  const int wm = w / (8 / WM), wn = w % (8 / WM), r16 = lane & 15, g = lane >> 4, q = r16 >> 2, pp = lane & 3;
  const int arow = 16 * w + (lane >> 3);
  const unsigned avo0 = (arow * lda + 8 * ((lane & 7) ^ ((arow >> 1) & 7))) * 2;
  const unsigned avo1 = ((arow + 8) * lda + 8 * ((lane & 7) ^ (((arow + 8) >> 1) & 7))) * 2;
  const int kr = 8 * w + (lane >> 3);
  const int krs = MODE == M_O ? ((kr & ~31) + 16 * ((kr >> 2) & 1) + 4 * ((kr >> 3) & 3) + (kr & 3)) : kr;
  const int fsw = ((kr >> 1) & 1) | (((kr >> 3) & 1) << 1);
  const unsigned wvo = (krs * ldw + 16 * (((lane & 7) >> 1) ^ fsw) + 8 * (lane & 1)) * 2;
  const unsigned lb = (unsigned)(size_t)L;
  auto piece = [&](int kt, int buf, int i) {
    const unsigned sb = lb + buf * STG;
    if (i >= 2) dma16<false>(W + (size_t)kt * 64 * ldw + 64 * (i - 2), wvo, sb + 16384 + (w * NB + i - 2) * 1024);
    else if (MODE == M_O) {
      const u8* ab = (const u8*)A + (unsigned)__builtin_amdgcn_readfirstlane(nr->frag(OFF_AX, lda + (w >> 1), kt >> 1)) + (w & 1) * 4096 + (kt & 1) * 2048;
      dma16<false>(ab + i * 1024, lane * 16, sb + (2 * w + i) * 1024);
    } else dma16<false>(A + kt * 64, i ? avo1 : avo0, sb + (2 * w + i) * 1024);
  };
  const unsigned aoff = lb + (16 * MT * wm + r16) * 128 + ((g ^ ((r16 >> 1) & 7)) * 16);
  const int fr = ((q >> 1) & 1) | ((g & 1) << 1);
  const int cgw = (wn * NT) >> 2, c0 = (wn * NT) & 3;
  unsigned boff[NT];
#pragma unroll
  for (int n = 0; n < NT; ++n) boff[n] = lb + 16384 + (g * NCG + cgw) * 1024 + q * 128 + (((c0 + n) ^ fr) * 32) + pp * 8;
#pragma unroll
  for (int m = 0; m < MT; ++m)
#pragma unroll
    for (int n = 0; n < NT; ++n) acc[m][n] = f32x4{0.f, 0.f, 0.f, 0.f};

  v4u af[2][MT], bf[2][NT];
  auto frags = [&](int buf, int ks) {
    const unsigned so = buf * STG;
#pragma unroll
    for (int n = 0; n < NT; ++n) {
      const v2u x0 = trread(boff[n] + so + ks * 4 * NCG * 1024), x1 = trread(boff[n] + so + ks * 4 * NCG * 1024 + 512);
      bf[ks][n] = v4u{x0.x, x0.y, x1.x, x1.y};
    }
#pragma unroll
    for (int m = 0; m < MT; ++m)
      af[ks][m] = MODE == M_O ? *(LAS v4u*)(size_t)(lb + so + wm * 8192 + lane * 16 + m * 2048 + ks * 1024) : *(LAS v4u*)(size_t)(((aoff + so) ^ (ks * 64)) + m * 2048);
    __builtin_amdgcn_sched_barrier(0);
  };
  auto mma = [&](int ks, bool fill, int kt, int buf) {
#pragma unroll
    for (int i = 0; i < MT * NT; ++i) {
      const int m = i / NT, n = i % NT;
      acc[m][n] = MODE == M_V ? mfma32(af[ks][m], bf[ks][n], acc[m][n]) : mfma32(bf[ks][n], af[ks][m], acc[m][n]);
      constexpr int EV = MT * NT >= 2 * (NB + 2) ? 2 : 1;
      if (fill && i % EV == EV - 1 && i / EV < NB + 2) { __builtin_amdgcn_sched_barrier(0); piece(kt, buf, i / EV); __builtin_amdgcn_sched_barrier(0); }
    }
    __builtin_amdgcn_sched_barrier(0);
  };
  static_assert(MT * NT >= NB + 2, "every piece needs a product to hide behind");
  wgbar();
#pragma unroll
  for (int st = 0; st < NSTG; ++st)
#pragma unroll
    for (int i = 0; i < NB + 2; ++i) piece(st, st, i);
  wait_vm<(NSTG - 1) * (2 + NB)>();
  __builtin_amdgcn_s_barrier(); CFENCE();
  frags(0, 0);
  int buf = 0;
  for (int kt = 0; kt < nkt - NSTG; ++kt) {
    const int nb = buf == NSTG - 1 ? 0 : buf + 1;
    frags(buf, 1);
    mma(0, false, 0, 0);
    wait_vm<(NSTG - 2) * (2 + NB)>();
    WAIT_LGKM();
    __builtin_amdgcn_s_barrier(); CFENCE();
    frags(nb, 0);
    mma(1, true, kt + NSTG, buf);
    buf = nb;
  }
  static_for<NSTG>([&](auto ic) {
    constexpr int R = decltype(ic)::v;
    const int nb = buf == NSTG - 1 ? 0 : buf + 1;
    frags(buf, 1);
    mma(0, false, 0, 0);
    if (R < NSTG - 1) {
      wait_vm<(NSTG - 2 - R > 0 ? NSTG - 2 - R : 0) * (2 + NB)>();
      __builtin_amdgcn_s_barrier(); CFENCE();
      frags(nb, 0);
    }
    mma(1, false, 0, 0);
    buf = nb;
  });
}

template <int BN, int MODE, int WM = 2>
__device__ __forceinline__ void proj_task(const Params& p, LAS u8* L, rsrc_t rs, Near& nr, bool& dead, int blk, int mtx, int col0) {
  constexpr int MT = 8 / WM, NT = BN * WM / 128;
  const int tid = threadIdx.x, lane = tid & 63, w = __builtin_amdgcn_readfirstlane(tid >> 6), wm = w / (8 / WM), wn = w % (8 / WM);
  f32x4 acc[MT][NT];
  const u16* W = (mtx == 0 ? p.Wk : mtx == 1 ? p.Wv : p.Wq) + col0;
  gemm_tile<BN, MODE, WM>(L, p.x + (size_t)blk * 128 * DM, DM, W, HD, DM / 64, acc);
  const int cw = col0 + wn * 16 * NT, h = cw >> 7;
  if (BN == 64 && blk == 0) near_wait((gu32*)p.ws, nr, dead);
  const unsigned reg = mtx == 0 ? OFF_KX : mtx == 1 ? OFF_VX : OFF_QX;
  const int s0 = blk * 4 + (MT / 2) * wm;
  if (MODE == M_K) {
#pragma unroll
    for (int m = 0; m < MT; ++m) {
      const unsigned o = nr.frag(reg, s0 + (m >> 1), h) + lane * 16 + ((m & 1) * 4 + ((cw & 127) >> 5)) * 1024;
#pragma unroll
      for (int v = 0; v < NT / 2; ++v)
        st16(rs, o + v * 1024, v4u{pk2(acc[m][2 * v][0], acc[m][2 * v + 1][0]), pk2(acc[m][2 * v][1], acc[m][2 * v + 1][1]),
                                   pk2(acc[m][2 * v][2], acc[m][2 * v + 1][2]), pk2(acc[m][2 * v][3], acc[m][2 * v + 1][3])});
    }
  } else {
#pragma unroll
    for (int sg = 0; sg < MT / 2; ++sg)
#pragma unroll
      for (int n = 0; n < NT; ++n)
        st16(rs, nr.frag(reg, s0 + sg, h) + lane * 16 + (((cw & 127) >> 4) + n) * 1024,
             v4u{pk2(acc[2 * sg][n][0], acc[2 * sg][n][1]), pk2(acc[2 * sg][n][2], acc[2 * sg][n][3]),
                 pk2(acc[2 * sg + 1][n][0], acc[2 * sg + 1][n][1]), pk2(acc[2 * sg + 1][n][2], acc[2 * sg + 1][n][3])});
  }
  DRAIN_VM();
  wgbar();
  gu32* ctrl = (gu32*)p.ws;
  if (tid < BN / 64) {
    const int word = mtx == 2 ? W_FQ + blk * 64 + (col0 >> 6) + tid : W_FKV + blk * 128 + mtx * 64 + (col0 >> 6) + tid;
    __hip_atomic_store(ctrl + word, 1u, RLX_AGENT);
  }
}

__device__ __forceinline__ float sigmoidf_(float v) { return 1.0f / (1.0f + __expf(-v)); }

__device__ __forceinline__ void rg_task(const Params& p, rsrc_t rs, int wk) {
  const int tid = threadIdx.x, lane = tid & 63, w = __builtin_amdgcn_readfirstlane(tid >> 6);
  v4u wr[2], gw[2];
#pragma unroll
  for (int i = 0; i < 2; ++i) { wr[i] = *(const GAS v4u*)(p.wr + i * 512 + lane * 8); gw[i] = *(const GAS v4u*)(p.gw + i * 512 + lane * 8); }
#pragma unroll
  for (int t = 0; t < 2; ++t) {
    const int tok = wk * 16 + 2 * w + t;
    float r = 0.f, gs = 0.f;
#pragma unroll
    for (int i = 0; i < 2; ++i) {
      const v4u xv = *(const GAS v4u*)(p.x + (size_t)tok * DM + i * 512 + lane * 8);
#pragma unroll
      for (int d = 0; d < 4; ++d) {
        r = fmaf(bflo(xv[d]), bflo(wr[i][d]), r); r = fmaf(bfhi(xv[d]), bfhi(wr[i][d]), r);
        gs += bfr(bflo(xv[d]) * bflo(gw[i][d])); gs += bfr(bfhi(xv[d]) * bfhi(gw[i][d]));
      }
    }
    r = bfr(wave_sum(r)); gs = bfr(wave_sum(gs));
    if (lane == 0) { st4(rs, OFF_R + tok * 4, f2u(r)); st4(rs, OFF_G + tok * 4, f2u(bfr(sigmoidf_(gs)))); }
  }
  DRAIN_VM();
  wgbar();
  if (tid == 0) __hip_atomic_fetch_add((gu32*)p.ws + W_RG, 1u, RLX_AGENT);
}

__device__ __forceinline__ unsigned long long top_key(unsigned bits, int idx) {
  const unsigned o = (bits & 0x7FFFFFFFu) > 0x7F800000u ? 0u : ((bits & 0x80000000u) ? ~bits : (bits | 0x80000000u));
  return ((unsigned long long)o << 32) | (unsigned)(0x7FFFFFFF - idx);
}
__device__ __forceinline__ void top_ins(unsigned long long k, unsigned long long& a, unsigned long long& b) {
  const unsigned long long lo = k < a ? k : a;
  a = k < a ? a : k;
  b = lo > b ? lo : b;
}
__device__ __forceinline__ void top2(rsrc_t rs, LAS u8* L, int& i0, int& i1) {
  const int tid = threadIdx.x, lane = tid & 63, w = tid >> 6;
  const v4u rv = ld16(rs, OFF_R + tid * 16);
  unsigned long long a = 0ull, b = 0ull;
#pragma unroll
  for (int d = 0; d < 4; ++d) top_ins(top_key(rv[d], tid * 4 + d), a, b);
#pragma unroll
  for (int o = 1; o < 64; o <<= 1) {
    const unsigned long long c0 = __shfl_xor(a, o), c1 = __shfl_xor(b, o);
    top_ins(c0, a, b); top_ins(c1, a, b);
  }
  LAS unsigned long long* tv = (LAS unsigned long long*)L;
  wgbar();
  if (lane == 0) { tv[2 * w] = a; tv[2 * w + 1] = b; }
  wgbar();
  a = b = 0ull;
#pragma unroll
  for (int k = 0; k < 16; ++k) top_ins(tv[k], a, b);
  wgbar();
  i0 = (0x7FFFFFFF - (int)(unsigned)a) & 2047; i1 = (0x7FFFFFFF - (int)(unsigned)b) & 2047;
}

__device__ __forceinline__ void mlp_task(const Params& p, LAS u8* L, rsrc_t rs, int t, bool& dead) {
  int tid = threadIdx.x;
  asm volatile("" : "+v"(tid));
  gu32* ctrl = (gu32*)p.ws;
  {
    const int rg = tid >> 3, cq = tid & 7;
    v4u gv[16], uv[16];
#pragma unroll
    for (int i = 0; i < 16; ++i) {
      gv[i] = *(const GAS v4u*)(p.wg + (size_t)(rg + 64 * i) * FF + 64 * t + 8 * cq);
      uv[i] = *(const GAS v4u*)(p.wu + (size_t)(rg + 64 * i) * FF + 64 * t + 8 * cq);
    }
    __builtin_amdgcn_sched_barrier(0);
    wait_ge(ctrl, W_RG, NWORK, 0x40u, dead);
    int i0, i1;
    top2(rs, L, i0, i1);
    if (t == 0 && tid == 0) {
      const float r0 = u2f(ld4(rs, OFF_R + i0 * 4)), r1 = u2f(ld4(rs, OFF_R + i1 * 4));
      st16(rs, W_TOP * 4, v4u{(unsigned)i0, (unsigned)i1, f2u(bfr(sigmoidf_(r0))), f2u(bfr(sigmoidf_(r1)))});
    }
    LAS float* X = (LAS float*)(L + 69632);
#pragma unroll
    for (int k = 0; k < 4; ++k) X[tid + 512 * k] = bf2f(p.x[(size_t)(k < 2 ? i0 : i1) * DM + tid + 512 * (k & 1)]);
    wgbar();
    float ag[2][8], au[2][8];
#pragma unroll
    for (int e = 0; e < 8; ++e) { ag[0][e] = ag[1][e] = au[0][e] = au[1][e] = 0.f; }
#pragma unroll
    for (int i = 0; i < 16; ++i) {
      const float x0 = X[rg + 64 * i], x1 = X[1024 + rg + 64 * i];
#pragma unroll
      for (int d = 0; d < 4; ++d) {
        ag[0][2 * d] = fmaf(x0, bflo(gv[i][d]), ag[0][2 * d]); ag[0][2 * d + 1] = fmaf(x0, bfhi(gv[i][d]), ag[0][2 * d + 1]);
        ag[1][2 * d] = fmaf(x1, bflo(gv[i][d]), ag[1][2 * d]); ag[1][2 * d + 1] = fmaf(x1, bfhi(gv[i][d]), ag[1][2 * d + 1]);
        au[0][2 * d] = fmaf(x0, bflo(uv[i][d]), au[0][2 * d]); au[0][2 * d + 1] = fmaf(x0, bfhi(uv[i][d]), au[0][2 * d + 1]);
        au[1][2 * d] = fmaf(x1, bflo(uv[i][d]), au[1][2 * d]); au[1][2 * d + 1] = fmaf(x1, bfhi(uv[i][d]), au[1][2 * d + 1]);
      }
    }
    LAS float* R = (LAS float*)L;
#pragma unroll
    for (int e = 0; e < 8; ++e) {
      R[(0 * 64 + rg) * 65 + 8 * cq + e] = ag[0][e]; R[(1 * 64 + rg) * 65 + 8 * cq + e] = ag[1][e];
      R[(2 * 64 + rg) * 65 + 8 * cq + e] = au[0][e]; R[(3 * 64 + rg) * 65 + 8 * cq + e] = au[1][e];
    }
  }
  const int kc = t >> 2, cb = t & 3, rg = tid >> 5, cq = tid & 31;
  v4u dv[16];
#pragma unroll
  for (int i = 0; i < 16; ++i) dv[i] = *(const GAS v4u*)(p.wd + (size_t)(256 * kc + rg + 16 * i) * DM + 256 * cb + 8 * cq);
  __builtin_amdgcn_sched_barrier(0);
  wgbar();
  if (tid < 128) {
    const LAS float* R = (const LAS float*)L;
    const int tok = tid >> 6, col = tid & 63;
    float gs = 0.f, us = 0.f;
#pragma unroll 16
    for (int r = 0; r < 64; ++r) { gs += R[(tok * 64 + r) * 65 + col]; us += R[((2 + tok) * 64 + r) * 65 + col]; }
    gs = bfr(gs); us = bfr(us);
    const float act = bfr(gs * sigmoidf_(gs));
    st4(rs, OFF_HID + (tok * FF + 64 * t + col) * 4, f2u(bfr(act * us)));
  }
  DRAIN_VM();
  wgbar();
  if (tid == 0) __hip_atomic_store(ctrl + W_M1 + t * LN, 1u, RLX_AGENT);
  if (tid < 64) wait_lines(ctrl, W_M1 + 4 * kc * LN, LN, 4, 1u, tid, 0x41u, dead);
  wgbar();
  LAS float* Hh = (LAS float*)(L + 69632);
  Hh[tid] = u2f(ld4(rs, OFF_HID + ((tid >> 8) * FF + 256 * kc + (tid & 255)) * 4));
  wgbar();
  float a[2][8];
#pragma unroll
  for (int e = 0; e < 8; ++e) a[0][e] = a[1][e] = 0.f;
#pragma unroll
  for (int i = 0; i < 16; ++i) {
    const float h0 = Hh[rg + 16 * i], h1 = Hh[256 + rg + 16 * i];
#pragma unroll
    for (int d = 0; d < 4; ++d) {
      a[0][2 * d] = fmaf(h0, bflo(dv[i][d]), a[0][2 * d]); a[0][2 * d + 1] = fmaf(h0, bfhi(dv[i][d]), a[0][2 * d + 1]);
      a[1][2 * d] = fmaf(h1, bflo(dv[i][d]), a[1][2 * d]); a[1][2 * d + 1] = fmaf(h1, bfhi(dv[i][d]), a[1][2 * d + 1]);
    }
  }
  LAS float* R2 = (LAS float*)L;
#pragma unroll
  for (int e = 0; e < 8; ++e) { R2[(0 * 16 + rg) * 257 + 8 * cq + e] = a[0][e]; R2[(1 * 16 + rg) * 257 + 8 * cq + e] = a[1][e]; }
  wgbar();
  {
    const int tok = tid >> 8, col = tid & 255;
    float sacc = 0.f;
#pragma unroll
    for (int r = 0; r < 16; ++r) sacc += R2[(tok * 16 + r) * 257 + col];
    st4(rs, OFF_YP + ((kc * 2 + tok) * DM + 256 * cb + col) * 4, f2u(sacc));
  }
  DRAIN_VM();
  wgbar();
  if (tid == 0) __hip_atomic_store(ctrl + W_M2 + t * LN, 1u, RLX_AGENT);
}

__device__ __forceinline__ void seg_softmax(const v4u (&F)[4], const v4u (&KX)[2][4], const v4u (&QX)[4], int tq, int g, v4u& pb, v2u& pm) {
  const float scale = 0.08838834764831845f, l2e = 1.4426950408889634f, neg = bfr(-1000000000.0f);
  f32x4 sA = {0.f, 0.f, 0.f, 0.f}, sB = sA, sM = sA;
#pragma unroll
  for (int u = 0; u < 4; ++u) { sM = mfma32(F[u], QX[u], sM); sA = mfma32(KX[0][u], QX[u], sA); sB = mfma32(KX[1][u], QX[u], sB); }
#pragma unroll
  for (int r = 0; r < 4; ++r) {
    sA[r] = 4 * g + r > tq ? neg : bfr(bfr(sA[r]) * scale);
    sB[r] = 16 + 4 * g + r > tq ? neg : bfr(bfr(sB[r]) * scale);
    sM[r] = g < 2 ? -INFINITY : bfr(bfr(sM[r]) * scale);
  }
  float mx = fmaxf(fmaxf(fmaxf(sA[0], sA[1]), fmaxf(sA[2], sA[3])), fmaxf(fmaxf(sB[0], sB[1]), fmaxf(sB[2], sB[3])));
  mx = fmaxf(mx, fmaxf(fmaxf(sM[0], sM[1]), fmaxf(sM[2], sM[3])));
  mx = xmax32(xmax16(mx));
  float sum = 0.f;
#pragma unroll
  for (int r = 0; r < 4; ++r) {
    sA[r] = __builtin_amdgcn_exp2f((sA[r] - mx) * l2e); sB[r] = __builtin_amdgcn_exp2f((sB[r] - mx) * l2e); sM[r] = __builtin_amdgcn_exp2f((sM[r] - mx) * l2e);
    sum += sA[r] + sB[r] + sM[r];
  }
  const float inv = 1.0f / xadd32(xadd16(sum));
  pb = v4u{pk2(sA[0] * inv, sA[1] * inv), pk2(sA[2] * inv, sA[3] * inv), pk2(sB[0] * inv, sB[1] * inv), pk2(sB[2] * inv, sB[3] * inv)};
  pm = v2u{pk2(sM[0] * inv, sM[1] * inv), pk2(sM[2] * inv, sM[3] * inv)};
}

__device__ __forceinline__ void attn_task(const Params& p, rsrc_t rs, const Near& nr, int s, int h, bool& dead) {
  const int lane = threadIdx.x & 63, r16 = lane & 15, g = lane >> 4, gg = g & 1;
  gu32* ctrl = (gu32*)p.ws;
  const int blk = s >> 2;
  wait_flags2(ctrl, W_FKV + blk * 128 + 2 * h, W_FKV + blk * 128 + 64 + 2 * h, 0x50u, dead);
  wait_flags2(ctrl, W_FQ + blk * 64 + 2 * h, W_FQ + blk * 64 + 2 * h, 0x51u, dead);
  CFENCE();
  const unsigned fk = nr.frag(OFF_KX, s, h) + lane * 16, fq = nr.frag(OFF_QX, s, h) + lane * 16, fv = nr.frag(OFF_VX, s, h) + lane * 16;
  v4u KX[2][4], QX[2][4], VX[8], F[4]; v2u VM[8];
#pragma unroll
  for (int i = 0; i < 8; ++i) { KX[i >> 2][i & 3] = ld16(rs, fk + i * 1024); QX[i >> 2][i & 3] = ld16(rs, fq + i * 1024); VX[i] = ld16(rs, fv + i * 1024); }
  wait_ge(ctrl, W_PROG + LN * h, (unsigned)(s + 1), 0x52u, dead);
  const unsigned qo = nr.blk(OFF_QKV + (unsigned)(s * QH + (h & (QH - 1))) * 16384u, nr.rel(h * 4));
  for (unsigned sp = 0;;) {
    CFENCE();
    unsigned m = 0;
#pragma unroll
    for (int u = 0; u < 4; ++u) { F[u] = ld16(rs, qo + u * 1024 + lane * 16); m = max(m, umax4(F[u])); }
#pragma unroll
    for (int e = 0; e < 8; ++e) { VM[e] = ld8(rs, qo + 4096 + (16 * e + r16) * 16 + gg * 8); m = max(m, max(VM[e].x, VM[e].y)); }
    if (__all(m != SENT) || dead) break;
    if (spin_fail(sp, ctrl, 0x53u)) { dead = true; break; }
  }
#pragma unroll
  for (int qt = 0; qt < 2; ++qt) {
    v4u pb; v2u pm;
    seg_softmax(F, KX, QX[qt], 16 * qt + r16, g, pb, pm);
    const unsigned ao = nr.frag(OFF_AX, s, h) + lane * 16 + qt * 4096;
#pragma unroll
    for (int v = 0; v < 4; ++v) {
      f32x4 o0 = mfma32(VX[2 * v], pb, f32x4{0.f, 0.f, 0.f, 0.f}), o1 = mfma32(VX[2 * v + 1], pb, f32x4{0.f, 0.f, 0.f, 0.f});
      o0 = mfma16(VM[2 * v], pm, o0); o1 = mfma16(VM[2 * v + 1], pm, o1);
      st16(rs, ao + v * 1024, v4u{pk2(o0[0], o0[1]), pk2(o0[2], o0[3]), pk2(o1[0], o1[1]), pk2(o1[2], o1[3])});
    }
  }
  DRAIN_VM();
}

__device__ __forceinline__ void mix_store(const Params& p, rsrc_t rs, const v4u& top, int tok, int col, f32x4 a) {
  const float gt = u2f(ld4(rs, OFF_G + tok * 4)), og = bfr(1.0f - gt);
  const int sel = tok == (int)top.x ? 0 : tok == (int)top.y ? 1 : -1;
  const v2u xv = *(const GAS v2u*)(p.x + (size_t)tok * DM + col);
  float bv[4] = {bflo(xv.x), bfhi(xv.x), bflo(xv.y), bfhi(xv.y)};
  if (sel >= 0) {
    f32x4 y = {0.f, 0.f, 0.f, 0.f};
    for (int kc = 0; kc < 14; ++kc) y += __builtin_bit_cast(f32x4, ld16(rs, OFF_YP + ((kc * 2 + sel) * DM + col) * 4));
    const float rsel = u2f(sel == 0 ? top.z : top.w);
#pragma unroll
    for (int r = 0; r < 4; ++r) bv[r] = bfr(bv[r] + bfr(bfr(y[r]) * rsel));
  }
  float o[4];
#pragma unroll
  for (int r = 0; r < 4; ++r) o[r] = bfr(gt * bfr(a[r])) + bfr(og * bv[r]);
  *(GAS v2u*)(p.out + (size_t)tok * DM + col) = v2u{pk2(o[0], o[1]), pk2(o[2], o[3])};
}

__device__ __forceinline__ void oproj_task(const Params& p, LAS u8* L, rsrc_t rs, const Near& nr, int blk, int ct, bool& dead) {
  const int tid = threadIdx.x, lane = tid & 63, w = __builtin_amdgcn_readfirstlane(tid >> 6), wm = w >> 2, wn = w & 3, r16 = lane & 15, g = lane >> 4;
  gu32* ctrl = (gu32*)p.ws;
  if (w == 0) {
    wait_lines(ctrl, W_M2, LN, 56, 1u, lane, 0x60u, dead);
    wait_lines(ctrl, W_T2 + 8 * blk * LN, LN, 8, 1u, lane, 0x61u, dead);
    __builtin_amdgcn_fence(__ATOMIC_ACQUIRE, "agent");
    DRAIN_VM();
  }
  f32x4 acc[4][2];
  gemm_tile<128, M_O>(L, (const u16*)p.ws, blk * 4, p.Wo + ct * 128, DM, HD / 64, acc, &nr);
  const v4u top = ld16(rs, W_TOP * 4);
#pragma unroll
  for (int m = 0; m < 4; ++m)
#pragma unroll
    for (int n = 0; n < 2; ++n) mix_store(p, rs, top, blk * 128 + 64 * wm + 16 * m + r16, ct * 128 + 32 * wn + 16 * n + 4 * g, acc[m][n]);
}

__device__ __forceinline__ void stream_role(const Params& p, LAS u8* L, rsrc_t rs, const Near& nr, int set, int q, bool& dead) {
  int wst = 100; const int wk = set * 32 + q; (void)wst; (void)wk;
  const int tid = threadIdx.x, lane = tid & 63, w = __builtin_amdgcn_readfirstlane(tid >> 6), r16 = lane & 15, g = lane >> 4, gg = g & 1;
  const int ks = q >> 3, ct = q & 7, np = w & 3, kh = w >> 2;
  gu32* ctrl = (gu32*)p.ws;
  const unsigned lb = (unsigned)(size_t)L;
  v4u Wf[16][2];
  LAS u8* Wl = L + 65536 + w * 8192 + lane * 16;
  {
    const int kr = 8 * w + (lane >> 3);
    const int krs = (kr & ~31) + 16 * ((kr >> 2) & 1) + 4 * ((kr >> 3) & 3) + (kr & 3);
    const int fsw = ((kr >> 1) & 1) | (((kr >> 3) & 1) << 1);
    const unsigned wvo = (krs * DM + 16 * (((lane & 7) >> 1) ^ fsw) + 8 * (lane & 1)) * 2;
    const int qq = r16 >> 2, fr = ((qq >> 1) & 1) | ((g & 1) << 1);
    const unsigned boff = lb + kh * 65536 + (g * 2 + (np >> 1)) * 1024 + qq * 128 + (lane & 3) * 8;
    const unsigned b0 = boff + (((2 * (np & 1)) ^ fr) * 32), b1 = boff + (((2 * (np & 1) + 1) ^ fr) * 32);
    static_for<2>([&](auto rc) {
      constexpr int RD = decltype(rc)::v;
      wgbar();
#pragma unroll
      for (int kt = 0; kt < 8; ++kt) {
        const u16* wb = p.Wo + (size_t)(1024 * ks + 512 * (kt >> 2) + 64 * (4 * RD + (kt & 3))) * DM + 128 * ct;
        dma16<false>(wb, wvo, lb + kt * 16384 + (w * 2) * 1024);
        dma16<false>(wb + 64, wvo, lb + kt * 16384 + (w * 2 + 1) * 1024);
      }
      DRAIN_VM();
      wgbar();
#pragma unroll
      for (int i = 0; i < 8; ++i) {
        const unsigned o = (i >> 1) * 16384 + (i & 1) * 8192;
        const v2u x0 = trread(b0 + o), x1 = trread(b0 + o + 512), y0 = trread(b1 + o), y1 = trread(b1 + o + 512);
        Wf[8 * RD + i][0] = v4u{x0.x, x0.y, x1.x, x1.y}; Wf[8 * RD + i][1] = v4u{y0.x, y0.y, y1.x, y1.y};
      }
    });
  }
  wgbar();
#pragma unroll
  for (int i = 0; i < 8; ++i) *(LAS v4u*)(Wl + i * 1024) = Wf[12 + (i >> 1)][i & 1];
  wait_lines(ctrl, W_M2, LN, 56, 1u, lane, 0x70u, dead);
  CFENCE();
  const v4u top = ld16(rs, W_TOP * 4);

  auto run = [&](auto role) {
    constexpr bool ATT = decltype(role)::v == 0;
    const int qt = w & 1, eh = w >> 1;
    v4u KX[2][4], QX[4], VX[4];
    auto prefetch = [&](int s) {
      if ((s & 3) < 2) {
        const int kind = (lane >> 1) % 3;
        gu32* f = ctrl + (kind == 2 ? W_FQ + (s >> 2) * 64 : W_FKV + (s >> 2) * 128 + kind * 64) + 2 * q + (lane & 1);
        for (unsigned sp = 0; !dead;) {
          if (__all(__hip_atomic_load(f, RLX_AGENT) != 0u)) break;
          __builtin_amdgcn_s_sleep(8);
          if (spin_fail(sp, ctrl, 0x73u)) dead = true;
        }
        CFENCE();
      }
      const unsigned fk = nr.frag(OFF_KX, s, q) + lane * 16, fq = nr.frag(OFF_QX, s, q) + lane * 16, fv = nr.frag(OFF_VX, s, q) + lane * 16;
#pragma unroll
      for (int i = 0; i < 8; ++i) KX[i >> 2][i & 3] = ld16(rs, fk + i * 1024);
#pragma unroll
      for (int i = 0; i < 4; ++i) { QX[i] = ld16(rs, fq + qt * 4096 + i * 1024); VX[i] = ld16(rs, fv + (4 * eh + i) * 1024); }
    };
    auto finish = [&](int s) {
      const int f = 4 * ks + (w & 3);
      wait_lines(ctrl, W_SP + (set * 32 + ct) * LN, 8 * LN, 4, (unsigned)(s + 1), lane, 0x72u, dead);
      CFENCE();
      f32x4 sum = {0.f, 0.f, 0.f, 0.f};
#pragma unroll
      for (int k = 0; k < 8; ++k) sum += __builtin_bit_cast(f32x4, ld16(rs, nr.frag(OFF_PS, 0, (((s & 7) * 8 + ct) * 8 + k) * 2 + (f >> 3)) + (f & 7) * 1024 + lane * 16));
      mix_store(p, rs, top, s * 32 + 16 * (f & 1) + r16, ct * 128 + 16 * (f >> 1) + 4 * g, sum);
    };
    unsigned prog = 0;
    if (ATT) prefetch(NBULK * 4 + set);
    for (int s = NBULK * 4 + set; s < NSEG; s += 2) {
      const int si = s - NBULK * 4;
      if (ATT) {
        const unsigned need = (unsigned)(s + 1 < NSEG - 1 ? s + 1 : NSEG - 1);
        if (prog < need) {
          for (unsigned sp = 0; !dead;) {
            prog = __builtin_amdgcn_readfirstlane(__hip_atomic_load(ctrl + W_PROG + LN * q, RLX_AGENT));
            if (prog >= need) break;
            __builtin_amdgcn_s_sleep(2);
            if (spin_fail(sp, ctrl, 0x52u)) dead = true;
          }
        }
        const unsigned qo = nr.blk(OFF_QKV + (unsigned)(s * QH + (q & (QH - 1))) * 16384u, nr.rel(q * 4));
        v4u F[4]; v2u VM[4];
        for (unsigned sp = 0;;) {
          CFENCE();
          unsigned m = 0;
#pragma unroll
          for (int u = 0; u < 4; ++u) { F[u] = ld16(rs, qo + u * 1024 + lane * 16); m = max(m, umax4(F[u])); }
#pragma unroll
          for (int e = 0; e < 4; ++e) { VM[e] = ld8(rs, qo + 4096 + (16 * (4 * eh + e) + r16) * 16 + gg * 8); m = max(m, max(VM[e].x, VM[e].y)); }
          if (__all(m != SENT) || dead) break;
          if (spin_fail(sp, ctrl, 0x53u)) { dead = true; break; }
        }
        v4u pb; v2u pm;
        seg_softmax(F, KX, QX, 16 * qt + r16, g, pb, pm);
        const unsigned ao = nr.frag(OFF_AX, s, q) + qt * 4096 + eh * 2048 + lane * 16;
#pragma unroll
        for (int v = 0; v < 2; ++v) {
          f32x4 o0 = mfma32(VX[2 * v], pb, f32x4{0.f, 0.f, 0.f, 0.f}), o1 = mfma32(VX[2 * v + 1], pb, f32x4{0.f, 0.f, 0.f, 0.f});
          o0 = mfma16(VM[2 * v], pm, o0); o1 = mfma16(VM[2 * v + 1], pm, o1);
          st16(rs, ao + v * 1024, v4u{pk2(o0[0], o0[1]), pk2(o0[2], o0[3]), pk2(o1[0], o1[1]), pk2(o1[2], o1[3])});
        }
        DRAIN_VM();
      } else {
        if (si >= 4) finish(s - 4);
        DRAIN_VM();
      }
      wgbar();
      WSTAMP();
      if (tid == 0) __hip_atomic_store(ctrl + W_SA + (set * 32 + q) * LN, (unsigned)(s + 1), RLX_AGENT);
      if (tid == 64 && si >= 2) __hip_atomic_store(ctrl + W_SP + (set * 32 + q) * LN, (unsigned)(s - 1), RLX_AGENT);
      if (w == 4) wait_lines(ctrl, W_SA + (set * 32 + ks * 8) * LN, LN, 8, (unsigned)(s + 1), lane, 0x71u, dead);
      wgbar();
      WSTAMP();
      {
        const unsigned ao = nr.frag(OFF_AX, s, 8 * ks + w) + lane * 16;
        v4u t[8];
#pragma unroll
        for (int i = 0; i < 8; ++i) t[i] = ld16(rs, ao + i * 1024);
#pragma unroll
        for (int i = 0; i < 8; ++i) *(LAS v4u*)(L + w * 8192 + i * 1024 + lane * 16) = t[i];
      }
      if (ATT && s + 2 < NSEG) prefetch(s + 2);
      wgbar();
      f32x4 a[2][2];
#pragma unroll
      for (int i = 0; i < 4; ++i) a[i >> 1][i & 1] = f32x4{0.f, 0.f, 0.f, 0.f};
#pragma unroll
      for (int k = 0; k < 16; ++k) {
        const LAS u8* fp = L + kh * 32768 + (k >> 2) * 8192 + (k & 3) * 1024 + lane * 16;
        const v4u x0 = *(const LAS v4u*)fp, x1 = *(const LAS v4u*)(fp + 4096);
#pragma unroll
        for (int n = 0; n < 2; ++n) {
          const v4u wf = k < 12 ? Wf[k][n] : *(const LAS v4u*)(Wl + ((k - 12) * 2 + n) * 1024);
          a[n][0] = mfma32(wf, x0, a[n][0]); a[n][1] = mfma32(wf, x1, a[n][1]);
        }
      }
      WSTAMP();
      const unsigned pb = nr.frag(OFF_PS, 0, (((s & 7) * 8 + ct) * 8 + 2 * ks + kh) * 2 + (np >> 1)) + 4 * (np & 1) * 1024 + lane * 16;
#pragma unroll
      for (int i = 0; i < 4; ++i) st16(rs, pb + i * 1024, __builtin_bit_cast(v4u, a[i >> 1][i & 1]));
      WSTAMP();
    }
    DRAIN_VM();
    wgbar();
    if (tid == 0) __hip_atomic_store(ctrl + W_SP + (set * 32 + q) * LN, (unsigned)(NSEG - 1 + set), RLX_AGENT);
    finish(ATT ? NSEG - 4 + set : NSEG - 2 + set);
  };
  if (w < 4) run(IC<0>{}); else run(IC<1>{});
}

__device__ __forceinline__ void worker_role(const Params& p, LAS u8* L, rsrc_t rs, int b) {
  const int tid = threadIdx.x, lane = tid & 63, w = __builtin_amdgcn_readfirstlane(tid >> 6);
  const int x8 = 2 * (b & 3) + (b >> 7), r = (b >> 3) & 15, wk = r * 8 + x8;
  gu32* ctrl = (gu32*)p.ws;
  bool dead = false;
  int wst = 0; (void)wst;
  Near nr = near_init(p.ws);
  WSTAMP();
  {
    const int cg = x8 * 16 + r;
    for (int blk = 0; blk < 2; ++blk) {
      if (cg < 64) proj_task<64, M_K, 4>(p, L, rs, nr, dead, blk, 0, cg * 64); else proj_task<64, M_V, 4>(p, L, rs, nr, dead, blk, 1, (cg - 64) * 64);
      WSTAMP();
      if (blk == 0) WSTAMP();
    }
  }
  rg_task(p, rs, wk);
  WSTAMP();
  for (int rnd = 0; rnd < 7; ++rnd) {
    const int ct = x8 * 4 + (r & 3);
    if (rnd < 3 || rnd == 6) {
      if (rnd == 6 && r >= 8) break;
      const int blk = rnd < 3 ? 2 + rnd * 4 + (r >> 2) : 14 + (r >> 2);
      if (ct < 16) proj_task<256, M_K>(p, L, rs, nr, dead, blk, 0, ct * 256); else proj_task<256, M_V>(p, L, rs, nr, dead, blk, 1, (ct - 16) * 256);
    } else if (rnd == 3) {
      if (r < 7) mlp_task(p, L, rs, r * 8 + x8, dead);
    } else {
      const int qct = x8 * 2 + (r & 1), blk = (r >> 3) * 8 + (rnd - 4) * 4 + ((r >> 1) & 3);
      proj_task<256, M_K>(p, L, rs, nr, dead, blk, 2, qct * 256);
    }
    WSTAMP();
  }
  if (r < 8) {
    const int blk = x8, m = r;
    for (int i = 0; i < 2; ++i) { const int t = (i * 8 + m) * 8 + w; attn_task(p, rs, nr, blk * 4 + (t >> 5), t & 31, dead); WSTAMP(); }
    wgbar();
    if (tid == 0) __hip_atomic_store(ctrl + W_T2 + (8 * blk + m) * LN, 1u, RLX_AGENT);
    oproj_task(p, L, rs, nr, blk, m, dead);
    WSTAMP();
  } else {
    const int sid = x8 * 8 + (r - 8);
    stream_role(p, L, rs, nr, sid >> 5, sid & 31, dead);
    WSTAMP();
  }
}

__global__ void __launch_bounds__(512) rmt_init_kernel(u8* ws, const u16* mem0) {
  const int tid = threadIdx.x;
  if (blockIdx.x >= 4) {
    const Near n = near_init(ws);
    arm_share(__builtin_amdgcn_make_buffer_rsrc(ws, 0, WS_END, 0x00020000), n.al, blockIdx.x - 4, tid);
    return;
  }
  if (blockIdx.x >= 2) {
    const Near n = near_init(ws);
    const int i = (blockIdx.x - 2) * 512 + tid, cb = i >> 3, row = i & 7;
    const u16* m0 = mem0 + row * DM + (cb >> 2) * 32 + (cb & 3) * 4;
    const v2u lo = *(const GAS v2u*)m0, hi = *(const GAS v2u*)(m0 + 16);
    const v4u v = {canon(__builtin_amdgcn_perm(hi.x, lo.x, 0x05040100u)), canon(__builtin_amdgcn_perm(hi.x, lo.x, 0x07060302u)),
                   canon(__builtin_amdgcn_perm(hi.y, lo.y, 0x05040100u)), canon(__builtin_amdgcn_perm(hi.y, lo.y, 0x07060302u))};
    u8* d = ws + n.al + OFF_MEM + (cb >> 6) * 16384 + (cb & 63) * 128 + row * 16;
    *(GAS v4u*)d = v; *(GAS v4u*)(d + 8192) = v;
    return;
  }
  if (blockIdx.x == 0) {
    for (int i = tid; i < (int)(CTRL_BYTES / 16); i += 512)
      if (i != W_CFG / 4) ((GAS v4u*)ws)[i] = v4u{0u, 0u, 0u, 0u};
  } else if (tid == 0) {
    const Near n = near_init(ws);
    gu32* a0 = (gu32*)(ws + n.al + OFF_CAL); gu32* a1 = a0 + 2048;
    unsigned long long b0 = ~0ull, b1 = ~0ull;
    for (int i = 0; i < 3; ++i) {
      const unsigned long long t0 = __builtin_amdgcn_s_memtime();
      __hip_atomic_store(a0, 0u, RLX_AGENT); DRAIN_VM();
      const unsigned long long t1 = __builtin_amdgcn_s_memtime();
      __hip_atomic_store(a1, 0u, RLX_AGENT); DRAIN_VM();
      const unsigned long long t2 = __builtin_amdgcn_s_memtime();
      b0 = t1 - t0 < b0 ? t1 - t0 : b0; b1 = t2 - t1 < b1 ? t2 - t1 : b1;
    }
    const unsigned nz = ((b1 < b0 ? 1u : 0u) ^ ((n.va21 + OFF_CAL) >> 21) ^ (xcc_id() >> 2)) & 1u;
    ((GAS v4u*)ws)[W_CFG / 4] = v4u{nz, 0u, 0u, 0u};
  }
}

__global__ void __launch_bounds__(NTHR, 2) rmt_mod_kernel(Params p) {
  extern __shared__ __attribute__((aligned(16))) u8 smem[];
  LAS u8* L = (LAS u8*)smem;
  const rsrc_t rs = __builtin_amdgcn_make_buffer_rsrc(p.ws, 0, WS_END, 0x00020000);
  const int b = blockIdx.x;
  if ((b & 4) == 0) chain_role(p, L, rs, b); else worker_role(p, L, rs, b);
}

}

extern "C" void kernel_launch(void* const* d_in, const int* in_sizes, int n_in, void* d_out, int out_size, void* d_ws, size_t ws_size,
                              hipStream_t stream) {
  (void)in_sizes; (void)n_in; (void)out_size; (void)ws_size;
  Params p;
  p.x = (const u16*)d_in[0]; p.mem0 = (const u16*)d_in[1]; p.Wq = (const u16*)d_in[2]; p.Wk = (const u16*)d_in[3];
  p.Wv = (const u16*)d_in[4]; p.Wo = (const u16*)d_in[5]; p.wr = (const u16*)d_in[8]; p.wg = (const u16*)d_in[9];
  p.wu = (const u16*)d_in[10]; p.wd = (const u16*)d_in[11]; p.gw = (const u16*)d_in[12];
  p.out = (u16*)d_out; p.ws = (u8*)d_ws;
  (void)hipFuncSetAttribute((const void*)rmt_mod_kernel, hipFuncAttributeMaxDynamicSharedMemorySize, LDS_BYTES);
  rmt_init_kernel<<<4 + NCHAIN, 512, 0, stream>>>(p.ws, p.mem0);
  rmt_mod_kernel<<<NWG, NTHR, LDS_BYTES, stream>>>(p);
}
```

```cpp
#include <hip/hip_runtime.h>
#include <hip/hip_bf16.h>
#include <stdint.h>

#define GAS __attribute__((address_space(1)))
#define LAS __attribute__((address_space(3)))
typedef unsigned v4u __attribute__((ext_vector_type(4)));
typedef unsigned v2u __attribute__((ext_vector_type(2)));
typedef unsigned v8u __attribute__((ext_vector_type(8)));
typedef float f32x4 __attribute__((ext_vector_type(4)));
typedef __bf16 bfx8 __attribute__((ext_vector_type(8)));
typedef __bf16 bfx16 __attribute__((ext_vector_type(16)));
typedef short s16x4 __attribute__((ext_vector_type(4)));
typedef GAS unsigned gu32;
typedef unsigned short u16;
typedef unsigned char u8;
#define RLX_AGENT __ATOMIC_RELAXED, __HIP_MEMORY_SCOPE_AGENT

#define PBF 1
#define SPLIT_XCD 2
namespace {

constexpr int SEQ = 2048, DM = 1024, NH = 32, HD = 4096, NSEG = 64, FF = 3584;
constexpr int NCHAIN = 128, NWORK = 128, NWG = 256, NTHR = 512;
constexpr unsigned SENT = 0xFFFFFFFFu;
constexpr int AUX_SC1 = 16;

constexpr unsigned CTRL_BYTES = 77824;
constexpr int LN = 32;
constexpr int W_TMO = 0, W_TOP = 3 * LN  , W_CFG = 4 * LN  ,
              W_XCC = 5 * LN, W_PROG = 8 * LN  , W_FKV = 64 * LN  , W_FQ = 128 * LN  ,
              W_RG = 160 * LN  , W_M2 = 288 * LN  , W_T2 = 344 * LN  ,
              W_SA = 408 * LN  , W_SP = 472 * LN  , W_M1 = 536 * LN  ;
constexpr unsigned OFF_MEM = 98304;
constexpr unsigned MEM_ST = 32768;
constexpr unsigned OFF_QKV = OFF_MEM + NSEG * MEM_ST;
constexpr unsigned QH = 32, PR = 128;
constexpr unsigned OFF_PART = OFF_QKV + NSEG * QH * 16384;
constexpr unsigned PART_SZ = PR * 16384;
#define NSLOT 3
constexpr unsigned OFF_ARM_END = OFF_PART + NSLOT * PART_SZ;
constexpr unsigned OFF_CAL = OFF_ARM_END;
constexpr unsigned OFF_KX = OFF_CAL + 16384 + 16384;
#define WFAR 1
constexpr unsigned FB = WFAR ? 16384 : 8192;
constexpr unsigned OFF_VX = OFF_KX + NSEG * NH * FB;
constexpr unsigned OFF_QX = OFF_VX + NSEG * NH * FB;
constexpr unsigned OFF_AX = OFF_QX + NSEG * NH * FB;
constexpr unsigned OFF_R = OFF_AX + NSEG * NH * FB + 1024 * FB + 16384;
constexpr unsigned OFF_G = OFF_R + SEQ * 4;
constexpr unsigned OFF_HID = OFF_G + SEQ * 4;
constexpr unsigned OFF_YP = OFF_HID + 2 * FF * 4;
constexpr unsigned OFF_PS = OFF_AX + NSEG * NH * FB;
constexpr unsigned WS_END = OFF_YP + 14 * 2 * DM * 4;
constexpr int NBULK = 8;
constexpr int LDS_BYTES = 160 * 1024;
constexpr unsigned SPIN_CAP = 1u << 18;

#define TAGP(...) (__VA_ARGS__)
#define BG_DELAY 16
#define ARM_LATE 0
#define TLB_WARM 0
#define NQK 4
#define FOK_AT 4
#define ROT_A 0
#define ROT_B 0
#define ROT_O 0
#define W_A 300
#define W_B 120
#define W_C 240

struct Params {
  const u16 *x, *mem0, *Wq, *Wk, *Wv, *Wo, *wr, *wg, *wu, *wd, *gw;
  u16* out;
  u8* ws;
};

__device__ __forceinline__ float u2f(unsigned u) { return __builtin_bit_cast(float, u); }
__device__ __forceinline__ unsigned f2u(float f) { return __builtin_bit_cast(unsigned, f); }
__device__ __forceinline__ float bf2f(unsigned b) { return u2f(b << 16); }
__device__ __forceinline__ float bflo(unsigned d) { return u2f(d << 16); }
__device__ __forceinline__ float bfhi(unsigned d) { return u2f(d & 0xFFFF0000u); }
__device__ __forceinline__ unsigned pk2(float lo, float hi) {
  typedef __bf16 bfx2 __attribute__((ext_vector_type(2)));
  bfx2 v; v.x = (__bf16)lo; v.y = (__bf16)hi;
  return __builtin_bit_cast(unsigned, v);
}
__device__ __forceinline__ float bfr(float f) { return (float)(__bf16)f; }
__device__ __forceinline__ unsigned canon(unsigned d) { return min(d, 0xFFFFFFFEu); }
__device__ __forceinline__ f32x4 mfma32(v4u a, v4u b, f32x4 c) {
  return __builtin_amdgcn_mfma_f32_16x16x32_bf16(__builtin_bit_cast(bfx8, a), __builtin_bit_cast(bfx8, b), c, 0, 0, 0);
}
__device__ __forceinline__ f32x4 mfma16(v2u a, v2u b, f32x4 c) { return mfma32(v4u{a.x, a.y, 0u, 0u}, v4u{b.x, b.y, 0u, 0u}, c); }
__device__ __forceinline__ f32x4 smf64(v4u a, v8u b, f32x4 c, int idx) {
  return __builtin_amdgcn_smfmac_f32_16x16x64_bf16(__builtin_bit_cast(bfx8, a), __builtin_bit_cast(bfx16, b), c, idx, 0, 0);
}
__device__ __forceinline__ v2u trread(unsigned lds_addr) {
  return __builtin_bit_cast(v2u, __builtin_amdgcn_ds_read_tr16_b64_v4i16((LAS s16x4*)(size_t)lds_addr));
}
typedef float f32x2 __attribute__((ext_vector_type(2)));
__device__ __forceinline__ f32x2 fold32(f32x4 v) {
  const auto r0 = __builtin_amdgcn_permlane32_swap(f2u(v[0]), f2u(v[2]), false, false);
  const auto r1 = __builtin_amdgcn_permlane32_swap(f2u(v[1]), f2u(v[3]), false, false);
  return f32x2{u2f(r0[0]), u2f(r1[0])} + f32x2{u2f(r0[1]), u2f(r1[1])};
}
__device__ __forceinline__ float xmax16(float a) { auto r = __builtin_amdgcn_permlane16_swap(f2u(a), f2u(a), false, false); return __builtin_amdgcn_fmed3f(u2f(r[0]), u2f(r[1]), INFINITY); }
__device__ __forceinline__ float xmax32(float a) { auto r = __builtin_amdgcn_permlane32_swap(f2u(a), f2u(a), false, false); return __builtin_amdgcn_fmed3f(u2f(r[0]), u2f(r[1]), INFINITY); }
__device__ __forceinline__ float xadd16(float a) { auto r = __builtin_amdgcn_permlane16_swap(f2u(a), f2u(a), false, false); return u2f(r[0]) + u2f(r[1]); }
__device__ __forceinline__ float xadd32(float a) { auto r = __builtin_amdgcn_permlane32_swap(f2u(a), f2u(a), false, false); return u2f(r[0]) + u2f(r[1]); }

__device__ __forceinline__ float wave_sum(float v) {
  v += __shfl_xor(v, 1); v += __shfl_xor(v, 2); v += __shfl_xor(v, 4); v += __shfl_xor(v, 8);
  return xadd32(xadd16(v));
}
__device__ __forceinline__ int fresh_tid() { int t = threadIdx.x; asm volatile("" : "+v"(t)); return t; }
#define CFENCE() asm volatile("" ::: "memory")
#define DRAIN_VM() asm volatile("s_waitcnt vmcnt(0)" ::: "memory")
#define WAIT_LGKM() asm volatile("s_waitcnt lgkmcnt(0)" ::: "memory")
__device__ __forceinline__ void wgbar() { WAIT_LGKM(); __builtin_amdgcn_s_barrier(); CFENCE(); }

typedef __amdgpu_buffer_rsrc_t rsrc_t;
__device__ __forceinline__ v4u ld16(rsrc_t rs, unsigned off) { return __builtin_amdgcn_raw_buffer_load_b128(rs, off, 0, AUX_SC1); }
__device__ __forceinline__ v2u ld8(rsrc_t rs, unsigned off) { return __builtin_amdgcn_raw_buffer_load_b64(rs, off, 0, AUX_SC1); }
__device__ __forceinline__ unsigned ld4(rsrc_t rs, unsigned off) { return __builtin_amdgcn_raw_buffer_load_b32(rs, off, 0, AUX_SC1); }
__device__ __forceinline__ void st16(rsrc_t rs, unsigned off, v4u v) { __builtin_amdgcn_raw_buffer_store_b128(v, rs, off, 0, AUX_SC1); }
__device__ __forceinline__ void st8(rsrc_t rs, unsigned off, v2u v) { __builtin_amdgcn_raw_buffer_store_b64(v, rs, off, 0, AUX_SC1); }
__device__ __forceinline__ void st4(rsrc_t rs, unsigned off, unsigned v) { __builtin_amdgcn_raw_buffer_store_b32(v, rs, off, 0, AUX_SC1); }
__device__ __forceinline__ unsigned umax4(v4u v) { return max(max(v.x, v.y), max(v.z, v.w)); }

template <int N> __device__ __forceinline__ void delay_cyc() {
#pragma unroll
  for (int i = 0; i < N / 64; ++i) asm volatile("s_nop 15");
  if ((N % 64) >= 4) asm volatile("s_nop %0" :: "i"((N % 64) / 4 - 1));
}

#define PACE 0
#define PACE_GAIN 0.3f
#define PACE_CLAMP 2.0f
#define GAP_A 6
#define DB3 5
#define EXTRA_B 0
#define EXTRA_C 0
#define GAP_C 6
#define AD_DEC 0
#define AD_INC 0
__device__ __forceinline__ void delay_rt(int n) {
  asm volatile("s_cmp_lt_i32 %0, 1\n\ts_cbranch_scc1 2f\n\ts_mov_b32 vcc_lo, %0\n1:\n\ts_nop 1\n\ts_sub_i32 vcc_lo, vcc_lo, 1\n\ts_cmp_gt_i32 vcc_lo, 0\n\ts_cbranch_scc1 1b\n2:" :: "s"(n) : "vcc", "scc");
}
__device__ __forceinline__ void adapt(int& n, int misses) { n = misses ? min(n + AD_INC, 120) : max(n - AD_DEC, 0); }

__device__ __forceinline__ bool spin_fail(unsigned& spins, gu32* ctrl, unsigned code) {
  ++spins;
  if (spins > SPIN_CAP) { __hip_atomic_store(ctrl + W_TMO, 1u, RLX_AGENT); __hip_atomic_fetch_max(ctrl + W_TMO + 1, code, RLX_AGENT); return true; }
  return (spins & 255u) == 0u && __builtin_amdgcn_readfirstlane(__hip_atomic_load(ctrl + W_TMO, RLX_AGENT)) != 0u;
}
constexpr int RETRY_CAP = 1 << 15;
__device__ __forceinline__ void wait_ge(gu32* ctrl, int word, unsigned want, unsigned code, bool& dead) {
  if (dead) return;
  for (unsigned sp = 0;;) {
    if ((unsigned)__builtin_amdgcn_readfirstlane(__hip_atomic_load(ctrl + word, RLX_AGENT)) >= want) return;
    __builtin_amdgcn_s_sleep(4);
    if (spin_fail(sp, ctrl, code)) { dead = true; return; }
  }
}
__device__ __forceinline__ void wait_flags2(gu32* ctrl, int w0, int w1, unsigned code, bool& dead) {
  if (dead) return;
  for (unsigned sp = 0;;) {
    const unsigned long long a = __hip_atomic_load((GAS unsigned long long*)(ctrl + w0), RLX_AGENT);
    const unsigned long long b = __hip_atomic_load((GAS unsigned long long*)(ctrl + w1), RLX_AGENT);
    const unsigned long long m = a & b;
    if (__builtin_amdgcn_readfirstlane((unsigned)m & (unsigned)(m >> 32)) != 0u) return;
    __builtin_amdgcn_s_sleep(4);
    if (spin_fail(sp, ctrl, code)) { dead = true; return; }
  }
}
__device__ __forceinline__ void wait_lines(gu32* ctrl, int word, int stride, int n, unsigned want, int lane, unsigned code, bool& dead) {
  if (dead) return;
  gu32* mine = ctrl + word + (lane < n ? lane : 0) * stride;
  for (unsigned sp = 0;;) {
    if (__all(__hip_atomic_load(mine, RLX_AGENT) >= want)) return;
    __builtin_amdgcn_s_sleep(2);
    if (spin_fail(sp, ctrl, code)) { dead = true; return; }
  }
}
constexpr int ARM_AHEAD = 4;
__device__ __forceinline__ void arm_share(rsrc_t rs, unsigned al, int c, int tid) {
  const v4u s = {SENT, SENT, SENT, SENT};
  const unsigned lo = (tid & 63) * 16, w = tid >> 6;
  constexpr int NB = PR / 64;
  for (int sl = 0; sl < NSLOT; ++sl)
#pragma unroll
    for (int k = 0; k < NB; ++k) if (!PBF || w < 4) st16(rs, al + OFF_PART + sl * PART_SZ + (c * NB + k) * 8192 + w * 1024 + lo, s);
  if (w < 6) {
#pragma unroll
    for (int k = 0; k < NB; ++k) st16(rs, al + OFF_QKV + (c * NB + k) * 8192 + w * 1024 + lo, s);
  } else if (w == 6) st16(rs, al + OFF_MEM + MEM_ST + c * 1024 + lo, s);
  else if (c < 32) st16(rs, al + OFF_MEM + 5 * MEM_ST + c * 1024 + lo, s);
}
struct Near {
  unsigned al, va21, nz, k;
  __device__ __forceinline__ unsigned rel(int c) const { return k >> 2; }
  __device__ __forceinline__ unsigned frag(unsigned reg, int s, int h) const { return WFAR ? blk(reg + (unsigned)(s * NH + h) * FB, (k >> 2) ^ 1u) : reg + (unsigned)(s * NH + h) * FB; }
  __device__ __forceinline__ unsigned blk(unsigned pair_off, unsigned iod) const { return al + pair_off + ((((va21 + pair_off) >> 21) ^ nz ^ iod) & 1u) * 8192u; }
};
__device__ __forceinline__ Near near_init(const u8* ws) {
  Near n; const unsigned lo = (unsigned)(size_t)ws;
  n.al = (0u - lo) & 16383u; n.va21 = lo + n.al; n.nz = 0; n.k = 0; return n;
}
__device__ __forceinline__ unsigned xcc_id() { return (unsigned)__builtin_amdgcn_s_getreg(20 | (3 << 11)) & 7u; }
__device__ __forceinline__ void near_wait(gu32* ctrl, Near& n, bool& dead) {
  wait_ge(ctrl, W_XCC, 16u, 0x11u, dead);
  n.k = __builtin_amdgcn_readfirstlane(__hip_atomic_load(ctrl + W_XCC, RLX_AGENT)) & 7u;
  n.nz = __builtin_amdgcn_readfirstlane(__hip_atomic_load(ctrl + W_CFG, RLX_AGENT)) & 1u;
}

#define CSTAMP(wv, slot)
#define PT(id)
#define CTRIES(slot, n)
#define WSTAMP()
template <bool SC1>
__device__ __forceinline__ void dma16(const void* base, unsigned voff, unsigned lds_addr) {
  unsigned keep;
  if (SC1) asm volatile("s_mov_b32 %0, m0\n\ts_mov_b32 m0, %3\n\ts_nop 0\n\tglobal_load_lds_dwordx4 %1, %2 sc1\n\ts_mov_b32 m0, %0" : "=&s"(keep) : "v"(voff), "s"(base), "s"(lds_addr) : "memory");
  else asm volatile("s_mov_b32 %0, m0\n\ts_mov_b32 m0, %3\n\ts_nop 0\n\tglobal_load_lds_dwordx4 %1, %2\n\ts_mov_b32 m0, %0" : "=&s"(keep) : "v"(voff), "s"(base), "s"(lds_addr) : "memory");
}

template <int N> __device__ __forceinline__ void wait_vm() { static_assert(N >= 0 && N < 64, "vmcnt"); asm volatile("s_waitcnt vmcnt(%0)" :: "i"(N) : "memory"); }
template <int V> struct IC { static constexpr int v = V; };
template <int N, int I = 0, class F> __device__ __forceinline__ void static_for(F&& f) { if constexpr (I < N) { f(IC<I>{}); static_for<N, I + 1>(f); } }
__device__ __forceinline__ void slice_dma(const u16* src, int ld, unsigned dst, int lane) {
  const unsigned vo = ((lane >> 2) * ld + 8 * (lane & 3)) * 2;
#pragma unroll
  for (int p = 0; p < 8; ++p) dma16<false>(src + (size_t)16 * p * ld, vo, dst + p * 1024);
}
template <bool PERM>
__device__ __forceinline__ void slice_frags(unsigned base, int lane, v8u (&f)[2][2]) {
  const int gB = lane >> 4, q = (lane & 15) >> 2, p = lane & 3;
#pragma unroll
  for (int cc = 0; cc < 2; ++cc)
#pragma unroll
    for (int rr = 0; rr < 4; ++rr) {
      const int kp = 64 * cc + 32 * (rr >> 1) + 16 * (gB >> 1) + 4 * (gB & 1) + 2 * (rr & 1) + (q & 1) + 8 * (q >> 1);
      const int row = PERM ? ((kp & ~31) + 16 * (kp & 1) + ((kp & 31) >> 1)) : kp;
#pragma unroll
      for (int tb = 0; tb < 2; ++tb) {
        const v2u v = trread(base + row * 64 + tb * 32 + p * 8);
        f[tb][cc][2 * rr] = v.x; f[tb][cc][2 * rr + 1] = v.y;
      }
    }
}

__device__ __forceinline__ void chain_role(const Params& p, LAS u8* L, rsrc_t rs, int b) {
  const int tid = threadIdx.x, lane = tid & 63, w = __builtin_amdgcn_readfirstlane(tid >> 6);
  const int c = (b & 3) * 32 + (b >> 3);
  const int h = c >> 2, j = c & 3;
  const int dbgi = (c >> 4) + 8 * (c & 15); (void)dbgi;
  const int r16 = lane & 15, g = lane >> 4;
  gu32* ctrl = (gu32*)p.ws;
  bool dead = false;
  const unsigned lbase = (unsigned)(size_t)L;
  Near nr = near_init(p.ws);
  if (b == 0 && tid == 0) __hip_atomic_store(ctrl + W_XCC, 16u | xcc_id(), RLX_AGENT);

  v8u W1[3][2][2];
  v8u WO[2][2];
  {
    const unsigned mine = lbase + w * 16384;
    const size_t wcol = (size_t)h * 128 + 32 * j;
    slice_dma(p.Wk + (size_t)128 * w * HD + wcol, HD, mine, lane);
    slice_dma(p.Wv + (size_t)128 * w * HD + wcol, HD, mine + 8192, lane);
    asm volatile("s_waitcnt vmcnt(8)" ::: "memory");
    slice_frags<true>(lbase + w * 16384, lane, W1[1]);
    WAIT_LGKM(); __builtin_amdgcn_sched_barrier(0);
    slice_dma(p.Wq + (size_t)128 * w * HD + wcol, HD, mine, lane);
    asm volatile("s_waitcnt vmcnt(8)" ::: "memory");
    slice_frags<true>(lbase + w * 16384 + 8192, lane, W1[2]);
    WAIT_LGKM(); __builtin_amdgcn_sched_barrier(0);
    DRAIN_VM();
    slice_frags<true>(lbase + w * 16384, lane, W1[0]);
    WAIT_LGKM(); __builtin_amdgcn_sched_barrier(0);
  }
  near_wait(ctrl, nr, dead);
  const unsigned myrel = nr.rel(c);

  LAS float* RED = (LAS float*)L;
  LAS u8* AHF = L + 24576;
  volatile LAS unsigned* FOK = (volatile LAS unsigned*)(L + 31744);
  LAS float* INV = (LAS float*)(L + 30976);
  LAS u8* KVL = L + 49152;
  const int sidx = r16 < 8 ? 0x4444 : 0xEEEE;
  const int gg = g & 1, hi = g >> 1, rp = 2 * gg + hi;
  int gone = dead ? 1 : 0;

  auto fetch_kv_issue = [&](int s, int lo, int hi) {
    if ((s & 3) == 0) { bool d = gone != 0; const int blk = s >> 2; wait_flags2(ctrl, W_FKV + blk * 128 + 2 * h, W_FKV + blk * 128 + 64 + 2 * h, 0x20u, d); gone = __builtin_amdgcn_readfirstlane(d ? 1 : 0); }
    CFENCE();
    const unsigned dst = lbase + 49152 + (s & 1) * 16384;
#pragma unroll
    for (int i = lo; i < hi; ++i) dma16<true>(p.ws + nr.frag((i >> 3) ? OFF_VX : OFF_KX, s, h) + (i & 7) * 1024, lane * 16, dst + i * 1024);
  };
  auto kv_ready = [&](int s) {
    if ((s & 3) == 0) { bool d = gone != 0; const int blk = s >> 2; wait_flags2(ctrl, W_FKV + blk * 128 + 2 * h, W_FKV + blk * 128 + 64 + 2 * h, 0x20u, d); gone = __builtin_amdgcn_readfirstlane(d ? 1 : 0); CFENCE(); }
  };
  auto arm_addr = [&](int s, int slot, int q, unsigned& a0, unsigned& a1) {
    a0 = s > 0 ? nr.blk(OFF_PART + (unsigned)(slot == 0 ? 2 : slot - 1) * PART_SZ + (c & (PR - 1)) * 16384u, myrel) + q * 1024 + lane * 16 : 0xFFFFFF00u;
    const unsigned qn = nr.blk(OFF_QKV + (unsigned)((s + ARM_AHEAD) * QH + (h & (QH - 1))) * 16384u, myrel);
    a1 = q == 0 ? qn + j * 1024 + lane * 16
                : lane < 32 ? qn + 4096 + j * 512 + lane * 16
                            : (lane < 48 && s + ARM_AHEAD + 2 < NSEG) ? nr.al + OFF_MEM + (unsigned)(s + ARM_AHEAD + 2) * MEM_ST + (c >> 6) * 16384u + ((lane >> 3) & 1) * 8192u + (c & 63) * 128 + (lane & 7) * 16
                                                                      : 0xFFFFFF00u;
    a1 = s + ARM_AHEAD < NSEG ? a1 : 0xFFFFFF00u;
  };
  if (tid == 0) { FOK[0] = 0u; FOK[1] = 0u; }
  int dA = W_A / 20, dB = W_B / 20, dC = W_C / 20;
  wgbar();

  auto run = [&](auto role) {
    constexpr int ROLE = decltype(role)::v;
    LAS f32x2* redw = (LAS f32x2*)(RED + w * 768) + (rp * 16 + r16) * 2;
    const int R = 32 * j + 4 * w + (r16 >> 2);
    const unsigned rrel = nr.rel(R), part_w = (R & (PR - 1)) * 16384u, part_l = PBF ? h * 128 + rp * 32 + (r16 & 3) * 8 : h * 256 + rp * 64 + (r16 & 3) * 16;
    const unsigned pub_l = ROLE == 0 ? j * 1024 + (8 * w + 2 * g + (r16 & 1) + 16 * (r16 >> 2)) * 16 + (r16 & 2) * 4 : 4096 + (32 * j + r16) * 16 + g * 4;
    unsigned qo, a_part, mo, a_pub;
    float pinf = INFINITY, mi = g < 2 ? -INFINITY : 0.f;
    asm volatile("" : "+v"(pinf), "+v"(mi));
    const f32x4 minit = {mi, mi, mi, mi};
    const unsigned s1 = (r16 & 3) == 0 ? 0x00003F80u : (r16 & 3) == 1 ? 0x3F800000u : 0u;
    const v4u rsel = {(r16 & 4) ? 0u : s1, (r16 & 4) ? s1 : 0u, (r16 & 4) ? 0u : s1, (r16 & 4) ? s1 : 0u};
    int slot = 0, lim = gone ? 0 : RETRY_CAP;
    auto prepare = [&](int s, int sl) {
      qo = nr.blk(OFF_QKV + (unsigned)(s * QH + (h & (QH - 1))) * 16384u, myrel);
      a_part = nr.blk(OFF_PART + (unsigned)sl * PART_SZ + part_w, rrel) + part_l;
      mo = nr.blk(OFF_MEM + (unsigned)s * MEM_ST + (w >> 2) * 16384u, myrel) + (w & 3) * 2048 + lane * 16;
      a_pub = qo + pub_l;
      asm volatile("" : "+v"(a_part), "+v"(mo), "+v"(a_pub));
    };
    prepare(0, 0);
    auto step = [&](auto first, int s) -> bool {
      constexpr bool FIRST = decltype(first)::v != 0;
      const unsigned par = (unsigned)(s / 3) & 1u; (void)par;
      PT(0);
      delay_rt(dA);
      PT(1);
      f32x4 acc[6];
      {
        int tries = 0; bool again;
        do {
          CFENCE();
          const v4u a0 = ld16(rs, mo), a1 = ld16(rs, mo + 1024);
          if (FIRST && tries == 0) slice_dma(p.Wo + (size_t)128 * h * DM + 256 * j + 32 * w, DM, lbase + 98304 + w * 8192, lane);
#pragma unroll
          for (int t = 0; t < 6; ++t) { acc[t] = f32x4{0.f, 0.f, 0.f, 0.f}; asm volatile("" : "+v"(acc[t])); }
          __builtin_amdgcn_sched_barrier(0);
#pragma unroll
          for (int t = 0; t < 6; ++t) acc[t] = smf64(a0, W1[t >> 1][t & 1][0], acc[t], sidx);
          __builtin_amdgcn_sched_barrier(0);
#pragma unroll
          for (int t = 0; t < 6; ++t) acc[t] = smf64(a1, W1[t >> 1][t & 1][1], acc[t], sidx);
          again = (__ballot(max(umax4(a0), umax4(a1)) == SENT) != 0ull) & (tries < lim);
          ++tries;
        } while (__builtin_expect(again, 0));
        lim = tries > lim ? 0 : lim;
        if (w == 0) CTRIES(0, tries - 1);
      }
      CSTAMP(0, 0);
      PT(2);
#pragma unroll
      for (int t = 0; t < 6; ++t) redw[(t >> 1) * 128 + (t & 1)] = fold32(acc[t]);
      PT(3);
      wgbar();
      PT(4);
      CSTAMP(0, 1);
      if (ROLE <= 2) {
        const LAS f32x4* redr = (const LAS f32x4*)RED + w * 64 + lane;
        f32x4 t0 = redr[0], t1 = redr[192];
#pragma unroll
        for (int k = 2; k < 8; k += 2) { t0 += redr[k * 192]; t1 += redr[(k + 1) * 192]; }
        t0 += t1;
        if (ROLE == 0) {
          const unsigned d0 = pk2(t0[0], t0[2]), d1 = pk2(t0[1], t0[3]);
          const unsigned got = __builtin_amdgcn_mov_dpp((r16 & 1) ? d0 : d1, 0xB1, 0xF, 0xF, true);
          st8(rs, a_pub, (r16 & 1) ? v2u{canon(got), canon(d1)} : v2u{canon(d0), canon(got)});
        } else {
          st4(rs, a_pub, canon(pk2(t0[0], t0[1])));
          st4(rs, a_pub + 256, canon(pk2(t0[2], t0[3])));
        }
      }
      CSTAMP(0, 2);
      PT(5);
      if (!FIRST && s == NSEG - 1) return false;
      if (FIRST) {
        if (ROLE >= 4) fetch_kv_issue(0, 4 * (ROLE - 4), 4 * (ROLE - 4) + 4);
        DRAIN_VM();
        slice_frags<false>(lbase + 98304 + w * 8192, lane, WO);
        wgbar();
      }
      acc[0] = f32x4{0.f, 0.f, 0.f, 0.f}; acc[1] = acc[0];
      asm volatile("" : "+v"(acc[0]), "+v"(acc[1]));
      if (ROLE < NQK) {
        constexpr int NE = 8 / NQK;
        v4u KX[2][4], VX[NE], F[4]; v2u VM[NE];
        const LAS u8* kv = KVL + (s & 1) * 16384 + lane * 16;
#pragma unroll
        for (int i = 0; i < 8; ++i) KX[i >> 2][i & 3] = *(const LAS v4u*)(kv + i * 1024);
#pragma unroll
        for (int e = 0; e < NE; ++e) VX[e] = *(const LAS v4u*)(kv + 8192 + (NE * w + e) * 1024);
        const unsigned fo = qo + lane * 16, vo = qo + 4096 + (16 * NE * w + r16) * 16 + gg * 8;
        __builtin_amdgcn_sched_barrier(0);
        delay_rt((ROLE == 3 ? dB + DB3 : dB));
        PT(6);
        f32x4 sA, sB, sM;
        {
          int tries = 0; bool again;
          do {
            CFENCE();
#pragma unroll
            for (int u = 0; u < 4; ++u) F[u] = ld16(rs, fo + u * 1024);
#pragma unroll
            for (int e = 0; e < NE; ++e) VM[e] = ld8(rs, vo + e * 256);
            sA = f32x4{0.f, 0.f, 0.f, 0.f}; sB = sA; sM = minit;
#pragma unroll
            for (int u = 0; u < 4; ++u) {
              sM = mfma32(F[u], F[u], sM); sA = mfma32(KX[0][u], F[u], sA); sB = mfma32(KX[1][u], F[u], sB);
              if (u == FOK_AT) { __builtin_amdgcn_sched_barrier(0); FOK[0] = (unsigned)(s + 1); __builtin_amdgcn_sched_barrier(0); }
            }
            unsigned m = max(max(umax4(F[0]), umax4(F[1])), max(umax4(F[2]), umax4(F[3])));
#pragma unroll
            for (int e = 0; e < NE; ++e) m = max(m, max(VM[e].x, VM[e].y));
            again = (__ballot(m == SENT) != 0ull) & (tries < lim);
            ++tries;
          } while (__builtin_expect(again, 0));
          lim = tries > lim ? 0 : lim;
          if (w == 0) CTRIES(1, tries - 1);
        }
        if (FOK_AT > 3) FOK[0] = (unsigned)(s + 1);
        CSTAMP(0, 3);
        PT(7);
        const float sc2 = 0.08838834764831845f * 1.4426950408889634f;
        sA *= sc2; sB *= sc2; sM *= sc2;
        float mx = fmaxf(fmaxf(fmaxf(fmaxf(sA[0], sA[1]), sA[2]), fmaxf(fmaxf(sA[3], sB[0]), sB[1])), fmaxf(fmaxf(fmaxf(sB[2], sB[3]), sM[0]), fmaxf(fmaxf(sM[1], sM[2]), sM[3])));
        { auto r = __builtin_amdgcn_permlane16_swap(f2u(mx), f2u(mx), false, false); mx = __builtin_amdgcn_fmed3f(u2f(r[0]), u2f(r[1]), pinf); }
        { auto r = __builtin_amdgcn_permlane32_swap(f2u(mx), f2u(mx), false, false); mx = __builtin_amdgcn_fmed3f(u2f(r[0]), u2f(r[1]), pinf); }
        sA -= mx; sB -= mx; sM -= mx;
#pragma unroll
        for (int r = 0; r < 4; ++r) { sA[r] = __builtin_amdgcn_exp2f(sA[r]); sB[r] = __builtin_amdgcn_exp2f(sB[r]); sM[r] = __builtin_amdgcn_exp2f(sM[r]); }
        const v4u pb = {pk2(sA[0], sA[1]), pk2(sA[2], sA[3]), pk2(sB[0], sB[1]), pk2(sB[2], sB[3])};
        const v2u pm = {pk2(sM[0], sM[1]), pk2(sM[2], sM[3])};
        f32x4 eo[NE];
#pragma unroll
        for (int e = 0; e < NE; ++e) eo[e] = mfma32(VX[e], pb, f32x4{0.f, 0.f, 0.f, 0.f});
#pragma unroll
        for (int e = 0; e < NE; ++e) eo[e] = mfma16(VM[e], pm, eo[e]);
        if (ROLE == 3 || NQK == 2) {
          const f32x4 ss = sA + sB + sM;
          INV[lane] = __builtin_amdgcn_rcpf(xadd32(xadd16((ss[0] + ss[1]) + (ss[2] + ss[3]))));
        }
        LAS u8* ah = AHF + (r16 < 8 ? (2 * NE * w + hi) * 128 + r16 * 16 + gg * 8 : 4096 + lane * 8);
#pragma unroll
        for (int e = 0; e < NE; ++e) *(LAS v2u*)(ah + e * 256) = v2u{pk2(eo[e][0], eo[e][1]), pk2(eo[e][2], eo[e][3])};
      } else if (ROLE >= 4) {
        constexpr int Q = ROLE - 4;
        kv_ready(s + 1);
        const u8* kb = p.ws + nr.frag((Q >> 1) ? OFF_VX : OFF_KX, s + 1, h) + (Q & 1) * 4096;
        const unsigned dst = lbase + 49152 + ((s + 1) & 1) * 16384 + Q * 4096;
        unsigned ar0, ar1;
        arm_addr(s, slot, Q, ar0, ar1);
        asm volatile("" : "+v"(ar0), "+v"(ar1));
        __builtin_amdgcn_sched_barrier(0);
        for (unsigned sp = 0; sp < (1u << 20); ++sp)
          if ((unsigned)__builtin_amdgcn_readfirstlane(FOK[0]) == (unsigned)(s + 1)) break;
#pragma unroll
        for (int i = 0; i < 4; ++i) dma16<true>(kb + i * 1024, lane * 16, dst + i * 1024);
        st16(rs, ar0, v4u{SENT, SENT, SENT, SENT});
        if (Q < 2) st16(rs, ar1, v4u{SENT, SENT, SENT, SENT});
      }
      PT(9);
      wgbar();
      PT(10);
      CSTAMP(7, 4);
      {
        const v4u b0 = *(LAS v4u*)(AHF + lane * 16), b1 = *(LAS v4u*)(AHF + 1024 + lane * 16);
        const f32x2 iv = *(LAS f32x2*)(INV + 2 * rp);
        acc[0] = smf64(b0, WO[0][0], acc[0], sidx); acc[1] = smf64(b0, WO[1][0], acc[1], sidx);
        acc[0] = smf64(b1, WO[0][1], acc[0], sidx); acc[1] = smf64(b1, WO[1][1], acc[1], sidx);
        const f32x2 f0 = fold32(acc[0]) * iv, f1 = fold32(acc[1]) * iv;
        const v4u pv = {canon(f2u(f0[0])), canon(f2u(f1[0])), canon(f2u(f0[1])), canon(f2u(f1[1]))};
        if (PBF) st8(rs, a_part, TAGP(v2u{canon(pk2(f0[0], f1[0])), canon(pk2(f0[1], f1[1]))})); else st16(rs, a_part, pv);
      }
      __builtin_amdgcn_sched_barrier(0);
      CSTAMP(7, 5);
      PT(11);
      const int nslot = slot == NSLOT - 1 ? 0 : slot + 1;
      if (ROLE == 7) {
        unsigned po = nr.blk(OFF_PART + (unsigned)slot * PART_SZ + (c & (PR - 1)) * 16384u, myrel) + lane * 16;
        unsigned a_mem = nr.blk(OFF_MEM + (unsigned)(s + 1) * MEM_ST + (c >> 6) * 16384u, myrel) + (c & 63) * 128 + (2 * (r16 >> 2) + g) * 16 + (r16 & 3) * 4;
        a_mem = lane < 32 ? a_mem : 0xFFFFFF00u;
        prepare(s + 1, nslot);
        asm volatile("" : "+v"(a_mem), "+v"(po));
        delay_rt(dC);
        PT(12);
        f32x4 t;
        {
          int tries = 0; bool again;
          const unsigned p8 = po - lane * 8;
          do {
            CFENCE();
            v2u pq[8];
#pragma unroll
            for (int i = 0; i < 8; ++i) pq[i] = ld8(rs, p8 + i * 512);
            t = f32x4{0.f, 0.f, 0.f, 0.f};
#pragma unroll
            for (int i = 0; i < 8; i += 2) t = mfma32(rsel, v4u{pq[i].x, pq[i].y, pq[i + 1].x, pq[i + 1].y}, t);
            unsigned m = 0;
#pragma unroll
            for (int i = 0; i < 8; ++i) m = max(m, max(pq[i].x, pq[i].y));
            again = (__ballot(m == SENT) != 0ull) & (tries < lim);
            ++tries;
          } while (__builtin_expect(again, 0));
          lim = tries > lim ? 0 : lim;
          CTRIES(2, tries - 1);
        }
        CSTAMP(7, 6);
        PT(13);
        st4(rs, a_mem, canon(pk2(t[0], t[1])));
      } else {
        prepare(s + 1, nslot);
        if (ROLE == 2 && j == 0 && lane == 0) __hip_atomic_store(ctrl + W_PROG + LN * h, (unsigned)(s + 1), RLX_AGENT);
        if (ROLE >= 4 || ARM_LATE) DRAIN_VM();
      }
      slot = nslot;
      PT(14);
      wgbar();
      CSTAMP(7, 7);
      return true;
    };
    step(IC<1>{}, 0);
    for (int s = 1; step(IC<0>{}, s); ++s) {}
    if (lim == 0 && lane == 0) __hip_atomic_store(ctrl + W_TMO, 1u, RLX_AGENT);
  };
  switch (w) {
    case 0: case 1: run(IC<0>{}); break;
    case 2: run(IC<2>{}); break;
    case 3: run(IC<3>{}); break;
    case 4: run(IC<4>{}); break;
    case 5: run(IC<5>{}); break;
    case 6: run(IC<6>{}); break;
    default: run(IC<7>{}); break;
  }
}

enum { M_K = 0, M_V = 1, M_O = 2 };

template <int BN, int MODE, int WM = 2>
__device__ __forceinline__ void gemm_tile(LAS u8* L, const u16* A, int lda, const u16* W, int ldw, int nkt, f32x4 (&acc)[8 / WM][BN * WM / 128], int rot = 0, const Near* nr = nullptr) {
  constexpr int MT = 8 / WM, NT = BN * WM / 128, NCG = BN / 64, STG = 16384 + BN * 128, NB = BN / 64;
  constexpr int NSTG = LDS_BYTES / STG;
  const int tid = fresh_tid(), lane = tid & 63, w = __builtin_amdgcn_readfirstlane(tid >> 6);
  const int wm = w / (8 / WM), wn = w % (8 / WM), r16 = lane & 15, g = lane >> 4, q = r16 >> 2, pp = lane & 3;
  const int arow = 16 * w + (lane >> 3);
  const unsigned avo0 = (arow * lda + 8 * ((lane & 7) ^ ((arow >> 1) & 7))) * 2;
  const unsigned avo1 = ((arow + 8) * lda + 8 * ((lane & 7) ^ (((arow + 8) >> 1) & 7))) * 2;
  const int kr = 8 * w + (lane >> 3);
  const int krs = MODE == M_O ? ((kr & ~31) + 16 * ((kr >> 2) & 1) + 4 * ((kr >> 3) & 3) + (kr & 3)) : kr;
  const int fsw = ((kr >> 1) & 1) | (((kr >> 3) & 1) << 1);
  const unsigned wvo = (krs * ldw + 16 * (((lane & 7) >> 1) ^ fsw) + 8 * (lane & 1)) * 2;
  const unsigned lb = (unsigned)(size_t)L;
  auto piece = [&](int kn, int buf, int i) {
    const int kt = (kn + rot) & (nkt - 1);
    const unsigned sb = lb + buf * STG;
    if (i >= 2) dma16<false>(W + (size_t)kt * 64 * ldw + 64 * (i - 2), wvo, sb + 16384 + (w * NB + i - 2) * 1024);
    else if (MODE == M_O) {
      const u8* ab = (const u8*)A + (unsigned)__builtin_amdgcn_readfirstlane(nr->frag(OFF_AX, lda + (w >> 1), kt >> 1)) + (w & 1) * 4096 + (kt & 1) * 2048;
      dma16<false>(ab + i * 1024, lane * 16, sb + (2 * w + i) * 1024);
    } else dma16<false>(A + kt * 64, i ? avo1 : avo0, sb + (2 * w + i) * 1024);
  };
  const unsigned aoff = lb + (16 * MT * wm + r16) * 128 + ((g ^ ((r16 >> 1) & 7)) * 16);
  const int fr = ((q >> 1) & 1) | ((g & 1) << 1);
  const int cgw = (wn * NT) >> 2, c0 = (wn * NT) & 3;
  unsigned boff[NT];
#pragma unroll
  for (int n = 0; n < NT; ++n) boff[n] = lb + 16384 + (g * NCG + cgw) * 1024 + q * 128 + (((c0 + n) ^ fr) * 32) + pp * 8;
#pragma unroll
  for (int m = 0; m < MT; ++m)
#pragma unroll
    for (int n = 0; n < NT; ++n) acc[m][n] = f32x4{0.f, 0.f, 0.f, 0.f};

  v4u af[2][MT], bf[2][NT];
  auto frag1 = [&](int buf, int ks, int i) {
    const unsigned so = buf * STG;
    if (i < NT) {
      const v2u x0 = trread(boff[i] + so + ks * 4 * NCG * 1024), x1 = trread(boff[i] + so + ks * 4 * NCG * 1024 + 512);
      bf[ks][i] = v4u{x0.x, x0.y, x1.x, x1.y};
    } else {
      const int m = i - NT;
      af[ks][m] = MODE == M_O ? *(LAS v4u*)(size_t)(lb + so + wm * 8192 + lane * 16 + m * 2048 + ks * 1024) : *(LAS v4u*)(size_t)(((aoff + so) ^ (ks * 64)) + m * 2048);
    }
  };
  auto mma = [&](int ks, int fb, bool fill, int kt, int buf) {
#pragma unroll
    for (int i = 0; i < MT * NT; ++i) {
      const int m = i / NT, n = i % NT;
      acc[m][n] = MODE == M_V ? mfma32(af[ks][m], bf[ks][n], acc[m][n]) : mfma32(bf[ks][n], af[ks][m], acc[m][n]);
      __builtin_amdgcn_sched_barrier(0);
      if (fb >= 0 && i < MT + NT) frag1(fb, ks ^ 1, i);
      constexpr int F0 = MT * NT - (NB + 2);
      if (fill && i >= F0) piece(kt, buf, i - F0);
      __builtin_amdgcn_sched_barrier(0);
    }
  };
  static_assert(MT * NT >= NB + 2 && MT * NT >= MT + NT, "every piece and fragment needs a product to hide behind");
  wgbar();
#pragma unroll
  for (int st = 0; st < NSTG; ++st)
#pragma unroll
    for (int i = 0; i < NB + 2; ++i) piece(st, st, i);
  wait_vm<(NSTG - 1) * (2 + NB)>();
  __builtin_amdgcn_s_barrier(); CFENCE();
#pragma unroll
  for (int i = 0; i < MT + NT; ++i) frag1(0, 0, i);
  int buf = 0;
  for (int kt = 0; kt < nkt - NSTG; ++kt) {
    const int nb = buf == NSTG - 1 ? 0 : buf + 1;
    mma(0, buf, false, 0, 0);
    wait_vm<(NSTG - 2) * (2 + NB)>();
    WAIT_LGKM();
    __builtin_amdgcn_s_barrier(); CFENCE();
    mma(1, nb, true, kt + NSTG, buf);
    buf = nb;
  }
  static_for<NSTG>([&](auto ic) {
    constexpr int R = decltype(ic)::v;
    const int nb = buf == NSTG - 1 ? 0 : buf + 1;
    mma(0, buf, false, 0, 0);
    if (R < NSTG - 1) {
      wait_vm<(NSTG - 2 - R > 0 ? NSTG - 2 - R : 0) * (2 + NB)>();
      __builtin_amdgcn_s_barrier(); CFENCE();
    }
    mma(1, R < NSTG - 1 ? nb : -1, false, 0, 0);
    buf = nb;
  });
}

template <int BN, int MODE, int WM = 2>
__device__ __forceinline__ void proj_task(const Params& p, LAS u8* L, rsrc_t rs, Near& nr, bool& dead, int blk, int mtx, int col0) {
  constexpr int MT = 8 / WM, NT = BN * WM / 128;
  const int tid = fresh_tid(), lane = tid & 63, w = __builtin_amdgcn_readfirstlane(tid >> 6), wm = w / (8 / WM), wn = w % (8 / WM);
  f32x4 acc[MT][NT];
  const u16* W = (mtx == 0 ? p.Wk : mtx == 1 ? p.Wv : p.Wq) + col0;
  gemm_tile<BN, MODE, WM>(L, p.x + (size_t)blk * 128 * DM, DM, W, HD, DM / 64, acc, BN == 256 ? ROT_A * blk + ROT_B * (col0 >> 8) : 0);
  const int cw = col0 + wn * 16 * NT, h = cw >> 7;
  if (BN == 64 && blk == 0) near_wait((gu32*)p.ws, nr, dead);
  const unsigned reg = mtx == 0 ? OFF_KX : mtx == 1 ? OFF_VX : OFF_QX;
  const int s0 = blk * 4 + (MT / 2) * wm;
  if (MODE == M_K) {
#pragma unroll
    for (int m = 0; m < MT; ++m) {
      const unsigned o = nr.frag(reg, s0 + (m >> 1), h) + lane * 16 + ((m & 1) * 4 + ((cw & 127) >> 5)) * 1024;
#pragma unroll
      for (int v = 0; v < NT / 2; ++v)
        st16(rs, o + v * 1024, v4u{pk2(acc[m][2 * v][0], acc[m][2 * v + 1][0]), pk2(acc[m][2 * v][1], acc[m][2 * v + 1][1]),
                                   pk2(acc[m][2 * v][2], acc[m][2 * v + 1][2]), pk2(acc[m][2 * v][3], acc[m][2 * v + 1][3])});
    }
  } else {
#pragma unroll
    for (int sg = 0; sg < MT / 2; ++sg)
#pragma unroll
      for (int n = 0; n < NT; ++n)
        st16(rs, nr.frag(reg, s0 + sg, h) + lane * 16 + (((cw & 127) >> 4) + n) * 1024,
             v4u{pk2(acc[2 * sg][n][0], acc[2 * sg][n][1]), pk2(acc[2 * sg][n][2], acc[2 * sg][n][3]),
                 pk2(acc[2 * sg + 1][n][0], acc[2 * sg + 1][n][1]), pk2(acc[2 * sg + 1][n][2], acc[2 * sg + 1][n][3])});
  }
  DRAIN_VM();
  wgbar();
  gu32* ctrl = (gu32*)p.ws;
  if (tid < BN / 64) {
    const int word = mtx == 2 ? W_FQ + blk * 64 + (col0 >> 6) + tid : W_FKV + blk * 128 + mtx * 64 + (col0 >> 6) + tid;
    __hip_atomic_store(ctrl + word, 1u, RLX_AGENT);
  }
}

template <int MODE>
__device__ __forceinline__ void proj_big(const Params& p, LAS u8* L, rsrc_t rs, const Near& nr, int blk, int mtx, int col0) {
  constexpr int NS = 5, STG = 32768, NK = DM / 32;
  const int tid = fresh_tid();
  const int lane = tid & 63, w = __builtin_amdgcn_readfirstlane(tid >> 6), wm = w >> 2, wn = w & 3, r16 = lane & 15, g = lane >> 4, q = r16 >> 2;
  const u16* A = p.x + (size_t)blk * 128 * DM;
  const u16* W = (mtx == 0 ? p.Wk : mtx == 1 ? p.Wv : p.Wq) + col0;
  const unsigned lb = (unsigned)(size_t)L;
  const int ar = lane >> 2;
  const unsigned avo = ((32 * w + ar) * DM + 8 * ((lane & 3) ^ ((4 - (ar >> 2)) & 3))) * 2;
  const int kr = 8 * (w >> 1) + (lane >> 3);
  const unsigned wvo = (kr * HD + 128 * (w & 1) + 16 * (((lane & 7) >> 1) ^ (((kr >> 1) & 1) | (((kr >> 3) & 1) << 1))) + 8 * (lane & 1)) * 2;
  auto piece = [&](int kt, int buf, int i) {
    const unsigned sb = lb + buf * STG;
    if (i < 2) dma16<false>(A + kt * 32 + i * 16 * DM, avo, sb + (2 * w + i) * 1024);
    else dma16<false>(W + (size_t)kt * 32 * HD + 64 * (i - 2), wvo, sb + 16384 + (2 * w + i - 2) * 1024);
  };
  const unsigned aoff = lb + (128 * wm + r16) * 64 + ((g ^ ((4 - q) & 3)) * 16);
  const int fr = ((q >> 1) & 1) | ((g & 1) << 1);
  const unsigned boff = lb + 16384 + (g * 4 + wn) * 1024 + q * 128 + (lane & 3) * 8;
  f32x4 acc[8][4];
#pragma unroll
  for (int i = 0; i < 32; ++i) acc[i >> 2][i & 3] = f32x4{0.f, 0.f, 0.f, 0.f};
  v4u af[2][8], bf[2][4];
  auto frag1 = [&](int buf, int set, int i) {
    const unsigned so = buf * STG;
    if (i < 4) {
      const v2u x0 = trread(boff + so + ((i ^ fr) * 32)), x1 = trread(boff + so + ((i ^ fr) * 32) + 512);
      bf[set][i] = v4u{x0.x, x0.y, x1.x, x1.y};
    } else af[set][i - 4] = *(LAS v4u*)(size_t)(aoff + so + (i - 4) * 1024);
  };
  auto mma = [&](int set, int nb, bool fill, int kt, int buf) {
#pragma unroll
    for (int i = 0; i < 32; ++i) {
      const int m = i >> 2, n = i & 3;
      acc[m][n] = MODE == M_V ? mfma32(af[set][m], bf[set][n], acc[m][n]) : mfma32(bf[set][n], af[set][m], acc[m][n]);
      if ((i & 1) == 0 && i < 24 && nb >= 0) { __builtin_amdgcn_sched_barrier(0); frag1(nb, set ^ 1, i >> 1); __builtin_amdgcn_sched_barrier(0); }
      if (fill && (i & 7) == 5) { __builtin_amdgcn_sched_barrier(0); piece(kt, buf, i >> 3); __builtin_amdgcn_sched_barrier(0); }
    }
    __builtin_amdgcn_sched_barrier(0);
  };
  wgbar();
#pragma unroll
  for (int st = 0; st < NS; ++st)
#pragma unroll
    for (int i = 0; i < 4; ++i) piece(st, st, i);
  wait_vm<(NS - 1) * 4>();
  __builtin_amdgcn_s_barrier(); CFENCE();
#pragma unroll
  for (int i = 0; i < 12; ++i) frag1(0, 0, i);
  int buf = 0;
  auto step = [&](int kt, int set, auto inflight, bool fill) {
    const int nb = buf == NS - 1 ? 0 : buf + 1;
    wait_vm<decltype(inflight)::v * 4>();
    WAIT_LGKM();
    __builtin_amdgcn_s_barrier(); CFENCE();
    mma(set, nb, fill, kt + NS, buf);
    buf = nb;
  };
  for (int kt = 0; kt < NK - NS - 1; kt += 2) { step(kt, 0, IC<NS - 2>{}, true); step(kt + 1, 1, IC<NS - 2>{}, true); }
  static_assert((NK - NS - 1) % 2 == 0 && NS == 5, "tail written out for these sizes");
  step(NK - 6, 0, IC<3>{}, true);
  step(NK - 5, 1, IC<3>{}, false);
  step(NK - 4, 0, IC<2>{}, false);
  step(NK - 3, 1, IC<1>{}, false);
  step(NK - 2, 0, IC<0>{}, false);
  mma(1, -1, false, 0, 0);

  const int cw = col0 + wn * 64, h = cw >> 7, s0 = blk * 4 + 4 * wm;
  const unsigned reg = mtx == 0 ? OFF_KX : mtx == 1 ? OFF_VX : OFF_QX;
#pragma unroll
  for (int sg = 0; sg < 4; ++sg) {
    const unsigned o = nr.frag(reg, s0 + sg, h) + lane * 16;
    if (MODE == M_K) {
#pragma unroll
      for (int i = 0; i < 4; ++i) {
        const int m = 2 * sg + (i >> 1), v = i & 1;
        st16(rs, o + ((i >> 1) * 4 + ((cw & 127) >> 5) + v) * 1024,
             v4u{pk2(acc[m][2 * v][0], acc[m][2 * v + 1][0]), pk2(acc[m][2 * v][1], acc[m][2 * v + 1][1]),
                 pk2(acc[m][2 * v][2], acc[m][2 * v + 1][2]), pk2(acc[m][2 * v][3], acc[m][2 * v + 1][3])});
      }
    } else {
#pragma unroll
      for (int n = 0; n < 4; ++n)
        st16(rs, o + (((cw & 127) >> 4) + n) * 1024,
             v4u{pk2(acc[2 * sg][n][0], acc[2 * sg][n][1]), pk2(acc[2 * sg][n][2], acc[2 * sg][n][3]),
                 pk2(acc[2 * sg + 1][n][0], acc[2 * sg + 1][n][1]), pk2(acc[2 * sg + 1][n][2], acc[2 * sg + 1][n][3])});
    }
  }
  DRAIN_VM();
  wgbar();
  if (tid < 8) {
    const int b2 = blk + (tid >> 2);
    const int word = (mtx == 2 ? W_FQ + b2 * 64 : W_FKV + b2 * 128 + mtx * 64) + (col0 >> 6) + (tid & 3);
    __hip_atomic_store((gu32*)p.ws + word, 1u, RLX_AGENT);
  }
}

__device__ __forceinline__ float sigmoidf_(float v) { return 1.0f / (1.0f + __expf(-v)); }

__device__ __forceinline__ void rg_task(const Params& p, rsrc_t rs, int wk) {
  const int tid = fresh_tid(), lane = tid & 63, w = __builtin_amdgcn_readfirstlane(tid >> 6);
  v4u wr[2], gw[2];
#pragma unroll
  for (int i = 0; i < 2; ++i) { wr[i] = *(const GAS v4u*)(p.wr + i * 512 + lane * 8); gw[i] = *(const GAS v4u*)(p.gw + i * 512 + lane * 8); }
#pragma unroll
  for (int t = 0; t < 2; ++t) {
    const int tok = wk * 16 + 2 * w + t;
    float r = 0.f, gs = 0.f;
#pragma unroll
    for (int i = 0; i < 2; ++i) {
      const v4u xv = *(const GAS v4u*)(p.x + (size_t)tok * DM + i * 512 + lane * 8);
#pragma unroll
      for (int d = 0; d < 4; ++d) {
        r = fmaf(bflo(xv[d]), bflo(wr[i][d]), r); r = fmaf(bfhi(xv[d]), bfhi(wr[i][d]), r);
        gs += bfr(bflo(xv[d]) * bflo(gw[i][d])); gs += bfr(bfhi(xv[d]) * bfhi(gw[i][d]));
      }
    }
    r = bfr(wave_sum(r)); gs = bfr(wave_sum(gs));
    if (lane == 0) { st4(rs, OFF_R + tok * 4, f2u(r)); st4(rs, OFF_G + tok * 4, f2u(bfr(sigmoidf_(gs)))); }
  }
  DRAIN_VM();
  wgbar();
  if (tid == 0) __hip_atomic_store((gu32*)p.ws + W_RG + wk * LN, 1u, RLX_AGENT);
}

__device__ __forceinline__ unsigned long long top_key(unsigned bits, int idx) {
  const unsigned o = (bits & 0x7FFFFFFFu) > 0x7F800000u ? 0u : ((bits & 0x80000000u) ? ~bits : (bits | 0x80000000u));
  return ((unsigned long long)o << 32) | (unsigned)(0x7FFFFFFF - idx);
}
__device__ __forceinline__ void top_ins(unsigned long long k, unsigned long long& a, unsigned long long& b) {
  const unsigned long long lo = k < a ? k : a;
  a = k < a ? a : k;
  b = lo > b ? lo : b;
}
__device__ __forceinline__ void top2(rsrc_t rs, LAS u8* L, int& i0, int& i1) {
  const int tid = fresh_tid(), lane = tid & 63, w = tid >> 6;
  const v4u rv = ld16(rs, OFF_R + tid * 16);
  unsigned long long a = 0ull, b = 0ull;
#pragma unroll
  for (int d = 0; d < 4; ++d) top_ins(top_key(rv[d], tid * 4 + d), a, b);
#pragma unroll
  for (int o = 1; o < 64; o <<= 1) {
    const unsigned long long c0 = __shfl_xor(a, o), c1 = __shfl_xor(b, o);
    top_ins(c0, a, b); top_ins(c1, a, b);
  }
  LAS unsigned long long* tv = (LAS unsigned long long*)L;
  wgbar();
  if (lane == 0) { tv[2 * w] = a; tv[2 * w + 1] = b; }
  wgbar();
  a = b = 0ull;
#pragma unroll
  for (int k = 0; k < 16; ++k) top_ins(tv[k], a, b);
  wgbar();
  i0 = (0x7FFFFFFF - (int)(unsigned)a) & 2047; i1 = (0x7FFFFFFF - (int)(unsigned)b) & 2047;
}

__device__ __forceinline__ void mlp1_task(const Params& p, LAS u8* L, rsrc_t rs, int t, bool& dead) {
  const int tid = fresh_tid();
  gu32* ctrl = (gu32*)p.ws;
  {
    const int rg = tid >> 3, cq = tid & 7;
    v4u gv[16], uv[16];
#pragma unroll
    for (int i = 0; i < 16; ++i) {
      gv[i] = *(const GAS v4u*)(p.wg + (size_t)(rg + 64 * i) * FF + 64 * t + 8 * cq);
      uv[i] = *(const GAS v4u*)(p.wu + (size_t)(rg + 64 * i) * FF + 64 * t + 8 * cq);
    }
    __builtin_amdgcn_sched_barrier(0);
    if (tid < 128) wait_lines(ctrl, W_RG + (tid >> 6) * 64 * LN, LN, 64, 1u, tid & 63, 0x40u, dead);
    wgbar();
    int i0, i1;
    top2(rs, L, i0, i1);
    if (t == 0 && tid == 0) {
      const float r0 = u2f(ld4(rs, OFF_R + i0 * 4)), r1 = u2f(ld4(rs, OFF_R + i1 * 4));
      st16(rs, W_TOP * 4, v4u{(unsigned)i0, (unsigned)i1, f2u(bfr(sigmoidf_(r0))), f2u(bfr(sigmoidf_(r1)))});
    }
    LAS float* X = (LAS float*)(L + 69632);
#pragma unroll
    for (int k = 0; k < 4; ++k) X[tid + 512 * k] = bf2f(p.x[(size_t)(k < 2 ? i0 : i1) * DM + tid + 512 * (k & 1)]);
    wgbar();
    float ag[2][8], au[2][8];
#pragma unroll
    for (int e = 0; e < 8; ++e) { ag[0][e] = ag[1][e] = au[0][e] = au[1][e] = 0.f; }
#pragma unroll
    for (int i = 0; i < 16; ++i) {
      const float x0 = X[rg + 64 * i], x1 = X[1024 + rg + 64 * i];
#pragma unroll
      for (int d = 0; d < 4; ++d) {
        ag[0][2 * d] = fmaf(x0, bflo(gv[i][d]), ag[0][2 * d]); ag[0][2 * d + 1] = fmaf(x0, bfhi(gv[i][d]), ag[0][2 * d + 1]);
        ag[1][2 * d] = fmaf(x1, bflo(gv[i][d]), ag[1][2 * d]); ag[1][2 * d + 1] = fmaf(x1, bfhi(gv[i][d]), ag[1][2 * d + 1]);
        au[0][2 * d] = fmaf(x0, bflo(uv[i][d]), au[0][2 * d]); au[0][2 * d + 1] = fmaf(x0, bfhi(uv[i][d]), au[0][2 * d + 1]);
        au[1][2 * d] = fmaf(x1, bflo(uv[i][d]), au[1][2 * d]); au[1][2 * d + 1] = fmaf(x1, bfhi(uv[i][d]), au[1][2 * d + 1]);
      }
    }
    LAS float* R = (LAS float*)L;
#pragma unroll
    for (int e = 0; e < 8; ++e) {
      R[(0 * 64 + rg) * 65 + 8 * cq + e] = ag[0][e]; R[(1 * 64 + rg) * 65 + 8 * cq + e] = ag[1][e];
      R[(2 * 64 + rg) * 65 + 8 * cq + e] = au[0][e]; R[(3 * 64 + rg) * 65 + 8 * cq + e] = au[1][e];
    }
  }
  wgbar();
  if (tid < 128) {
    const LAS float* R = (const LAS float*)L;
    const int tok = tid >> 6, col = tid & 63;
    float gs = 0.f, us = 0.f;
#pragma unroll 16
    for (int r = 0; r < 64; ++r) { gs += R[(tok * 64 + r) * 65 + col]; us += R[((2 + tok) * 64 + r) * 65 + col]; }
    gs = bfr(gs); us = bfr(us);
    const float act = bfr(gs * sigmoidf_(gs));
    st4(rs, OFF_HID + (tok * FF + 64 * t + col) * 4, f2u(bfr(act * us)));
  }
  DRAIN_VM();
  wgbar();
  if (tid == 0) __hip_atomic_store(ctrl + W_M1 + t * LN, 1u, RLX_AGENT);
}
__device__ __forceinline__ void mlp2_task(const Params& p, LAS u8* L, rsrc_t rs, int t, bool& dead) {
  const int tid = fresh_tid();
  gu32* ctrl = (gu32*)p.ws;
  const int kc = t >> 2, cb = t & 3, rg = tid >> 5, cq = tid & 31;
  v4u dv[16];
#pragma unroll
  for (int i = 0; i < 16; ++i) dv[i] = *(const GAS v4u*)(p.wd + (size_t)(256 * kc + rg + 16 * i) * DM + 256 * cb + 8 * cq);
  __builtin_amdgcn_sched_barrier(0);
  if (tid < 64) wait_lines(ctrl, W_M1 + 4 * kc * LN, LN, 4, 1u, tid, 0x41u, dead);
  wgbar();
  LAS float* Hh = (LAS float*)(L + 69632);
  Hh[tid] = u2f(ld4(rs, OFF_HID + ((tid >> 8) * FF + 256 * kc + (tid & 255)) * 4));
  wgbar();
  float a[2][8];
#pragma unroll
  for (int e = 0; e < 8; ++e) a[0][e] = a[1][e] = 0.f;
#pragma unroll
  for (int i = 0; i < 16; ++i) {
    const float h0 = Hh[rg + 16 * i], h1 = Hh[256 + rg + 16 * i];
#pragma unroll
    for (int d = 0; d < 4; ++d) {
      a[0][2 * d] = fmaf(h0, bflo(dv[i][d]), a[0][2 * d]); a[0][2 * d + 1] = fmaf(h0, bfhi(dv[i][d]), a[0][2 * d + 1]);
      a[1][2 * d] = fmaf(h1, bflo(dv[i][d]), a[1][2 * d]); a[1][2 * d + 1] = fmaf(h1, bfhi(dv[i][d]), a[1][2 * d + 1]);
    }
  }
  LAS float* R2 = (LAS float*)L;
#pragma unroll
  for (int e = 0; e < 8; ++e) { R2[(0 * 16 + rg) * 257 + 8 * cq + e] = a[0][e]; R2[(1 * 16 + rg) * 257 + 8 * cq + e] = a[1][e]; }
  wgbar();
  {
    const int tok = tid >> 8, col = tid & 255;
    float sacc = 0.f;
#pragma unroll
    for (int r = 0; r < 16; ++r) sacc += R2[(tok * 16 + r) * 257 + col];
    st4(rs, OFF_YP + ((kc * 2 + tok) * DM + 256 * cb + col) * 4, f2u(sacc));
  }
  DRAIN_VM();
  wgbar();
  if (tid == 0) __hip_atomic_store(ctrl + W_M2 + t * LN, 1u, RLX_AGENT);
}

__device__ __forceinline__ void seg_softmax(const v4u (&F)[4], const v4u (&KX)[2][4], const v4u (&QX)[4], int tq, int g, v4u& pb, v2u& pm) {
  const float scale = 0.08838834764831845f, l2e = 1.4426950408889634f, neg = bfr(-1000000000.0f);
  f32x4 sA = {0.f, 0.f, 0.f, 0.f}, sB = sA, sM = sA;
#pragma unroll
  for (int u = 0; u < 4; ++u) { sM = mfma32(F[u], QX[u], sM); sA = mfma32(KX[0][u], QX[u], sA); sB = mfma32(KX[1][u], QX[u], sB); }
#pragma unroll
  for (int r = 0; r < 4; ++r) {
    sA[r] = 4 * g + r > tq ? neg : bfr(bfr(sA[r]) * scale);
    sB[r] = 16 + 4 * g + r > tq ? neg : bfr(bfr(sB[r]) * scale);
    sM[r] = g < 2 ? -INFINITY : bfr(bfr(sM[r]) * scale);
  }
  float mx = fmaxf(fmaxf(fmaxf(sA[0], sA[1]), fmaxf(sA[2], sA[3])), fmaxf(fmaxf(sB[0], sB[1]), fmaxf(sB[2], sB[3])));
  mx = fmaxf(mx, fmaxf(fmaxf(sM[0], sM[1]), fmaxf(sM[2], sM[3])));
  mx = xmax32(xmax16(mx));
  float sum = 0.f;
#pragma unroll
  for (int r = 0; r < 4; ++r) {
    sA[r] = __builtin_amdgcn_exp2f((sA[r] - mx) * l2e); sB[r] = __builtin_amdgcn_exp2f((sB[r] - mx) * l2e); sM[r] = __builtin_amdgcn_exp2f((sM[r] - mx) * l2e);
    sum += sA[r] + sB[r] + sM[r];
  }
  const float inv = 1.0f / xadd32(xadd16(sum));
  pb = v4u{pk2(sA[0] * inv, sA[1] * inv), pk2(sA[2] * inv, sA[3] * inv), pk2(sB[0] * inv, sB[1] * inv), pk2(sB[2] * inv, sB[3] * inv)};
  pm = v2u{pk2(sM[0] * inv, sM[1] * inv), pk2(sM[2] * inv, sM[3] * inv)};
}

__device__ __forceinline__ void attn_task(const Params& p, rsrc_t rs, const Near& nr, int s, int h, bool& dead) {
  const int lane = fresh_tid() & 63, r16 = lane & 15, g = lane >> 4, gg = g & 1;
  gu32* ctrl = (gu32*)p.ws;
  const int blk = s >> 2;
  wait_flags2(ctrl, W_FKV + blk * 128 + 2 * h, W_FKV + blk * 128 + 64 + 2 * h, 0x50u, dead);
  wait_flags2(ctrl, W_FQ + blk * 64 + 2 * h, W_FQ + blk * 64 + 2 * h, 0x51u, dead);
  CFENCE();
  const unsigned fk = nr.frag(OFF_KX, s, h) + lane * 16, fq = nr.frag(OFF_QX, s, h) + lane * 16, fv = nr.frag(OFF_VX, s, h) + lane * 16;
  v4u KX[2][4], QX[2][4], VX[8], F[4]; v2u VM[8];
#pragma unroll
  for (int i = 0; i < 8; ++i) { KX[i >> 2][i & 3] = ld16(rs, fk + i * 1024); QX[i >> 2][i & 3] = ld16(rs, fq + i * 1024); VX[i] = ld16(rs, fv + i * 1024); }
  wait_ge(ctrl, W_PROG + LN * h, (unsigned)(s + 1), 0x52u, dead);
  const unsigned qo = nr.blk(OFF_QKV + (unsigned)(s * QH + (h & (QH - 1))) * 16384u, nr.rel(h * 4));
  for (unsigned sp = 0;;) {
    CFENCE();
    unsigned m = 0;
#pragma unroll
    for (int u = 0; u < 4; ++u) { F[u] = ld16(rs, qo + u * 1024 + lane * 16); m = max(m, umax4(F[u])); }
#pragma unroll
    for (int e = 0; e < 8; ++e) { VM[e] = ld8(rs, qo + 4096 + (16 * e + r16) * 16 + gg * 8); m = max(m, max(VM[e].x, VM[e].y)); }
    if (__all(m != SENT) || dead) break;
    if (spin_fail(sp, ctrl, 0x53u)) { dead = true; break; }
  }
#pragma unroll
  for (int qt = 0; qt < 2; ++qt) {
    v4u pb; v2u pm;
    seg_softmax(F, KX, QX[qt], 16 * qt + r16, g, pb, pm);
    const unsigned ao = nr.frag(OFF_AX, s, h) + lane * 16 + qt * 4096;
#pragma unroll
    for (int v = 0; v < 4; ++v) {
      f32x4 o0 = mfma32(VX[2 * v], pb, f32x4{0.f, 0.f, 0.f, 0.f}), o1 = mfma32(VX[2 * v + 1], pb, f32x4{0.f, 0.f, 0.f, 0.f});
      o0 = mfma16(VM[2 * v], pm, o0); o1 = mfma16(VM[2 * v + 1], pm, o1);
      st16(rs, ao + v * 1024, v4u{pk2(o0[0], o0[1]), pk2(o0[2], o0[3]), pk2(o1[0], o1[1]), pk2(o1[2], o1[3])});
    }
  }
  DRAIN_VM();
}

__device__ __forceinline__ void mix_store(const Params& p, rsrc_t rs, const v4u& top, int tok, int col, f32x4 a, float gt, v2u xv) {
  const float og = bfr(1.0f - gt);
  const int sel = tok == (int)top.x ? 0 : tok == (int)top.y ? 1 : -1;
  float bv[4] = {bflo(xv.x), bfhi(xv.x), bflo(xv.y), bfhi(xv.y)};
  if (sel >= 0) {
    f32x4 y = {0.f, 0.f, 0.f, 0.f};
    for (int kc = 0; kc < 14; ++kc) y += __builtin_bit_cast(f32x4, ld16(rs, OFF_YP + ((kc * 2 + sel) * DM + col) * 4));
    const float rsel = u2f(sel == 0 ? top.z : top.w);
#pragma unroll
    for (int r = 0; r < 4; ++r) bv[r] = bfr(bv[r] + bfr(bfr(y[r]) * rsel));
  }
  float o[4];
#pragma unroll
  for (int r = 0; r < 4; ++r) o[r] = bfr(gt * bfr(a[r])) + bfr(og * bv[r]);
  *(GAS v2u*)(p.out + (size_t)tok * DM + col) = v2u{pk2(o[0], o[1]), pk2(o[2], o[3])};
}

__device__ __forceinline__ void oproj_task(const Params& p, LAS u8* L, rsrc_t rs, const Near& nr, int blk, int ct, bool& dead) {
  const int tid = fresh_tid(), lane = tid & 63, w = __builtin_amdgcn_readfirstlane(tid >> 6), wm = w >> 2, wn = w & 3, r16 = lane & 15, g = lane >> 4;
  gu32* ctrl = (gu32*)p.ws;
  if (w == 0) {
    wait_lines(ctrl, W_M2, LN, 56, 1u, lane, 0x60u, dead);
    wait_lines(ctrl, W_T2 + 8 * blk * LN, LN, 8, 1u, lane, 0x61u, dead);
    __builtin_amdgcn_fence(__ATOMIC_ACQUIRE, "agent");
    DRAIN_VM();
  }
  f32x4 acc[4][2];
  gemm_tile<128, M_O>(L, (const u16*)p.ws, blk * 4, p.Wo + ct * 128, DM, HD / 64, acc, ROT_O * ct, &nr);
  const v4u top = ld16(rs, W_TOP * 4);
#pragma unroll
  for (int m = 0; m < 4; ++m)
#pragma unroll
    for (int n = 0; n < 2; ++n) {
      const int tok = blk * 128 + 64 * wm + 16 * m + r16, col = ct * 128 + 32 * wn + 16 * n + 4 * g;
      mix_store(p, rs, top, tok, col, acc[m][n], u2f(ld4(rs, OFF_G + tok * 4)), *(const GAS v2u*)(p.x + (size_t)tok * DM + col));
    }
}

__device__ __forceinline__ void stream_role(const Params& p, LAS u8* L, rsrc_t rs, const Near& nr, int set, int q, bool& dead) {
  int wst = 100; const int wk = set * 32 + q; (void)wst; (void)wk;
  const int tid = fresh_tid(), lane = tid & 63, w = __builtin_amdgcn_readfirstlane(tid >> 6), r16 = lane & 15, g = lane >> 4, gg = g & 1;
  const int ks = q >> 3, ct = q & 7, np = w & 3, kh = w >> 2;
  gu32* ctrl = (gu32*)p.ws;
  const unsigned lb = (unsigned)(size_t)L;
  v4u Wf[16][2];
  LAS u8* Wl = L + 65536 + w * 8192 + lane * 16;
  {
    const int kr = 8 * w + (lane >> 3);
    const int krs = (kr & ~31) + 16 * ((kr >> 2) & 1) + 4 * ((kr >> 3) & 3) + (kr & 3);
    const int fsw = ((kr >> 1) & 1) | (((kr >> 3) & 1) << 1);
    const unsigned wvo = (krs * DM + 16 * (((lane & 7) >> 1) ^ fsw) + 8 * (lane & 1)) * 2;
    const int qq = r16 >> 2, fr = ((qq >> 1) & 1) | ((g & 1) << 1);
    const unsigned boff = lb + kh * 65536 + (g * 2 + (np >> 1)) * 1024 + qq * 128 + (lane & 3) * 8;
    const unsigned b0 = boff + (((2 * (np & 1)) ^ fr) * 32), b1 = boff + (((2 * (np & 1) + 1) ^ fr) * 32);
    static_for<2>([&](auto rc) {
      constexpr int RD = decltype(rc)::v;
      wgbar();
#pragma unroll
      for (int kt = 0; kt < 8; ++kt) {
        const u16* wb = p.Wo + (size_t)(1024 * ks + 512 * (kt >> 2) + 64 * (4 * RD + (kt & 3))) * DM + 128 * ct;
        dma16<false>(wb, wvo, lb + kt * 16384 + (w * 2) * 1024);
        dma16<false>(wb + 64, wvo, lb + kt * 16384 + (w * 2 + 1) * 1024);
      }
      DRAIN_VM();
      wgbar();
#pragma unroll
      for (int i = 0; i < 8; ++i) {
        const unsigned o = (i >> 1) * 16384 + (i & 1) * 8192;
        const v2u x0 = trread(b0 + o), x1 = trread(b0 + o + 512), y0 = trread(b1 + o), y1 = trread(b1 + o + 512);
        Wf[8 * RD + i][0] = v4u{x0.x, x0.y, x1.x, x1.y}; Wf[8 * RD + i][1] = v4u{y0.x, y0.y, y1.x, y1.y};
      }
    });
  }
  wgbar();
#pragma unroll
  for (int i = 0; i < 8; ++i) *(LAS v4u*)(Wl + i * 1024) = Wf[12 + (i >> 1)][i & 1];
  wait_lines(ctrl, W_M2, LN, 56, 1u, lane, 0x70u, dead);
  CFENCE();
  const v4u top = ld16(rs, W_TOP * 4);

  auto run = [&](auto role) {
    constexpr bool ATT = decltype(role)::v == 0;
    const int qt = w & 1, eh = w >> 1;
    v4u KX[2][4], QX[4], VX[4];
    auto prefetch = [&](int s) {
      if ((s & 3) < 2) {
        const int kind = (lane >> 1) % 3;
        gu32* f = ctrl + (kind == 2 ? W_FQ + (s >> 2) * 64 : W_FKV + (s >> 2) * 128 + kind * 64) + 2 * q + (lane & 1);
        for (unsigned sp = 0; !dead;) {
          if (__all(__hip_atomic_load(f, RLX_AGENT) != 0u)) break;
          __builtin_amdgcn_s_sleep(8);
          if (spin_fail(sp, ctrl, 0x73u)) dead = true;
        }
        CFENCE();
      }
      const unsigned fk = nr.frag(OFF_KX, s, q) + lane * 16, fq = nr.frag(OFF_QX, s, q) + lane * 16, fv = nr.frag(OFF_VX, s, q) + lane * 16;
#pragma unroll
      for (int i = 0; i < 8; ++i) KX[i >> 2][i & 3] = ld16(rs, fk + i * 1024);
#pragma unroll
      for (int i = 0; i < 4; ++i) { QX[i] = ld16(rs, fq + qt * 4096 + i * 1024); VX[i] = ld16(rs, fv + (4 * eh + i) * 1024); }
    };
    auto finish = [&](int s) {
      const int f = 4 * ks + (w & 3);
      wait_lines(ctrl, W_SP + (set * 32 + ct) * LN, 8 * LN, 4, (unsigned)(s + 1), lane, 0x72u, dead);
      CFENCE();
      f32x4 sum = {0.f, 0.f, 0.f, 0.f};
#pragma unroll
      for (int k = 0; k < 8; ++k) sum += __builtin_bit_cast(f32x4, ld16(rs, nr.frag(OFF_PS, 0, (((s & 7) * 8 + ct) * 8 + k) * 2 + (f >> 3)) + (f & 7) * 1024 + lane * 16));
      { const int tok = s * 32 + 16 * (f & 1) + r16, col = ct * 128 + 16 * (f >> 1) + 4 * g;
        mix_store(p, rs, top, tok, col, sum, u2f(ld4(rs, OFF_G + tok * 4)), *(const GAS v2u*)(p.x + (size_t)tok * DM + col)); }
    };
    unsigned prog = 0;
    if (ATT) prefetch(NBULK * 4 + set);
    for (int s = NBULK * 4 + set; s < NSEG; s += 2) {
      const int si = s - NBULK * 4;
      if (ATT) {
        const unsigned need = (unsigned)(s + 1 < NSEG - 1 ? s + 1 : NSEG - 1);
        if (prog < need) {
          for (unsigned sp = 0; !dead;) {
            prog = __builtin_amdgcn_readfirstlane(__hip_atomic_load(ctrl + W_PROG + LN * q, RLX_AGENT));
            if (prog >= need) break;
            __builtin_amdgcn_s_sleep(2);
            if (spin_fail(sp, ctrl, 0x52u)) dead = true;
          }
        }
        const unsigned qo = nr.blk(OFF_QKV + (unsigned)(s * QH + (q & (QH - 1))) * 16384u, nr.rel(q * 4));
        v4u F[4]; v2u VM[4];
        for (unsigned sp = 0;;) {
          CFENCE();
          unsigned m = 0;
#pragma unroll
          for (int u = 0; u < 4; ++u) { F[u] = ld16(rs, qo + u * 1024 + lane * 16); m = max(m, umax4(F[u])); }
#pragma unroll
          for (int e = 0; e < 4; ++e) { VM[e] = ld8(rs, qo + 4096 + (16 * (4 * eh + e) + r16) * 16 + gg * 8); m = max(m, max(VM[e].x, VM[e].y)); }
          if (__all(m != SENT) || dead) break;
          if (spin_fail(sp, ctrl, 0x53u)) { dead = true; break; }
        }
        v4u pb; v2u pm;
        seg_softmax(F, KX, QX, 16 * qt + r16, g, pb, pm);
        const unsigned ao = nr.frag(OFF_AX, s, q) + qt * 4096 + eh * 2048 + lane * 16;
#pragma unroll
        for (int v = 0; v < 2; ++v) {
          f32x4 o0 = mfma32(VX[2 * v], pb, f32x4{0.f, 0.f, 0.f, 0.f}), o1 = mfma32(VX[2 * v + 1], pb, f32x4{0.f, 0.f, 0.f, 0.f});
          o0 = mfma16(VM[2 * v], pm, o0); o1 = mfma16(VM[2 * v + 1], pm, o1);
          st16(rs, ao + v * 1024, v4u{pk2(o0[0], o0[1]), pk2(o0[2], o0[3]), pk2(o1[0], o1[1]), pk2(o1[2], o1[3])});
        }
        DRAIN_VM();
      } else {
        if (si >= 4) finish(s - 4);
        DRAIN_VM();
      }
      wgbar();
      WSTAMP();
      if (tid == 0) __hip_atomic_store(ctrl + W_SA + (set * 32 + q) * LN, (unsigned)(s + 1), RLX_AGENT);
      if (tid == 64 && si >= 2) __hip_atomic_store(ctrl + W_SP + (set * 32 + q) * LN, (unsigned)(s - 1), RLX_AGENT);
      if (w == 4) wait_lines(ctrl, W_SA + (set * 32 + ks * 8) * LN, LN, 8, (unsigned)(s + 1), lane, 0x71u, dead);
      wgbar();
      WSTAMP();
      {
        const unsigned ao = nr.frag(OFF_AX, s, 8 * ks + w) + lane * 16;
        v4u t[8];
#pragma unroll
        for (int i = 0; i < 8; ++i) t[i] = ld16(rs, ao + i * 1024);
#pragma unroll
        for (int i = 0; i < 8; ++i) *(LAS v4u*)(L + w * 8192 + i * 1024 + lane * 16) = t[i];
      }
      if (ATT && s + 2 < NSEG) prefetch(s + 2);
      wgbar();
      f32x4 a[2][2];
#pragma unroll
      for (int i = 0; i < 4; ++i) a[i >> 1][i & 1] = f32x4{0.f, 0.f, 0.f, 0.f};
#pragma unroll
      for (int k = 0; k < 16; ++k) {
        const LAS u8* fp = L + kh * 32768 + (k >> 2) * 8192 + (k & 3) * 1024 + lane * 16;
        const v4u x0 = *(const LAS v4u*)fp, x1 = *(const LAS v4u*)(fp + 4096);
#pragma unroll
        for (int n = 0; n < 2; ++n) {
          const v4u wf = k < 12 ? Wf[k][n] : *(const LAS v4u*)(Wl + ((k - 12) * 2 + n) * 1024);
          a[n][0] = mfma32(wf, x0, a[n][0]); a[n][1] = mfma32(wf, x1, a[n][1]);
        }
      }
      WSTAMP();
      const unsigned pb = nr.frag(OFF_PS, 0, (((s & 7) * 8 + ct) * 8 + 2 * ks + kh) * 2 + (np >> 1)) + 4 * (np & 1) * 1024 + lane * 16;
#pragma unroll
      for (int i = 0; i < 4; ++i) st16(rs, pb + i * 1024, __builtin_bit_cast(v4u, a[i >> 1][i & 1]));
      WSTAMP();
    }
    DRAIN_VM();
    wgbar();
    if (tid == 0) __hip_atomic_store(ctrl + W_SP + (set * 32 + q) * LN, (unsigned)(NSEG - 1 + set), RLX_AGENT);
    finish(ATT ? NSEG - 4 + set : NSEG - 2 + set);
  };
  if (w < 4) run(IC<0>{}); else run(IC<1>{});
}

__device__ __forceinline__ void worker_role(const Params& p, LAS u8* L, rsrc_t rs, int b) {
  const int w = __builtin_amdgcn_readfirstlane(threadIdx.x >> 6);
  const int x8 = 2 * (b & 3) + (b >> 7), r = (b >> 3) & 15, wk = r * 8 + x8;
  gu32* ctrl = (gu32*)p.ws;
  bool dead = false;
  int wst = 0; (void)wst;
  Near nr = near_init(p.ws);
  WSTAMP();
  {
    const int cg = x8 * 16 + r;
    for (int blk = 0; blk < 4; ++blk) {
      if (cg < 64) proj_task<64, M_K, 4>(p, L, rs, nr, dead, blk, 0, cg * 64); else proj_task<64, M_V, 4>(p, L, rs, nr, dead, blk, 1, (cg - 64) * 64);
      WSTAMP();
    }
  }
  for (int rnd = 0; rnd < 3; ++rnd) {
    if (rnd == 1 && r >= 8) { if (r < 15) mlp1_task(p, L, rs, (r - 8) * 8 + x8, dead); WSTAMP(); continue; }
    const bool isq = rnd == 2;
    const int ct = isq ? x8 * 2 + (r & 1) : x8 * 4 + (r & 3);
    const int blk = isq ? (r >> 3) * 8 + 2 * ((r >> 1) & 3) : 4 + 8 * rnd + 2 * (r >> 2);
    if (isq || ct < 16) proj_big<M_K>(p, L, rs, nr, blk, isq ? 2 : 0, ct * 256); else proj_big<M_V>(p, L, rs, nr, blk, 1, (ct - 16) * 256);
    if (rnd == 0) rg_task(p, rs, wk);
    if (rnd == 1 && r < 7) mlp2_task(p, L, rs, r * 8 + x8, dead);
    WSTAMP();
  }
  WSTAMP();
  if (r < 8) {
    const int blk = x8, m = r;
    for (int i = 0; i < 2; ++i) { const int t = (i * 8 + m) * 8 + w; attn_task(p, rs, nr, blk * 4 + (t >> 5), t & 31, dead); WSTAMP(); }
    wgbar();
    if (threadIdx.x == 0) __hip_atomic_store(ctrl + W_T2 + (8 * blk + m) * LN, 1u, RLX_AGENT);
    oproj_task(p, L, rs, nr, blk, m, dead);
    WSTAMP();
  } else {
    const int sid = x8 * 8 + (r - 8);
    stream_role(p, L, rs, nr, sid >> 5, sid & 31, dead);
    WSTAMP();
  }
}

__global__ void __launch_bounds__(512) rmt_init_kernel(u8* ws, const u16* mem0) {
  const int tid = fresh_tid();
  if (blockIdx.x >= 4) {
    const Near n = near_init(ws);
    arm_share(__builtin_amdgcn_make_buffer_rsrc(ws, 0, WS_END, 0x00020000), n.al, blockIdx.x - 4, tid);
    return;
  }
  if (blockIdx.x >= 2) {
    const Near n = near_init(ws);
    const int i = (blockIdx.x - 2) * 512 + tid, cb = i >> 3, row = i & 7;
    const u16* m0 = mem0 + row * DM + (cb >> 2) * 32 + (cb & 3) * 4;
    const v2u lo = *(const GAS v2u*)m0, hi = *(const GAS v2u*)(m0 + 16);
    const v4u v = {canon(__builtin_amdgcn_perm(hi.x, lo.x, 0x05040100u)), canon(__builtin_amdgcn_perm(hi.x, lo.x, 0x07060302u)),
                   canon(__builtin_amdgcn_perm(hi.y, lo.y, 0x05040100u)), canon(__builtin_amdgcn_perm(hi.y, lo.y, 0x07060302u))};
    u8* d = ws + n.al + OFF_MEM + (cb >> 6) * 16384 + (cb & 63) * 128 + row * 16;
    *(GAS v4u*)d = v; *(GAS v4u*)(d + 8192) = v;
    return;
  }
  if (blockIdx.x == 0) {
    for (int i = tid; i < (int)(CTRL_BYTES / 16); i += 512)
      if (i != W_CFG / 4) ((GAS v4u*)ws)[i] = v4u{0u, 0u, 0u, 0u};
  } else if (tid == 0) {
    const Near n = near_init(ws);
    gu32* a0 = (gu32*)(ws + n.al + OFF_CAL); gu32* a1 = a0 + 2048;
    unsigned long long b0 = ~0ull, b1 = ~0ull;
    for (int i = 0; i < 3; ++i) {
      const unsigned long long t0 = __builtin_amdgcn_s_memtime();
      __hip_atomic_store(a0, 0u, RLX_AGENT); DRAIN_VM();
      const unsigned long long t1 = __builtin_amdgcn_s_memtime();
      __hip_atomic_store(a1, 0u, RLX_AGENT); DRAIN_VM();
      const unsigned long long t2 = __builtin_amdgcn_s_memtime();
      b0 = t1 - t0 < b0 ? t1 - t0 : b0; b1 = t2 - t1 < b1 ? t2 - t1 : b1;
    }
    const unsigned nz = ((b1 < b0 ? 1u : 0u) ^ ((n.va21 + OFF_CAL) >> 21) ^ (xcc_id() >> 2)) & 1u;
    ((GAS v4u*)ws)[W_CFG / 4] = v4u{nz, 0u, 0u, 0u};
  }
}

__global__ void __launch_bounds__(NTHR, 2) rmt_mod_kernel(Params p) {
  extern __shared__ __attribute__((aligned(16))) u8 smem[];
  LAS u8* L = (LAS u8*)smem;
  const rsrc_t rs = __builtin_amdgcn_make_buffer_rsrc(p.ws, 0, WS_END, 0x00020000);
  const int b = blockIdx.x;
  if ((b & 4) == 0) chain_role(p, L, rs, b); else worker_role(p, L, rs, b);
}

}

extern "C" void kernel_launch(void* const* d_in, const int* in_sizes, int n_in, void* d_out, int out_size, void* d_ws, size_t ws_size,
                              hipStream_t stream) {
  (void)in_sizes; (void)n_in; (void)out_size; (void)ws_size;
  Params p;
  p.x = (const u16*)d_in[0]; p.mem0 = (const u16*)d_in[1]; p.Wq = (const u16*)d_in[2]; p.Wk = (const u16*)d_in[3];
  p.Wv = (const u16*)d_in[4]; p.Wo = (const u16*)d_in[5]; p.wr = (const u16*)d_in[8]; p.wg = (const u16*)d_in[9];
  p.wu = (const u16*)d_in[10]; p.wd = (const u16*)d_in[11]; p.gw = (const u16*)d_in[12];
  p.out = (u16*)d_out; p.ws = (u8*)d_ws;
  (void)hipFuncSetAttribute((const void*)rmt_mod_kernel, hipFuncAttributeMaxDynamicSharedMemorySize, LDS_BYTES);
  rmt_init_kernel<<<4 + NCHAIN, 512, 0, stream>>>(p.ws, p.mem0);
  rmt_mod_kernel<<<NWG, NTHR, LDS_BYTES, stream>>>(p);
}
```

```cpp
#include <hip/hip_runtime.h>
#include <hip/hip_bf16.h>
#include <stdint.h>

#define GAS __attribute__((address_space(1)))
#define LAS __attribute__((address_space(3)))
typedef unsigned v4u __attribute__((ext_vector_type(4)));
typedef unsigned v2u __attribute__((ext_vector_type(2)));
typedef unsigned v8u __attribute__((ext_vector_type(8)));
typedef float f32x4 __attribute__((ext_vector_type(4)));
typedef __bf16 bfx8 __attribute__((ext_vector_type(8)));
typedef __bf16 bfx16 __attribute__((ext_vector_type(16)));
typedef short s16x4 __attribute__((ext_vector_type(4)));
typedef GAS unsigned gu32;
typedef unsigned short u16;
typedef unsigned char u8;
#define RLX_AGENT __ATOMIC_RELAXED, __HIP_MEMORY_SCOPE_AGENT

#define PBF 1
#define SPLIT_XCD 2
namespace {

constexpr int SEQ = 2048, DM = 1024, NH = 32, HD = 4096, NSEG = 64, FF = 3584;
constexpr int NCHAIN = 128, NWORK = 128, NWG = 256, NTHR = 512;
constexpr unsigned SENT = 0xFFFFFFFFu;
constexpr int AUX_SC1 = 16;

constexpr unsigned CTRL_BYTES = 77824;
constexpr int LN = 32;
constexpr int W_TMO = 0, W_TOP = 3 * LN  , W_CFG = 4 * LN  ,
              W_XCC = 5 * LN, W_PROG = 8 * LN  , W_FKV = 64 * LN  , W_FQ = 128 * LN  ,
              W_RG = 160 * LN  , W_M2 = 288 * LN  , W_T2 = 344 * LN  ,
              W_SA = 408 * LN  , W_SP = 472 * LN  , W_M1 = 536 * LN  ;
constexpr unsigned OFF_MEM = 98304;
constexpr unsigned MEM_ST = 32768;
constexpr unsigned OFF_QKV = OFF_MEM + NSEG * MEM_ST;
constexpr unsigned QH = 32, PR = 128;
constexpr unsigned OFF_PART = OFF_QKV + NSEG * QH * 16384;
constexpr unsigned PART_SZ = PR * 16384;
#define NSLOT 3
constexpr unsigned OFF_ARM_END = OFF_PART + NSLOT * PART_SZ;
constexpr unsigned OFF_CAL = OFF_ARM_END;
constexpr unsigned OFF_KX = OFF_CAL + 16384 + 16384;
#define WFAR 1
constexpr unsigned FB = WFAR ? 16384 : 8192;
constexpr unsigned OFF_VX = OFF_KX + NSEG * NH * FB;
constexpr unsigned OFF_QX = OFF_VX + NSEG * NH * FB;
constexpr unsigned OFF_AX = OFF_QX + NSEG * NH * FB;
constexpr unsigned OFF_R = OFF_AX + NSEG * NH * FB + 1024 * FB + 16384;
constexpr unsigned OFF_G = OFF_R + SEQ * 4;
constexpr unsigned OFF_HID = OFF_G + SEQ * 4;
constexpr unsigned OFF_YP = OFF_HID + 2 * FF * 4;
constexpr unsigned OFF_PS = OFF_AX + NSEG * NH * FB;
constexpr unsigned WS_END = OFF_YP + 14 * 2 * DM * 4;
constexpr int NBULK = 8;
constexpr int LDS_BYTES = 160 * 1024;
constexpr unsigned SPIN_CAP = 1u << 18;

#define TAGP(...) (__VA_ARGS__)
#define BG_DELAY 16
#define ARM_LATE 0
#define TLB_WARM 0
#define NQK 4
#define FOK_AT 4
#define ROT_A 0
#define ROT_B 0
#define ROT_O 0
#define W_A 300
#define W_B 120
#define W_C 240

struct Params {
  const u16 *x, *mem0, *Wq, *Wk, *Wv, *Wo, *wr, *wg, *wu, *wd, *gw;
  u16* out;
  u8* ws;
};

__device__ __forceinline__ float u2f(unsigned u) { return __builtin_bit_cast(float, u); }
__device__ __forceinline__ unsigned f2u(float f) { return __builtin_bit_cast(unsigned, f); }
__device__ __forceinline__ float bf2f(unsigned b) { return u2f(b << 16); }
__device__ __forceinline__ float bflo(unsigned d) { return u2f(d << 16); }
__device__ __forceinline__ float bfhi(unsigned d) { return u2f(d & 0xFFFF0000u); }
__device__ __forceinline__ unsigned pk2(float lo, float hi) {
  typedef __bf16 bfx2 __attribute__((ext_vector_type(2)));
  bfx2 v; v.x = (__bf16)lo; v.y = (__bf16)hi;
  return __builtin_bit_cast(unsigned, v);
}
__device__ __forceinline__ float bfr(float f) { return (float)(__bf16)f; }
__device__ __forceinline__ unsigned canon(unsigned d) { return min(d, 0xFFFFFFFEu); }
__device__ __forceinline__ f32x4 mfma32(v4u a, v4u b, f32x4 c) {
  return __builtin_amdgcn_mfma_f32_16x16x32_bf16(__builtin_bit_cast(bfx8, a), __builtin_bit_cast(bfx8, b), c, 0, 0, 0);
}
__device__ __forceinline__ f32x4 mfma16(v2u a, v2u b, f32x4 c) { return mfma32(v4u{a.x, a.y, 0u, 0u}, v4u{b.x, b.y, 0u, 0u}, c); }
__device__ __forceinline__ f32x4 smf64(v4u a, v8u b, f32x4 c, int idx) {
  return __builtin_amdgcn_smfmac_f32_16x16x64_bf16(__builtin_bit_cast(bfx8, a), __builtin_bit_cast(bfx16, b), c, idx, 0, 0);
}
__device__ __forceinline__ v2u trread(unsigned lds_addr) {
  return __builtin_bit_cast(v2u, __builtin_amdgcn_ds_read_tr16_b64_v4i16((LAS s16x4*)(size_t)lds_addr));
}
typedef float f32x2 __attribute__((ext_vector_type(2)));
__device__ __forceinline__ f32x2 fold32(f32x4 v) {
  const auto r0 = __builtin_amdgcn_permlane32_swap(f2u(v[0]), f2u(v[2]), false, false);
  const auto r1 = __builtin_amdgcn_permlane32_swap(f2u(v[1]), f2u(v[3]), false, false);
  return f32x2{u2f(r0[0]), u2f(r1[0])} + f32x2{u2f(r0[1]), u2f(r1[1])};
}
__device__ __forceinline__ float xmax16(float a) { auto r = __builtin_amdgcn_permlane16_swap(f2u(a), f2u(a), false, false); return __builtin_amdgcn_fmed3f(u2f(r[0]), u2f(r[1]), INFINITY); }
__device__ __forceinline__ float xmax32(float a) { auto r = __builtin_amdgcn_permlane32_swap(f2u(a), f2u(a), false, false); return __builtin_amdgcn_fmed3f(u2f(r[0]), u2f(r[1]), INFINITY); }
__device__ __forceinline__ float xadd16(float a) { auto r = __builtin_amdgcn_permlane16_swap(f2u(a), f2u(a), false, false); return u2f(r[0]) + u2f(r[1]); }
__device__ __forceinline__ float xadd32(float a) { auto r = __builtin_amdgcn_permlane32_swap(f2u(a), f2u(a), false, false); return u2f(r[0]) + u2f(r[1]); }

__device__ __forceinline__ float wave_sum(float v) {
  v += __shfl_xor(v, 1); v += __shfl_xor(v, 2); v += __shfl_xor(v, 4); v += __shfl_xor(v, 8);
  return xadd32(xadd16(v));
}
__device__ __forceinline__ int fresh_tid() { int t = threadIdx.x; asm volatile("" : "+v"(t)); return t; }
#define CFENCE() asm volatile("" ::: "memory")
#define DRAIN_VM() asm volatile("s_waitcnt vmcnt(0)" ::: "memory")
#define WAIT_LGKM() asm volatile("s_waitcnt lgkmcnt(0)" ::: "memory")
__device__ __forceinline__ void wgbar() { WAIT_LGKM(); __builtin_amdgcn_s_barrier(); CFENCE(); }

typedef __amdgpu_buffer_rsrc_t rsrc_t;
__device__ __forceinline__ v4u ld16(rsrc_t rs, unsigned off) { return __builtin_amdgcn_raw_buffer_load_b128(rs, off, 0, AUX_SC1); }
__device__ __forceinline__ v2u ld8(rsrc_t rs, unsigned off) { return __builtin_amdgcn_raw_buffer_load_b64(rs, off, 0, AUX_SC1); }
__device__ __forceinline__ unsigned ld4(rsrc_t rs, unsigned off) { return __builtin_amdgcn_raw_buffer_load_b32(rs, off, 0, AUX_SC1); }
__device__ __forceinline__ void st16(rsrc_t rs, unsigned off, v4u v) { __builtin_amdgcn_raw_buffer_store_b128(v, rs, off, 0, AUX_SC1); }
__device__ __forceinline__ void st8(rsrc_t rs, unsigned off, v2u v) { __builtin_amdgcn_raw_buffer_store_b64(v, rs, off, 0, AUX_SC1); }
__device__ __forceinline__ void st4(rsrc_t rs, unsigned off, unsigned v) { __builtin_amdgcn_raw_buffer_store_b32(v, rs, off, 0, AUX_SC1); }
__device__ __forceinline__ unsigned umax4(v4u v) { return max(max(v.x, v.y), max(v.z, v.w)); }

template <int N> __device__ __forceinline__ void delay_cyc() {
#pragma unroll
  for (int i = 0; i < N / 64; ++i) asm volatile("s_nop 15");
  if ((N % 64) >= 4) asm volatile("s_nop %0" :: "i"((N % 64) / 4 - 1));
}

#define PACE 0
#define PACE_GAIN 0.3f
#define PACE_CLAMP 2.0f
#define GAP_A 6
#define DB3 5
#define EXTRA_B 0
#define EXTRA_C 0
#define GAP_C 6
#define AD_DEC 0
#define AD_INC 0
__device__ __forceinline__ void delay_rt(int n) {
  asm volatile("s_cmp_lt_i32 %0, 1\n\ts_cbranch_scc1 2f\n\ts_mov_b32 vcc_lo, %0\n1:\n\ts_nop 1\n\ts_sub_i32 vcc_lo, vcc_lo, 1\n\ts_cmp_gt_i32 vcc_lo, 0\n\ts_cbranch_scc1 1b\n2:" :: "s"(n) : "vcc", "scc");
}
__device__ __forceinline__ void adapt(int& n, int misses) { n = misses ? min(n + AD_INC, 120) : max(n - AD_DEC, 0); }

__device__ __forceinline__ bool spin_fail(unsigned& spins, gu32* ctrl, unsigned code) {
  ++spins;
  if (spins > SPIN_CAP) { __hip_atomic_store(ctrl + W_TMO, 1u, RLX_AGENT); __hip_atomic_fetch_max(ctrl + W_TMO + 1, code, RLX_AGENT); return true; }
  return (spins & 255u) == 0u && __builtin_amdgcn_readfirstlane(__hip_atomic_load(ctrl + W_TMO, RLX_AGENT)) != 0u;
}
constexpr int RETRY_CAP = 1 << 15;
__device__ __forceinline__ void wait_ge(gu32* ctrl, int word, unsigned want, unsigned code, bool& dead) {
  if (dead) return;
  for (unsigned sp = 0;;) {
    if ((unsigned)__builtin_amdgcn_readfirstlane(__hip_atomic_load(ctrl + word, RLX_AGENT)) >= want) return;
    __builtin_amdgcn_s_sleep(4);
    if (spin_fail(sp, ctrl, code)) { dead = true; return; }
  }
}
__device__ __forceinline__ void wait_flags2(gu32* ctrl, int w0, int w1, unsigned code, bool& dead) {
  if (dead) return;
  for (unsigned sp = 0;;) {
    const unsigned long long a = __hip_atomic_load((GAS unsigned long long*)(ctrl + w0), RLX_AGENT);
    const unsigned long long b = __hip_atomic_load((GAS unsigned long long*)(ctrl + w1), RLX_AGENT);
    const unsigned long long m = a & b;
    if (__builtin_amdgcn_readfirstlane((unsigned)m & (unsigned)(m >> 32)) != 0u) return;
    __builtin_amdgcn_s_sleep(4);
    if (spin_fail(sp, ctrl, code)) { dead = true; return; }
  }
}
__device__ __forceinline__ void wait_lines(gu32* ctrl, int word, int stride, int n, unsigned want, int lane, unsigned code, bool& dead) {
  if (dead) return;
  gu32* mine = ctrl + word + (lane < n ? lane : 0) * stride;
  for (unsigned sp = 0;;) {
    if (__all(__hip_atomic_load(mine, RLX_AGENT) >= want)) return;
    __builtin_amdgcn_s_sleep(2);
    if (spin_fail(sp, ctrl, code)) { dead = true; return; }
  }
}
constexpr int ARM_AHEAD = 4;
__device__ __forceinline__ void arm_share(rsrc_t rs, unsigned al, int c, int tid) {
  const v4u s = {SENT, SENT, SENT, SENT};
  const unsigned lo = (tid & 63) * 16, w = tid >> 6;
  constexpr int NB = PR / 64;
  for (int sl = 0; sl < NSLOT; ++sl)
#pragma unroll
    for (int k = 0; k < NB; ++k) if (!PBF || w < 4) st16(rs, al + OFF_PART + sl * PART_SZ + (c * NB + k) * 8192 + w * 1024 + lo, s);
  if (w < 6) {
#pragma unroll
    for (int k = 0; k < NB; ++k) st16(rs, al + OFF_QKV + (c * NB + k) * 8192 + w * 1024 + lo, s);
  } else if (w == 6) st16(rs, al + OFF_MEM + MEM_ST + c * 1024 + lo, s);
  else if (c < 32) st16(rs, al + OFF_MEM + 5 * MEM_ST + c * 1024 + lo, s);
}
struct Near {
  unsigned al, va21, nz, k;
  __device__ __forceinline__ unsigned rel(int c) const { return k >> 2; }
  __device__ __forceinline__ unsigned frag(unsigned reg, int s, int h) const { return WFAR ? blk(reg + (unsigned)(s * NH + h) * FB, (k >> 2) ^ 1u) : reg + (unsigned)(s * NH + h) * FB; }
  __device__ __forceinline__ unsigned blk(unsigned pair_off, unsigned iod) const { return al + pair_off + ((((va21 + pair_off) >> 21) ^ nz ^ iod) & 1u) * 8192u; }
};
__device__ __forceinline__ Near near_init(const u8* ws) {
  Near n; const unsigned lo = (unsigned)(size_t)ws;
  n.al = (0u - lo) & 16383u; n.va21 = lo + n.al; n.nz = 0; n.k = 0; return n;
}
__device__ __forceinline__ unsigned xcc_id() { return (unsigned)__builtin_amdgcn_s_getreg(20 | (3 << 11)) & 7u; }
__device__ __forceinline__ void near_wait(gu32* ctrl, Near& n, bool& dead) {
  wait_ge(ctrl, W_XCC, 16u, 0x11u, dead);
  n.k = __builtin_amdgcn_readfirstlane(__hip_atomic_load(ctrl + W_XCC, RLX_AGENT)) & 7u;
  n.nz = __builtin_amdgcn_readfirstlane(__hip_atomic_load(ctrl + W_CFG, RLX_AGENT)) & 1u;
}

#define CSTAMP(wv, slot)
#define PT(id)
#define CTRIES(slot, n)
#define WSTAMP()
template <bool SC1>
__device__ __forceinline__ void dma16(const void* base, unsigned voff, unsigned lds_addr) {
  unsigned keep;
  if (SC1) asm volatile("s_mov_b32 %0, m0\n\ts_mov_b32 m0, %3\n\ts_nop 0\n\tglobal_load_lds_dwordx4 %1, %2 sc1\n\ts_mov_b32 m0, %0" : "=&s"(keep) : "v"(voff), "s"(base), "s"(lds_addr) : "memory");
  else asm volatile("s_mov_b32 %0, m0\n\ts_mov_b32 m0, %3\n\ts_nop 0\n\tglobal_load_lds_dwordx4 %1, %2\n\ts_mov_b32 m0, %0" : "=&s"(keep) : "v"(voff), "s"(base), "s"(lds_addr) : "memory");
}

template <int N> __device__ __forceinline__ void wait_vm() { static_assert(N >= 0 && N < 64, "vmcnt"); asm volatile("s_waitcnt vmcnt(%0)" :: "i"(N) : "memory"); }
template <int V> struct IC { static constexpr int v = V; };
template <int N, int I = 0, class F> __device__ __forceinline__ void static_for(F&& f) { if constexpr (I < N) { f(IC<I>{}); static_for<N, I + 1>(f); } }
__device__ __forceinline__ void slice_dma(const u16* src, int ld, unsigned dst, int lane) {
  const unsigned vo = ((lane >> 2) * ld + 8 * (lane & 3)) * 2;
#pragma unroll
  for (int p = 0; p < 8; ++p) dma16<false>(src + (size_t)16 * p * ld, vo, dst + p * 1024);
}
template <bool PERM>
__device__ __forceinline__ void slice_frags(unsigned base, int lane, v8u (&f)[2][2]) {
  const int gB = lane >> 4, q = (lane & 15) >> 2, p = lane & 3;
#pragma unroll
  for (int cc = 0; cc < 2; ++cc)
#pragma unroll
    for (int rr = 0; rr < 4; ++rr) {
      const int kp = 64 * cc + 32 * (rr >> 1) + 16 * (gB >> 1) + 4 * (gB & 1) + 2 * (rr & 1) + (q & 1) + 8 * (q >> 1);
      const int row = PERM ? ((kp & ~31) + 16 * (kp & 1) + ((kp & 31) >> 1)) : kp;
#pragma unroll
      for (int tb = 0; tb < 2; ++tb) {
        const v2u v = trread(base + row * 64 + tb * 32 + p * 8);
        f[tb][cc][2 * rr] = v.x; f[tb][cc][2 * rr + 1] = v.y;
      }
    }
}

__device__ __forceinline__ void chain_role(const Params& p, LAS u8* L, rsrc_t rs, int b) {
  const int tid = threadIdx.x, lane = tid & 63, w = __builtin_amdgcn_readfirstlane(tid >> 6);
  const int c = (b & 3) * 32 + (b >> 3);
  const int h = c >> 2, j = c & 3;
  const int dbgi = (c >> 4) + 8 * (c & 15); (void)dbgi;
  const int r16 = lane & 15, g = lane >> 4;
  gu32* ctrl = (gu32*)p.ws;
  bool dead = false;
  const unsigned lbase = (unsigned)(size_t)L;
  Near nr = near_init(p.ws);
  if (b == 0 && tid == 0) __hip_atomic_store(ctrl + W_XCC, 16u | xcc_id(), RLX_AGENT);

  v8u W1[3][2][2];
  v8u WO[2][2];
  {
    const unsigned mine = lbase + w * 16384;
    const size_t wcol = (size_t)h * 128 + 32 * j;
    slice_dma(p.Wk + (size_t)128 * w * HD + wcol, HD, mine, lane);
    slice_dma(p.Wv + (size_t)128 * w * HD + wcol, HD, mine + 8192, lane);
    asm volatile("s_waitcnt vmcnt(8)" ::: "memory");
    slice_frags<true>(lbase + w * 16384, lane, W1[1]);
    WAIT_LGKM(); __builtin_amdgcn_sched_barrier(0);
    slice_dma(p.Wq + (size_t)128 * w * HD + wcol, HD, mine, lane);
    asm volatile("s_waitcnt vmcnt(8)" ::: "memory");
    slice_frags<true>(lbase + w * 16384 + 8192, lane, W1[2]);
    WAIT_LGKM(); __builtin_amdgcn_sched_barrier(0);
    DRAIN_VM();
    slice_frags<true>(lbase + w * 16384, lane, W1[0]);
    WAIT_LGKM(); __builtin_amdgcn_sched_barrier(0);
  }
  near_wait(ctrl, nr, dead);
  const unsigned myrel = nr.rel(c);

  LAS float* RED = (LAS float*)L;
  LAS u8* AHF = L + 24576;
  volatile LAS unsigned* FOK = (volatile LAS unsigned*)(L + 31744);
  LAS float* INV = (LAS float*)(L + 30976);
  LAS u8* KVL = L + 49152;
  const int sidx = r16 < 8 ? 0x4444 : 0xEEEE;
  const int gg = g & 1, hi = g >> 1, rp = 2 * gg + hi;
  int gone = dead ? 1 : 0;

  auto fetch_kv_issue = [&](int s, int lo, int hi) {
    if ((s & 3) == 0) { bool d = gone != 0; const int blk = s >> 2; wait_flags2(ctrl, W_FKV + blk * 128 + 2 * h, W_FKV + blk * 128 + 64 + 2 * h, 0x20u, d); gone = __builtin_amdgcn_readfirstlane(d ? 1 : 0); }
    CFENCE();
    const unsigned dst = lbase + 49152 + (s & 1) * 16384;
#pragma unroll
    for (int i = lo; i < hi; ++i) dma16<true>(p.ws + nr.frag((i >> 3) ? OFF_VX : OFF_KX, s, h) + (i & 7) * 1024, lane * 16, dst + i * 1024);
  };
  auto kv_ready = [&](int s) {
    if ((s & 3) == 0) { bool d = gone != 0; const int blk = s >> 2; wait_flags2(ctrl, W_FKV + blk * 128 + 2 * h, W_FKV + blk * 128 + 64 + 2 * h, 0x20u, d); gone = __builtin_amdgcn_readfirstlane(d ? 1 : 0); CFENCE(); }
  };
  auto arm_addr = [&](int s, int slot, int q, unsigned& a0, unsigned& a1) {
    a0 = s > 0 ? nr.blk(OFF_PART + (unsigned)(slot == 0 ? 2 : slot - 1) * PART_SZ + (c & (PR - 1)) * 16384u, myrel) + q * 1024 + lane * 16 : 0xFFFFFF00u;
    const unsigned qn = nr.blk(OFF_QKV + (unsigned)((s + ARM_AHEAD) * QH + (h & (QH - 1))) * 16384u, myrel);
    a1 = q == 0 ? qn + j * 1024 + lane * 16
                : lane < 32 ? qn + 4096 + j * 512 + lane * 16
                            : (lane < 48 && s + ARM_AHEAD + 2 < NSEG) ? nr.al + OFF_MEM + (unsigned)(s + ARM_AHEAD + 2) * MEM_ST + (c >> 6) * 16384u + ((lane >> 3) & 1) * 8192u + (c & 63) * 128 + (lane & 7) * 16
                                                                      : 0xFFFFFF00u;
    a1 = s + ARM_AHEAD < NSEG ? a1 : 0xFFFFFF00u;
  };
  if (tid == 0) { FOK[0] = 0u; FOK[1] = 0u; }
  int dA = W_A / 20, dB = W_B / 20, dC = W_C / 20;
  wgbar();

  auto run = [&](auto role) {
    constexpr int ROLE = decltype(role)::v;
    LAS f32x2* redw = (LAS f32x2*)(RED + w * 768) + (rp * 16 + r16) * 2;
    const int R = 32 * j + 4 * w + (r16 >> 2);
    const unsigned rrel = nr.rel(R), part_w = (R & (PR - 1)) * 16384u, part_l = PBF ? h * 128 + rp * 32 + (r16 & 3) * 8 : h * 256 + rp * 64 + (r16 & 3) * 16;
    const unsigned pub_l = ROLE == 0 ? j * 1024 + (8 * w + 2 * g + (r16 & 1) + 16 * (r16 >> 2)) * 16 + (r16 & 2) * 4 : 4096 + (32 * j + r16) * 16 + g * 4;
    unsigned qo, a_part, mo, a_pub;
    float pinf = INFINITY, mi = g < 2 ? -INFINITY : 0.f;
    asm volatile("" : "+v"(pinf), "+v"(mi));
    const f32x4 minit = {mi, mi, mi, mi};
    const unsigned s1 = (r16 & 3) == 0 ? 0x00003F80u : (r16 & 3) == 1 ? 0x3F800000u : 0u;
    const v4u rsel = {(r16 & 4) ? 0u : s1, (r16 & 4) ? s1 : 0u, (r16 & 4) ? 0u : s1, (r16 & 4) ? s1 : 0u};
    int slot = 0, lim = gone ? 0 : RETRY_CAP;
    auto prepare = [&](int s, int sl) {
      qo = nr.blk(OFF_QKV + (unsigned)(s * QH + (h & (QH - 1))) * 16384u, myrel);
      a_part = nr.blk(OFF_PART + (unsigned)sl * PART_SZ + part_w, rrel) + part_l;
      mo = nr.blk(OFF_MEM + (unsigned)s * MEM_ST + (w >> 2) * 16384u, myrel) + (w & 3) * 2048 + lane * 16;
      a_pub = qo + pub_l;
      asm volatile("" : "+v"(a_part), "+v"(mo), "+v"(a_pub));
    };
    prepare(0, 0);
    auto step = [&](auto first, int s) -> bool {
      constexpr bool FIRST = decltype(first)::v != 0;
      const unsigned par = (unsigned)(s / 3) & 1u; (void)par;
      PT(0);
      delay_rt(dA);
      PT(1);
      f32x4 acc[6];
      {
        int tries = 0; bool again;
        do {
          CFENCE();
          const v4u a0 = ld16(rs, mo), a1 = ld16(rs, mo + 1024);
          if (FIRST && tries == 0) slice_dma(p.Wo + (size_t)128 * h * DM + 256 * j + 32 * w, DM, lbase + 98304 + w * 8192, lane);
#pragma unroll
          for (int t = 0; t < 6; ++t) { acc[t] = f32x4{0.f, 0.f, 0.f, 0.f}; asm volatile("" : "+v"(acc[t])); }
          __builtin_amdgcn_sched_barrier(0);
#pragma unroll
          for (int t = 0; t < 6; ++t) acc[t] = smf64(a0, W1[t >> 1][t & 1][0], acc[t], sidx);
          __builtin_amdgcn_sched_barrier(0);
#pragma unroll
          for (int t = 0; t < 6; ++t) acc[t] = smf64(a1, W1[t >> 1][t & 1][1], acc[t], sidx);
          again = (__ballot(max(umax4(a0), umax4(a1)) == SENT) != 0ull) & (tries < lim);
          ++tries;
        } while (__builtin_expect(again, 0));
        lim = tries > lim ? 0 : lim;
        if (w == 0) CTRIES(0, tries - 1);
      }
      CSTAMP(0, 0);
      PT(2);
#pragma unroll
      for (int t = 0; t < 6; ++t) redw[(t >> 1) * 128 + (t & 1)] = fold32(acc[t]);
      PT(3);
      wgbar();
      PT(4);
      CSTAMP(0, 1);
      if (ROLE <= 2) {
        const LAS f32x4* redr = (const LAS f32x4*)RED + w * 64 + lane;
        f32x4 t0 = redr[0], t1 = redr[192];
#pragma unroll
        for (int k = 2; k < 8; k += 2) { t0 += redr[k * 192]; t1 += redr[(k + 1) * 192]; }
        t0 += t1;
        if (ROLE == 0) {
          const unsigned d0 = pk2(t0[0], t0[2]), d1 = pk2(t0[1], t0[3]);
          const unsigned got = __builtin_amdgcn_mov_dpp((r16 & 1) ? d0 : d1, 0xB1, 0xF, 0xF, true);
          st8(rs, a_pub, (r16 & 1) ? v2u{canon(got), canon(d1)} : v2u{canon(d0), canon(got)});
        } else {
          st4(rs, a_pub, canon(pk2(t0[0], t0[1])));
          st4(rs, a_pub + 256, canon(pk2(t0[2], t0[3])));
        }
      }
      CSTAMP(0, 2);
      PT(5);
      if (!FIRST && s == NSEG - 1) return false;
      if (FIRST) {
        if (ROLE >= 4) fetch_kv_issue(0, 4 * (ROLE - 4), 4 * (ROLE - 4) + 4);
        DRAIN_VM();
        slice_frags<false>(lbase + 98304 + w * 8192, lane, WO);
        wgbar();
      }
      acc[0] = f32x4{0.f, 0.f, 0.f, 0.f}; acc[1] = acc[0];
      asm volatile("" : "+v"(acc[0]), "+v"(acc[1]));
      if (ROLE < NQK) {
        constexpr int NE = 8 / NQK;
        v4u KX[2][4], VX[NE], F[4]; v2u VM[NE];
        const LAS u8* kv = KVL + (s & 1) * 16384 + lane * 16;
#pragma unroll
        for (int i = 0; i < 8; ++i) KX[i >> 2][i & 3] = *(const LAS v4u*)(kv + i * 1024);
#pragma unroll
        for (int e = 0; e < NE; ++e) VX[e] = *(const LAS v4u*)(kv + 8192 + (NE * w + e) * 1024);
        const unsigned fo = qo + lane * 16, vo = qo + 4096 + (16 * NE * w + r16) * 16 + gg * 8;
        __builtin_amdgcn_sched_barrier(0);
        delay_rt((ROLE == 3 ? dB + DB3 : dB));
        PT(6);
        f32x4 sA, sB, sM;
        {
          int tries = 0; bool again;
          do {
            CFENCE();
#pragma unroll
            for (int u = 0; u < 4; ++u) F[u] = ld16(rs, fo + u * 1024);
#pragma unroll
            for (int e = 0; e < NE; ++e) VM[e] = ld8(rs, vo + e * 256);
            sA = f32x4{0.f, 0.f, 0.f, 0.f}; sB = sA; sM = minit;
#pragma unroll
            for (int u = 0; u < 4; ++u) {
              sM = mfma32(F[u], F[u], sM); sA = mfma32(KX[0][u], F[u], sA); sB = mfma32(KX[1][u], F[u], sB);
              if (u == FOK_AT) { __builtin_amdgcn_sched_barrier(0); FOK[0] = (unsigned)(s + 1); __builtin_amdgcn_sched_barrier(0); }
            }
            unsigned m = max(max(umax4(F[0]), umax4(F[1])), max(umax4(F[2]), umax4(F[3])));
#pragma unroll
            for (int e = 0; e < NE; ++e) m = max(m, max(VM[e].x, VM[e].y));
            again = (__ballot(m == SENT) != 0ull) & (tries < lim);
            ++tries;
          } while (__builtin_expect(again, 0));
          lim = tries > lim ? 0 : lim;
          if (w == 0) CTRIES(1, tries - 1);
        }
        if (FOK_AT > 3) FOK[0] = (unsigned)(s + 1);
        CSTAMP(0, 3);
        PT(7);
        const float sc2 = 0.08838834764831845f * 1.4426950408889634f;
        sA *= sc2; sB *= sc2; sM *= sc2;
        float mx = fmaxf(fmaxf(fmaxf(fmaxf(sA[0], sA[1]), sA[2]), fmaxf(fmaxf(sA[3], sB[0]), sB[1])), fmaxf(fmaxf(fmaxf(sB[2], sB[3]), sM[0]), fmaxf(fmaxf(sM[1], sM[2]), sM[3])));
        { auto r = __builtin_amdgcn_permlane16_swap(f2u(mx), f2u(mx), false, false); mx = __builtin_amdgcn_fmed3f(u2f(r[0]), u2f(r[1]), pinf); }
        { auto r = __builtin_amdgcn_permlane32_swap(f2u(mx), f2u(mx), false, false); mx = __builtin_amdgcn_fmed3f(u2f(r[0]), u2f(r[1]), pinf); }
        sA -= mx; sB -= mx; sM -= mx;
#pragma unroll
        for (int r = 0; r < 4; ++r) { sA[r] = __builtin_amdgcn_exp2f(sA[r]); sB[r] = __builtin_amdgcn_exp2f(sB[r]); sM[r] = __builtin_amdgcn_exp2f(sM[r]); }
        const v4u pb = {pk2(sA[0], sA[1]), pk2(sA[2], sA[3]), pk2(sB[0], sB[1]), pk2(sB[2], sB[3])};
        const v2u pm = {pk2(sM[0], sM[1]), pk2(sM[2], sM[3])};
        f32x4 eo[NE];
#pragma unroll
        for (int e = 0; e < NE; ++e) eo[e] = mfma32(VX[e], pb, f32x4{0.f, 0.f, 0.f, 0.f});
#pragma unroll
        for (int e = 0; e < NE; ++e) eo[e] = mfma16(VM[e], pm, eo[e]);
        if (ROLE == 3 || NQK == 2) {
          const f32x4 ss = sA + sB + sM;
          INV[lane] = __builtin_amdgcn_rcpf(xadd32(xadd16((ss[0] + ss[1]) + (ss[2] + ss[3]))));
        }
        LAS u8* ah = AHF + (r16 < 8 ? (2 * NE * w + hi) * 128 + r16 * 16 + gg * 8 : 4096 + lane * 8);
#pragma unroll
        for (int e = 0; e < NE; ++e) *(LAS v2u*)(ah + e * 256) = v2u{pk2(eo[e][0], eo[e][1]), pk2(eo[e][2], eo[e][3])};
      } else if (ROLE >= 4) {
        constexpr int Q = ROLE - 4;
        kv_ready(s + 1);
        const u8* kb = p.ws + nr.frag((Q >> 1) ? OFF_VX : OFF_KX, s + 1, h) + (Q & 1) * 4096;
        const unsigned dst = lbase + 49152 + ((s + 1) & 1) * 16384 + Q * 4096;
        unsigned ar0, ar1;
        arm_addr(s, slot, Q, ar0, ar1);
        asm volatile("" : "+v"(ar0), "+v"(ar1));
        __builtin_amdgcn_sched_barrier(0);
        for (unsigned sp = 0; sp < (1u << 20); ++sp)
          if ((unsigned)__builtin_amdgcn_readfirstlane(FOK[0]) == (unsigned)(s + 1)) break;
#pragma unroll
        for (int i = 0; i < 4; ++i) dma16<true>(kb + i * 1024, lane * 16, dst + i * 1024);
        st16(rs, ar0, v4u{SENT, SENT, SENT, SENT});
        if (Q < 2) st16(rs, ar1, v4u{SENT, SENT, SENT, SENT});
      }
      PT(9);
      wgbar();
      PT(10);
      CSTAMP(7, 4);
      {
        const v4u b0 = *(LAS v4u*)(AHF + lane * 16), b1 = *(LAS v4u*)(AHF + 1024 + lane * 16);
        const f32x2 iv = *(LAS f32x2*)(INV + 2 * rp);
        acc[0] = smf64(b0, WO[0][0], acc[0], sidx); acc[1] = smf64(b0, WO[1][0], acc[1], sidx);
        acc[0] = smf64(b1, WO[0][1], acc[0], sidx); acc[1] = smf64(b1, WO[1][1], acc[1], sidx);
        const f32x2 f0 = fold32(acc[0]) * iv, f1 = fold32(acc[1]) * iv;
        const v4u pv = {canon(f2u(f0[0])), canon(f2u(f1[0])), canon(f2u(f0[1])), canon(f2u(f1[1]))};
        if (PBF) st8(rs, a_part, TAGP(v2u{canon(pk2(f0[0], f1[0])), canon(pk2(f0[1], f1[1]))})); else st16(rs, a_part, pv);
      }
      __builtin_amdgcn_sched_barrier(0);
      CSTAMP(7, 5);
      PT(11);
      const int nslot = slot == NSLOT - 1 ? 0 : slot + 1;
      if (ROLE == 7) {
        unsigned po = nr.blk(OFF_PART + (unsigned)slot * PART_SZ + (c & (PR - 1)) * 16384u, myrel) + lane * 16;
        unsigned a_mem = nr.blk(OFF_MEM + (unsigned)(s + 1) * MEM_ST + (c >> 6) * 16384u, myrel) + (c & 63) * 128 + (2 * (r16 >> 2) + g) * 16 + (r16 & 3) * 4;
        a_mem = lane < 32 ? a_mem : 0xFFFFFF00u;
        prepare(s + 1, nslot);
        asm volatile("" : "+v"(a_mem), "+v"(po));
        delay_rt(dC);
        PT(12);
        f32x4 t;
        {
          int tries = 0; bool again;
          const unsigned p8 = po - lane * 8;
          do {
            CFENCE();
            v2u pq[8];
#pragma unroll
            for (int i = 0; i < 8; ++i) pq[i] = ld8(rs, p8 + i * 512);
            t = f32x4{0.f, 0.f, 0.f, 0.f};
#pragma unroll
            for (int i = 0; i < 8; i += 2) t = mfma32(rsel, v4u{pq[i].x, pq[i].y, pq[i + 1].x, pq[i + 1].y}, t);
            unsigned m = 0;
#pragma unroll
            for (int i = 0; i < 8; ++i) m = max(m, max(pq[i].x, pq[i].y));
            again = (__ballot(m == SENT) != 0ull) & (tries < lim);
            ++tries;
          } while (__builtin_expect(again, 0));
          lim = tries > lim ? 0 : lim;
          CTRIES(2, tries - 1);
        }
        CSTAMP(7, 6);
        PT(13);
        st4(rs, a_mem, canon(pk2(t[0], t[1])));
      } else {
        prepare(s + 1, nslot);
        if (ROLE == 2 && j == 0 && lane == 0) __hip_atomic_store(ctrl + W_PROG + LN * h, (unsigned)(s + 1), RLX_AGENT);
        if (ROLE >= 4 || ARM_LATE) DRAIN_VM();
      }
      slot = nslot;
      PT(14);
      wgbar();
      CSTAMP(7, 7);
      return true;
    };
    step(IC<1>{}, 0);
    for (int s = 1; step(IC<0>{}, s); ++s) {}
    if (lim == 0 && lane == 0) __hip_atomic_store(ctrl + W_TMO, 1u, RLX_AGENT);
  };
  switch (w) {
    case 0: case 1: run(IC<0>{}); break;
    case 2: run(IC<2>{}); break;
    case 3: run(IC<3>{}); break;
    case 4: run(IC<4>{}); break;
    case 5: run(IC<5>{}); break;
    case 6: run(IC<6>{}); break;
    default: run(IC<7>{}); break;
  }
}

enum { M_K = 0, M_V = 1, M_O = 2 };

template <int BN, int MODE, int WM = 2>
__device__ __forceinline__ void gemm_tile(LAS u8* L, const u16* A, int lda, const u16* W, int ldw, int nkt, f32x4 (&acc)[8 / WM][BN * WM / 128], int rot = 0, const Near* nr = nullptr) {
  constexpr int MT = 8 / WM, NT = BN * WM / 128, NCG = BN / 64, STG = 16384 + BN * 128, NB = BN / 64;
  constexpr int NSTG = LDS_BYTES / STG;
  const int tid = fresh_tid(), lane = tid & 63, w = __builtin_amdgcn_readfirstlane(tid >> 6);
  const int wm = w / (8 / WM), wn = w % (8 / WM), r16 = lane & 15, g = lane >> 4, q = r16 >> 2, pp = lane & 3;
  const int arow = 16 * w + (lane >> 3);
  const unsigned avo0 = (arow * lda + 8 * ((lane & 7) ^ ((arow >> 1) & 7))) * 2;
  const unsigned avo1 = ((arow + 8) * lda + 8 * ((lane & 7) ^ (((arow + 8) >> 1) & 7))) * 2;
  const int kr = 8 * w + (lane >> 3);
  const int krs = MODE == M_O ? ((kr & ~31) + 16 * ((kr >> 2) & 1) + 4 * ((kr >> 3) & 3) + (kr & 3)) : kr;
  const int fsw = ((kr >> 1) & 1) | (((kr >> 3) & 1) << 1);
  const unsigned wvo = (krs * ldw + 16 * (((lane & 7) >> 1) ^ fsw) + 8 * (lane & 1)) * 2;
  const unsigned lb = (unsigned)(size_t)L;
  auto piece = [&](int kn, int buf, int i) {
    const int kt = (kn + rot) & (nkt - 1);
    const unsigned sb = lb + buf * STG;
    if (i >= 2) dma16<false>(W + (size_t)kt * 64 * ldw + 64 * (i - 2), wvo, sb + 16384 + (w * NB + i - 2) * 1024);
    else if (MODE == M_O) {
      const u8* ab = (const u8*)A + (unsigned)__builtin_amdgcn_readfirstlane(nr->frag(OFF_AX, lda + (w >> 1), kt >> 1)) + (w & 1) * 4096 + (kt & 1) * 2048;
      dma16<false>(ab + i * 1024, lane * 16, sb + (2 * w + i) * 1024);
    } else dma16<false>(A + kt * 64, i ? avo1 : avo0, sb + (2 * w + i) * 1024);
  };
  const unsigned aoff = lb + (16 * MT * wm + r16) * 128 + ((g ^ ((r16 >> 1) & 7)) * 16);
  const int fr = ((q >> 1) & 1) | ((g & 1) << 1);
  const int cgw = (wn * NT) >> 2, c0 = (wn * NT) & 3;
  unsigned boff[NT];
#pragma unroll
  for (int n = 0; n < NT; ++n) boff[n] = lb + 16384 + (g * NCG + cgw) * 1024 + q * 128 + (((c0 + n) ^ fr) * 32) + pp * 8;
#pragma unroll
  for (int m = 0; m < MT; ++m)
#pragma unroll
    for (int n = 0; n < NT; ++n) acc[m][n] = f32x4{0.f, 0.f, 0.f, 0.f};

  v4u af[2][MT], bf[2][NT];
  auto frag1 = [&](int buf, int ks, int i) {
    const unsigned so = buf * STG;
    if (i < NT) {
      const v2u x0 = trread(boff[i] + so + ks * 4 * NCG * 1024), x1 = trread(boff[i] + so + ks * 4 * NCG * 1024 + 512);
      bf[ks][i] = v4u{x0.x, x0.y, x1.x, x1.y};
    } else {
      const int m = i - NT;
      af[ks][m] = MODE == M_O ? *(LAS v4u*)(size_t)(lb + so + wm * 8192 + lane * 16 + m * 2048 + ks * 1024) : *(LAS v4u*)(size_t)(((aoff + so) ^ (ks * 64)) + m * 2048);
    }
  };
  auto mma = [&](int ks, int fb, bool fill, int kt, int buf) {
    if (fb >= 0) {
#pragma unroll
      for (int i = 0; i < MT + NT; ++i) frag1(fb, ks ^ 1, i);
    }
    __builtin_amdgcn_sched_barrier(0);
#pragma unroll
    for (int i = 0; i < MT * NT; ++i) {
      const int m = i / NT, n = i % NT;
      acc[m][n] = MODE == M_V ? mfma32(af[ks][m], bf[ks][n], acc[m][n]) : mfma32(bf[ks][n], af[ks][m], acc[m][n]);
      constexpr int EV = MT * NT >= 2 * (NB + 2) ? 2 : 1;
      if (fill && i % EV == EV - 1 && i / EV < NB + 2) { __builtin_amdgcn_sched_barrier(0); piece(kt, buf, i / EV); __builtin_amdgcn_sched_barrier(0); }
    }
    __builtin_amdgcn_sched_barrier(0);
  };
  static_assert(MT * NT >= NB + 2 && MT * NT >= MT + NT, "every piece and fragment needs a product to hide behind");
  wgbar();
#pragma unroll
  for (int st = 0; st < NSTG; ++st)
#pragma unroll
    for (int i = 0; i < NB + 2; ++i) piece(st, st, i);
  wait_vm<(NSTG - 1) * (2 + NB)>();
  __builtin_amdgcn_s_barrier(); CFENCE();
#pragma unroll
  for (int i = 0; i < MT + NT; ++i) frag1(0, 0, i);
  int buf = 0;
  for (int kt = 0; kt < nkt - NSTG; ++kt) {
    const int nb = buf == NSTG - 1 ? 0 : buf + 1;
    mma(0, buf, false, 0, 0);
    wait_vm<(NSTG - 2) * (2 + NB)>();
    WAIT_LGKM();
    __builtin_amdgcn_s_barrier(); CFENCE();
    mma(1, nb, true, kt + NSTG, buf);
    buf = nb;
  }
  static_for<NSTG>([&](auto ic) {
    constexpr int R = decltype(ic)::v;
    const int nb = buf == NSTG - 1 ? 0 : buf + 1;
    mma(0, buf, false, 0, 0);
    if (R < NSTG - 1) {
      wait_vm<(NSTG - 2 - R > 0 ? NSTG - 2 - R : 0) * (2 + NB)>();
      __builtin_amdgcn_s_barrier(); CFENCE();
    }
    mma(1, R < NSTG - 1 ? nb : -1, false, 0, 0);
    buf = nb;
  });
}

template <int BN, int MODE, int WM = 2>
__device__ __forceinline__ void proj_task(const Params& p, LAS u8* L, rsrc_t rs, Near& nr, bool& dead, int blk, int mtx, int col0) {
  constexpr int MT = 8 / WM, NT = BN * WM / 128;
  const int tid = fresh_tid(), lane = tid & 63, w = __builtin_amdgcn_readfirstlane(tid >> 6), wm = w / (8 / WM), wn = w % (8 / WM);
  f32x4 acc[MT][NT];
  const u16* W = (mtx == 0 ? p.Wk : mtx == 1 ? p.Wv : p.Wq) + col0;
  gemm_tile<BN, MODE, WM>(L, p.x + (size_t)blk * 128 * DM, DM, W, HD, DM / 64, acc, BN == 256 ? ROT_A * blk + ROT_B * (col0 >> 8) : 0);
  const int cw = col0 + wn * 16 * NT, h = cw >> 7;
  if (BN == 64 && blk == 0) near_wait((gu32*)p.ws, nr, dead);
  const unsigned reg = mtx == 0 ? OFF_KX : mtx == 1 ? OFF_VX : OFF_QX;
  const int s0 = blk * 4 + (MT / 2) * wm;
  if (MODE == M_K) {
#pragma unroll
    for (int m = 0; m < MT; ++m) {
      const unsigned o = nr.frag(reg, s0 + (m >> 1), h) + lane * 16 + ((m & 1) * 4 + ((cw & 127) >> 5)) * 1024;
#pragma unroll
      for (int v = 0; v < NT / 2; ++v)
        st16(rs, o + v * 1024, v4u{pk2(acc[m][2 * v][0], acc[m][2 * v + 1][0]), pk2(acc[m][2 * v][1], acc[m][2 * v + 1][1]),
                                   pk2(acc[m][2 * v][2], acc[m][2 * v + 1][2]), pk2(acc[m][2 * v][3], acc[m][2 * v + 1][3])});
    }
  } else {
#pragma unroll
    for (int sg = 0; sg < MT / 2; ++sg)
#pragma unroll
      for (int n = 0; n < NT; ++n)
        st16(rs, nr.frag(reg, s0 + sg, h) + lane * 16 + (((cw & 127) >> 4) + n) * 1024,
             v4u{pk2(acc[2 * sg][n][0], acc[2 * sg][n][1]), pk2(acc[2 * sg][n][2], acc[2 * sg][n][3]),
                 pk2(acc[2 * sg + 1][n][0], acc[2 * sg + 1][n][1]), pk2(acc[2 * sg + 1][n][2], acc[2 * sg + 1][n][3])});
  }
  DRAIN_VM();
  wgbar();
  gu32* ctrl = (gu32*)p.ws;
  if (tid < BN / 64) {
    const int word = mtx == 2 ? W_FQ + blk * 64 + (col0 >> 6) + tid : W_FKV + blk * 128 + mtx * 64 + (col0 >> 6) + tid;
    __hip_atomic_store(ctrl + word, 1u, RLX_AGENT);
  }
}

template <int MODE>
__device__ __forceinline__ void proj_big(const Params& p, LAS u8* L, rsrc_t rs, const Near& nr, int blk, int mtx, int col0) {
  constexpr int NS = 5, STG = 32768, NK = DM / 32;
  const int tid = fresh_tid();
  const int lane = tid & 63, w = __builtin_amdgcn_readfirstlane(tid >> 6), wm = w >> 2, wn = w & 3, r16 = lane & 15, g = lane >> 4, q = r16 >> 2;
  const u16* A = p.x + (size_t)blk * 128 * DM;
  const u16* W = (mtx == 0 ? p.Wk : mtx == 1 ? p.Wv : p.Wq) + col0;
  const unsigned lb = (unsigned)(size_t)L;
  const int ar = lane >> 2;
  const unsigned avo = ((32 * w + ar) * DM + 8 * ((lane & 3) ^ ((4 - (ar >> 2)) & 3))) * 2;
  const int kr = 8 * (w >> 1) + (lane >> 3);
  const unsigned wvo = (kr * HD + 128 * (w & 1) + 16 * (((lane & 7) >> 1) ^ (((kr >> 1) & 1) | (((kr >> 3) & 1) << 1))) + 8 * (lane & 1)) * 2;
  auto piece = [&](int kt, int buf, int i) {
    const unsigned sb = lb + buf * STG;
    if (i < 2) dma16<false>(A + kt * 32 + i * 16 * DM, avo, sb + (2 * w + i) * 1024);
    else dma16<false>(W + (size_t)kt * 32 * HD + 64 * (i - 2), wvo, sb + 16384 + (2 * w + i - 2) * 1024);
  };
  const unsigned aoff = lb + (128 * wm + r16) * 64 + ((g ^ ((4 - q) & 3)) * 16);
  const int fr = ((q >> 1) & 1) | ((g & 1) << 1);
  const unsigned boff = lb + 16384 + (g * 4 + wn) * 1024 + q * 128 + (lane & 3) * 8;
  f32x4 acc[8][4];
#pragma unroll
  for (int i = 0; i < 32; ++i) acc[i >> 2][i & 3] = f32x4{0.f, 0.f, 0.f, 0.f};
  v4u af[2][8], bf[2][4];
  auto frag1 = [&](int buf, int set, int i) {
    const unsigned so = buf * STG;
    if (i < 4) {
      const v2u x0 = trread(boff + so + ((i ^ fr) * 32)), x1 = trread(boff + so + ((i ^ fr) * 32) + 512);
      bf[set][i] = v4u{x0.x, x0.y, x1.x, x1.y};
    } else af[set][i - 4] = *(LAS v4u*)(size_t)(aoff + so + (i - 4) * 1024);
  };
  auto mma = [&](int set, int nb, bool fill, int kt, int buf) {
#pragma unroll
    for (int i = 0; i < 32; ++i) {
      const int m = i >> 2, n = i & 3;
      acc[m][n] = MODE == M_V ? mfma32(af[set][m], bf[set][n], acc[m][n]) : mfma32(bf[set][n], af[set][m], acc[m][n]);
      if ((i & 1) == 0 && i < 24 && nb >= 0) { __builtin_amdgcn_sched_barrier(0); frag1(nb, set ^ 1, i >> 1); __builtin_amdgcn_sched_barrier(0); }
      if (fill && (i & 7) == 5) { __builtin_amdgcn_sched_barrier(0); piece(kt, buf, i >> 3); __builtin_amdgcn_sched_barrier(0); }
    }
    __builtin_amdgcn_sched_barrier(0);
  };
  wgbar();
#pragma unroll
  for (int st = 0; st < NS; ++st)
#pragma unroll
    for (int i = 0; i < 4; ++i) piece(st, st, i);
  wait_vm<(NS - 1) * 4>();
  __builtin_amdgcn_s_barrier(); CFENCE();
#pragma unroll
  for (int i = 0; i < 12; ++i) frag1(0, 0, i);
  int buf = 0;
  auto step = [&](int kt, int set, auto inflight, bool fill) {
    const int nb = buf == NS - 1 ? 0 : buf + 1;
    wait_vm<decltype(inflight)::v * 4>();
    WAIT_LGKM();
    __builtin_amdgcn_s_barrier(); CFENCE();
    mma(set, nb, fill, kt + NS, buf);
    buf = nb;
  };
  for (int kt = 0; kt < NK - NS - 1; kt += 2) { step(kt, 0, IC<NS - 2>{}, true); step(kt + 1, 1, IC<NS - 2>{}, true); }
  static_assert((NK - NS - 1) % 2 == 0 && NS == 5, "tail written out for these sizes");
  step(NK - 6, 0, IC<3>{}, true);
  step(NK - 5, 1, IC<3>{}, false);
  step(NK - 4, 0, IC<2>{}, false);
  step(NK - 3, 1, IC<1>{}, false);
  step(NK - 2, 0, IC<0>{}, false);
  mma(1, -1, false, 0, 0);

  const int cw = col0 + wn * 64, h = cw >> 7, s0 = blk * 4 + 4 * wm;
  const unsigned reg = mtx == 0 ? OFF_KX : mtx == 1 ? OFF_VX : OFF_QX;
#pragma unroll
  for (int sg = 0; sg < 4; ++sg) {
    const unsigned o = nr.frag(reg, s0 + sg, h) + lane * 16;
    if (MODE == M_K) {
#pragma unroll
      for (int i = 0; i < 4; ++i) {
        const int m = 2 * sg + (i >> 1), v = i & 1;
        st16(rs, o + ((i >> 1) * 4 + ((cw & 127) >> 5) + v) * 1024,
             v4u{pk2(acc[m][2 * v][0], acc[m][2 * v + 1][0]), pk2(acc[m][2 * v][1], acc[m][2 * v + 1][1]),
                 pk2(acc[m][2 * v][2], acc[m][2 * v + 1][2]), pk2(acc[m][2 * v][3], acc[m][2 * v + 1][3])});
      }
    } else {
#pragma unroll
      for (int n = 0; n < 4; ++n)
        st16(rs, o + (((cw & 127) >> 4) + n) * 1024,
             v4u{pk2(acc[2 * sg][n][0], acc[2 * sg][n][1]), pk2(acc[2 * sg][n][2], acc[2 * sg][n][3]),
                 pk2(acc[2 * sg + 1][n][0], acc[2 * sg + 1][n][1]), pk2(acc[2 * sg + 1][n][2], acc[2 * sg + 1][n][3])});
    }
  }
  DRAIN_VM();
  wgbar();
  if (tid < 8) {
    const int b2 = blk + (tid >> 2);
    const int word = (mtx == 2 ? W_FQ + b2 * 64 : W_FKV + b2 * 128 + mtx * 64) + (col0 >> 6) + (tid & 3);
    __hip_atomic_store((gu32*)p.ws + word, 1u, RLX_AGENT);
  }
}

__device__ __forceinline__ float sigmoidf_(float v) { return 1.0f / (1.0f + __expf(-v)); }

__device__ __forceinline__ void rg_task(const Params& p, rsrc_t rs, int wk) {
  const int tid = fresh_tid(), lane = tid & 63, w = __builtin_amdgcn_readfirstlane(tid >> 6);
  v4u wr[2], gw[2];
#pragma unroll
  for (int i = 0; i < 2; ++i) { wr[i] = *(const GAS v4u*)(p.wr + i * 512 + lane * 8); gw[i] = *(const GAS v4u*)(p.gw + i * 512 + lane * 8); }
#pragma unroll
  for (int t = 0; t < 2; ++t) {
    const int tok = wk * 16 + 2 * w + t;
    float r = 0.f, gs = 0.f;
#pragma unroll
    for (int i = 0; i < 2; ++i) {
      const v4u xv = *(const GAS v4u*)(p.x + (size_t)tok * DM + i * 512 + lane * 8);
#pragma unroll
      for (int d = 0; d < 4; ++d) {
        r = fmaf(bflo(xv[d]), bflo(wr[i][d]), r); r = fmaf(bfhi(xv[d]), bfhi(wr[i][d]), r);
        gs += bfr(bflo(xv[d]) * bflo(gw[i][d])); gs += bfr(bfhi(xv[d]) * bfhi(gw[i][d]));
      }
    }
    r = bfr(wave_sum(r)); gs = bfr(wave_sum(gs));
    if (lane == 0) { st4(rs, OFF_R + tok * 4, f2u(r)); st4(rs, OFF_G + tok * 4, f2u(bfr(sigmoidf_(gs)))); }
  }
  DRAIN_VM();
  wgbar();
  if (tid == 0) __hip_atomic_store((gu32*)p.ws + W_RG + wk * LN, 1u, RLX_AGENT);
}

__device__ __forceinline__ unsigned long long top_key(unsigned bits, int idx) {
  const unsigned o = (bits & 0x7FFFFFFFu) > 0x7F800000u ? 0u : ((bits & 0x80000000u) ? ~bits : (bits | 0x80000000u));
  return ((unsigned long long)o << 32) | (unsigned)(0x7FFFFFFF - idx);
}
__device__ __forceinline__ void top_ins(unsigned long long k, unsigned long long& a, unsigned long long& b) {
  const unsigned long long lo = k < a ? k : a;
  a = k < a ? a : k;
  b = lo > b ? lo : b;
}
__device__ __forceinline__ void top2(rsrc_t rs, LAS u8* L, int& i0, int& i1) {
  const int tid = fresh_tid(), lane = tid & 63, w = tid >> 6;
  const v4u rv = ld16(rs, OFF_R + tid * 16);
  unsigned long long a = 0ull, b = 0ull;
#pragma unroll
  for (int d = 0; d < 4; ++d) top_ins(top_key(rv[d], tid * 4 + d), a, b);
#pragma unroll
  for (int o = 1; o < 64; o <<= 1) {
    const unsigned long long c0 = __shfl_xor(a, o), c1 = __shfl_xor(b, o);
    top_ins(c0, a, b); top_ins(c1, a, b);
  }
  LAS unsigned long long* tv = (LAS unsigned long long*)L;
  wgbar();
  if (lane == 0) { tv[2 * w] = a; tv[2 * w + 1] = b; }
  wgbar();
  a = b = 0ull;
#pragma unroll
  for (int k = 0; k < 16; ++k) top_ins(tv[k], a, b);
  wgbar();
  i0 = (0x7FFFFFFF - (int)(unsigned)a) & 2047; i1 = (0x7FFFFFFF - (int)(unsigned)b) & 2047;
}

__device__ __forceinline__ void mlp1_task(const Params& p, LAS u8* L, rsrc_t rs, int t, bool& dead) {
  const int tid = fresh_tid();
  gu32* ctrl = (gu32*)p.ws;
  {
    const int rg = tid >> 3, cq = tid & 7;
    v4u gv[16], uv[16];
#pragma unroll
    for (int i = 0; i < 16; ++i) {
      gv[i] = *(const GAS v4u*)(p.wg + (size_t)(rg + 64 * i) * FF + 64 * t + 8 * cq);
      uv[i] = *(const GAS v4u*)(p.wu + (size_t)(rg + 64 * i) * FF + 64 * t + 8 * cq);
    }
    __builtin_amdgcn_sched_barrier(0);
    if (tid < 128) wait_lines(ctrl, W_RG + (tid >> 6) * 64 * LN, LN, 64, 1u, tid & 63, 0x40u, dead);
    wgbar();
    int i0, i1;
    top2(rs, L, i0, i1);
    if (t == 0 && tid == 0) {
      const float r0 = u2f(ld4(rs, OFF_R + i0 * 4)), r1 = u2f(ld4(rs, OFF_R + i1 * 4));
      st16(rs, W_TOP * 4, v4u{(unsigned)i0, (unsigned)i1, f2u(bfr(sigmoidf_(r0))), f2u(bfr(sigmoidf_(r1)))});
    }
    LAS float* X = (LAS float*)(L + 69632);
#pragma unroll
    for (int k = 0; k < 4; ++k) X[tid + 512 * k] = bf2f(p.x[(size_t)(k < 2 ? i0 : i1) * DM + tid + 512 * (k & 1)]);
    wgbar();
    float ag[2][8], au[2][8];
#pragma unroll
    for (int e = 0; e < 8; ++e) { ag[0][e] = ag[1][e] = au[0][e] = au[1][e] = 0.f; }
#pragma unroll
    for (int i = 0; i < 16; ++i) {
      const float x0 = X[rg + 64 * i], x1 = X[1024 + rg + 64 * i];
#pragma unroll
      for (int d = 0; d < 4; ++d) {
        ag[0][2 * d] = fmaf(x0, bflo(gv[i][d]), ag[0][2 * d]); ag[0][2 * d + 1] = fmaf(x0, bfhi(gv[i][d]), ag[0][2 * d + 1]);
        ag[1][2 * d] = fmaf(x1, bflo(gv[i][d]), ag[1][2 * d]); ag[1][2 * d + 1] = fmaf(x1, bfhi(gv[i][d]), ag[1][2 * d + 1]);
        au[0][2 * d] = fmaf(x0, bflo(uv[i][d]), au[0][2 * d]); au[0][2 * d + 1] = fmaf(x0, bfhi(uv[i][d]), au[0][2 * d + 1]);
        au[1][2 * d] = fmaf(x1, bflo(uv[i][d]), au[1][2 * d]); au[1][2 * d + 1] = fmaf(x1, bfhi(uv[i][d]), au[1][2 * d + 1]);
      }
    }
    LAS float* R = (LAS float*)L;
#pragma unroll
    for (int e = 0; e < 8; ++e) {
      R[(0 * 64 + rg) * 65 + 8 * cq + e] = ag[0][e]; R[(1 * 64 + rg) * 65 + 8 * cq + e] = ag[1][e];
      R[(2 * 64 + rg) * 65 + 8 * cq + e] = au[0][e]; R[(3 * 64 + rg) * 65 + 8 * cq + e] = au[1][e];
    }
  }
  wgbar();
  if (tid < 128) {
    const LAS float* R = (const LAS float*)L;
    const int tok = tid >> 6, col = tid & 63;
    float gs = 0.f, us = 0.f;
#pragma unroll 16
    for (int r = 0; r < 64; ++r) { gs += R[(tok * 64 + r) * 65 + col]; us += R[((2 + tok) * 64 + r) * 65 + col]; }
    gs = bfr(gs); us = bfr(us);
    const float act = bfr(gs * sigmoidf_(gs));
    st4(rs, OFF_HID + (tok * FF + 64 * t + col) * 4, f2u(bfr(act * us)));
  }
  DRAIN_VM();
  wgbar();
  if (tid == 0) __hip_atomic_store(ctrl + W_M1 + t * LN, 1u, RLX_AGENT);
}
__device__ __forceinline__ void mlp2_task(const Params& p, LAS u8* L, rsrc_t rs, int t, bool& dead) {
  const int tid = fresh_tid();
  gu32* ctrl = (gu32*)p.ws;
  const int kc = t >> 2, cb = t & 3, rg = tid >> 5, cq = tid & 31;
  v4u dv[16];
#pragma unroll
  for (int i = 0; i < 16; ++i) dv[i] = *(const GAS v4u*)(p.wd + (size_t)(256 * kc + rg + 16 * i) * DM + 256 * cb + 8 * cq);
  __builtin_amdgcn_sched_barrier(0);
  if (tid < 64) wait_lines(ctrl, W_M1 + 4 * kc * LN, LN, 4, 1u, tid, 0x41u, dead);
  wgbar();
  LAS float* Hh = (LAS float*)(L + 69632);
  Hh[tid] = u2f(ld4(rs, OFF_HID + ((tid >> 8) * FF + 256 * kc + (tid & 255)) * 4));
  wgbar();
  float a[2][8];
#pragma unroll
  for (int e = 0; e < 8; ++e) a[0][e] = a[1][e] = 0.f;
#pragma unroll
  for (int i = 0; i < 16; ++i) {
    const float h0 = Hh[rg + 16 * i], h1 = Hh[256 + rg + 16 * i];
#pragma unroll
    for (int d = 0; d < 4; ++d) {
      a[0][2 * d] = fmaf(h0, bflo(dv[i][d]), a[0][2 * d]); a[0][2 * d + 1] = fmaf(h0, bfhi(dv[i][d]), a[0][2 * d + 1]);
      a[1][2 * d] = fmaf(h1, bflo(dv[i][d]), a[1][2 * d]); a[1][2 * d + 1] = fmaf(h1, bfhi(dv[i][d]), a[1][2 * d + 1]);
    }
  }
  LAS float* R2 = (LAS float*)L;
#pragma unroll
  for (int e = 0; e < 8; ++e) { R2[(0 * 16 + rg) * 257 + 8 * cq + e] = a[0][e]; R2[(1 * 16 + rg) * 257 + 8 * cq + e] = a[1][e]; }
  wgbar();
  {
    const int tok = tid >> 8, col = tid & 255;
    float sacc = 0.f;
#pragma unroll
    for (int r = 0; r < 16; ++r) sacc += R2[(tok * 16 + r) * 257 + col];
    st4(rs, OFF_YP + ((kc * 2 + tok) * DM + 256 * cb + col) * 4, f2u(sacc));
  }
  DRAIN_VM();
  wgbar();
  if (tid == 0) __hip_atomic_store(ctrl + W_M2 + t * LN, 1u, RLX_AGENT);
}

__device__ __forceinline__ void seg_softmax(const v4u (&F)[4], const v4u (&KX)[2][4], const v4u (&QX)[4], int tq, int g, v4u& pb, v2u& pm) {
  const float scale = 0.08838834764831845f, l2e = 1.4426950408889634f, neg = bfr(-1000000000.0f);
  f32x4 sA = {0.f, 0.f, 0.f, 0.f}, sB = sA, sM = sA;
#pragma unroll
  for (int u = 0; u < 4; ++u) { sM = mfma32(F[u], QX[u], sM); sA = mfma32(KX[0][u], QX[u], sA); sB = mfma32(KX[1][u], QX[u], sB); }
#pragma unroll
  for (int r = 0; r < 4; ++r) {
    sA[r] = 4 * g + r > tq ? neg : bfr(bfr(sA[r]) * scale);
    sB[r] = 16 + 4 * g + r > tq ? neg : bfr(bfr(sB[r]) * scale);
    sM[r] = g < 2 ? -INFINITY : bfr(bfr(sM[r]) * scale);
  }
  float mx = fmaxf(fmaxf(fmaxf(sA[0], sA[1]), fmaxf(sA[2], sA[3])), fmaxf(fmaxf(sB[0], sB[1]), fmaxf(sB[2], sB[3])));
  mx = fmaxf(mx, fmaxf(fmaxf(sM[0], sM[1]), fmaxf(sM[2], sM[3])));
  mx = xmax32(xmax16(mx));
  float sum = 0.f;
#pragma unroll
  for (int r = 0; r < 4; ++r) {
    sA[r] = __builtin_amdgcn_exp2f((sA[r] - mx) * l2e); sB[r] = __builtin_amdgcn_exp2f((sB[r] - mx) * l2e); sM[r] = __builtin_amdgcn_exp2f((sM[r] - mx) * l2e);
    sum += sA[r] + sB[r] + sM[r];
  }
  const float inv = 1.0f / xadd32(xadd16(sum));
  pb = v4u{pk2(sA[0] * inv, sA[1] * inv), pk2(sA[2] * inv, sA[3] * inv), pk2(sB[0] * inv, sB[1] * inv), pk2(sB[2] * inv, sB[3] * inv)};
  pm = v2u{pk2(sM[0] * inv, sM[1] * inv), pk2(sM[2] * inv, sM[3] * inv)};
}

__device__ __forceinline__ void attn_task(const Params& p, rsrc_t rs, const Near& nr, int s, int h, bool& dead) {
  const int lane = fresh_tid() & 63, r16 = lane & 15, g = lane >> 4, gg = g & 1;
  gu32* ctrl = (gu32*)p.ws;
  const int blk = s >> 2;
  wait_flags2(ctrl, W_FKV + blk * 128 + 2 * h, W_FKV + blk * 128 + 64 + 2 * h, 0x50u, dead);
  wait_flags2(ctrl, W_FQ + blk * 64 + 2 * h, W_FQ + blk * 64 + 2 * h, 0x51u, dead);
  CFENCE();
  const unsigned fk = nr.frag(OFF_KX, s, h) + lane * 16, fq = nr.frag(OFF_QX, s, h) + lane * 16, fv = nr.frag(OFF_VX, s, h) + lane * 16;
  v4u KX[2][4], QX[2][4], VX[8], F[4]; v2u VM[8];
#pragma unroll
  for (int i = 0; i < 8; ++i) { KX[i >> 2][i & 3] = ld16(rs, fk + i * 1024); QX[i >> 2][i & 3] = ld16(rs, fq + i * 1024); VX[i] = ld16(rs, fv + i * 1024); }
  wait_ge(ctrl, W_PROG + LN * h, (unsigned)(s + 1), 0x52u, dead);
  const unsigned qo = nr.blk(OFF_QKV + (unsigned)(s * QH + (h & (QH - 1))) * 16384u, nr.rel(h * 4));
  for (unsigned sp = 0;;) {
    CFENCE();
    unsigned m = 0;
#pragma unroll
    for (int u = 0; u < 4; ++u) { F[u] = ld16(rs, qo + u * 1024 + lane * 16); m = max(m, umax4(F[u])); }
#pragma unroll
    for (int e = 0; e < 8; ++e) { VM[e] = ld8(rs, qo + 4096 + (16 * e + r16) * 16 + gg * 8); m = max(m, max(VM[e].x, VM[e].y)); }
    if (__all(m != SENT) || dead) break;
    if (spin_fail(sp, ctrl, 0x53u)) { dead = true; break; }
  }
#pragma unroll
  for (int qt = 0; qt < 2; ++qt) {
    v4u pb; v2u pm;
    seg_softmax(F, KX, QX[qt], 16 * qt + r16, g, pb, pm);
    const unsigned ao = nr.frag(OFF_AX, s, h) + lane * 16 + qt * 4096;
#pragma unroll
    for (int v = 0; v < 4; ++v) {
      f32x4 o0 = mfma32(VX[2 * v], pb, f32x4{0.f, 0.f, 0.f, 0.f}), o1 = mfma32(VX[2 * v + 1], pb, f32x4{0.f, 0.f, 0.f, 0.f});
      o0 = mfma16(VM[2 * v], pm, o0); o1 = mfma16(VM[2 * v + 1], pm, o1);
      st16(rs, ao + v * 1024, v4u{pk2(o0[0], o0[1]), pk2(o0[2], o0[3]), pk2(o1[0], o1[1]), pk2(o1[2], o1[3])});
    }
  }
  DRAIN_VM();
}

__device__ __forceinline__ void mix_store(const Params& p, rsrc_t rs, const v4u& top, int tok, int col, f32x4 a, float gt, v2u xv) {
  const float og = bfr(1.0f - gt);
  const int sel = tok == (int)top.x ? 0 : tok == (int)top.y ? 1 : -1;
  float bv[4] = {bflo(xv.x), bfhi(xv.x), bflo(xv.y), bfhi(xv.y)};
  if (sel >= 0) {
    f32x4 y = {0.f, 0.f, 0.f, 0.f};
    for (int kc = 0; kc < 14; ++kc) y += __builtin_bit_cast(f32x4, ld16(rs, OFF_YP + ((kc * 2 + sel) * DM + col) * 4));
    const float rsel = u2f(sel == 0 ? top.z : top.w);
#pragma unroll
    for (int r = 0; r < 4; ++r) bv[r] = bfr(bv[r] + bfr(bfr(y[r]) * rsel));
  }
  float o[4];
#pragma unroll
  for (int r = 0; r < 4; ++r) o[r] = bfr(gt * bfr(a[r])) + bfr(og * bv[r]);
  *(GAS v2u*)(p.out + (size_t)tok * DM + col) = v2u{pk2(o[0], o[1]), pk2(o[2], o[3])};
}

__device__ __forceinline__ void oproj_task(const Params& p, LAS u8* L, rsrc_t rs, const Near& nr, int blk, int ct, bool& dead) {
  const int tid = fresh_tid(), lane = tid & 63, w = __builtin_amdgcn_readfirstlane(tid >> 6), wm = w >> 2, wn = w & 3, r16 = lane & 15, g = lane >> 4;
  gu32* ctrl = (gu32*)p.ws;
  if (w == 0) {
    wait_lines(ctrl, W_M2, LN, 56, 1u, lane, 0x60u, dead);
    wait_lines(ctrl, W_T2 + 8 * blk * LN, LN, 8, 1u, lane, 0x61u, dead);
    __builtin_amdgcn_fence(__ATOMIC_ACQUIRE, "agent");
    DRAIN_VM();
  }
  f32x4 acc[4][2];
  gemm_tile<128, M_O>(L, (const u16*)p.ws, blk * 4, p.Wo + ct * 128, DM, HD / 64, acc, ROT_O * ct, &nr);
  const v4u top = ld16(rs, W_TOP * 4);
#pragma unroll
  for (int m = 0; m < 4; ++m)
#pragma unroll
    for (int n = 0; n < 2; ++n) {
      const int tok = blk * 128 + 64 * wm + 16 * m + r16, col = ct * 128 + 32 * wn + 16 * n + 4 * g;
      mix_store(p, rs, top, tok, col, acc[m][n], u2f(ld4(rs, OFF_G + tok * 4)), *(const GAS v2u*)(p.x + (size_t)tok * DM + col));
    }
}

__device__ __forceinline__ void stream_role(const Params& p, LAS u8* L, rsrc_t rs, const Near& nr, int set, int q, bool& dead) {
  int wst = 100; const int wk = set * 32 + q; (void)wst; (void)wk;
  const int tid = fresh_tid(), lane = tid & 63, w = __builtin_amdgcn_readfirstlane(tid >> 6), r16 = lane & 15, g = lane >> 4, gg = g & 1;
  const int ks = q >> 3, ct = q & 7, np = w & 3, kh = w >> 2;
  gu32* ctrl = (gu32*)p.ws;
  const unsigned lb = (unsigned)(size_t)L;
  v4u Wf[16][2];
  LAS u8* Wl = L + 65536 + w * 8192 + lane * 16;
  {
    const int kr = 8 * w + (lane >> 3);
    const int krs = (kr & ~31) + 16 * ((kr >> 2) & 1) + 4 * ((kr >> 3) & 3) + (kr & 3);
    const int fsw = ((kr >> 1) & 1) | (((kr >> 3) & 1) << 1);
    const unsigned wvo = (krs * DM + 16 * (((lane & 7) >> 1) ^ fsw) + 8 * (lane & 1)) * 2;
    const int qq = r16 >> 2, fr = ((qq >> 1) & 1) | ((g & 1) << 1);
    const unsigned boff = lb + kh * 65536 + (g * 2 + (np >> 1)) * 1024 + qq * 128 + (lane & 3) * 8;
    const unsigned b0 = boff + (((2 * (np & 1)) ^ fr) * 32), b1 = boff + (((2 * (np & 1) + 1) ^ fr) * 32);
    static_for<2>([&](auto rc) {
      constexpr int RD = decltype(rc)::v;
      wgbar();
#pragma unroll
      for (int kt = 0; kt < 8; ++kt) {
        const u16* wb = p.Wo + (size_t)(1024 * ks + 512 * (kt >> 2) + 64 * (4 * RD + (kt & 3))) * DM + 128 * ct;
        dma16<false>(wb, wvo, lb + kt * 16384 + (w * 2) * 1024);
        dma16<false>(wb + 64, wvo, lb + kt * 16384 + (w * 2 + 1) * 1024);
      }
      DRAIN_VM();
      wgbar();
#pragma unroll
      for (int i = 0; i < 8; ++i) {
        const unsigned o = (i >> 1) * 16384 + (i & 1) * 8192;
        const v2u x0 = trread(b0 + o), x1 = trread(b0 + o + 512), y0 = trread(b1 + o), y1 = trread(b1 + o + 512);
        Wf[8 * RD + i][0] = v4u{x0.x, x0.y, x1.x, x1.y}; Wf[8 * RD + i][1] = v4u{y0.x, y0.y, y1.x, y1.y};
      }
    });
  }
  wgbar();
#pragma unroll
  for (int i = 0; i < 8; ++i) *(LAS v4u*)(Wl + i * 1024) = Wf[12 + (i >> 1)][i & 1];
  wait_lines(ctrl, W_M2, LN, 56, 1u, lane, 0x70u, dead);
  CFENCE();
  const v4u top = ld16(rs, W_TOP * 4);

  auto run = [&](auto role) {
    constexpr bool ATT = decltype(role)::v == 0;
    const int qt = w & 1, eh = w >> 1;
    v4u KX[2][4], QX[4], VX[4];
    auto prefetch = [&](int s) {
      if ((s & 3) < 2) {
        const int kind = (lane >> 1) % 3;
        gu32* f = ctrl + (kind == 2 ? W_FQ + (s >> 2) * 64 : W_FKV + (s >> 2) * 128 + kind * 64) + 2 * q + (lane & 1);
        for (unsigned sp = 0; !dead;) {
          if (__all(__hip_atomic_load(f, RLX_AGENT) != 0u)) break;
          __builtin_amdgcn_s_sleep(8);
          if (spin_fail(sp, ctrl, 0x73u)) dead = true;
        }
        CFENCE();
      }
      const unsigned fk = nr.frag(OFF_KX, s, q) + lane * 16, fq = nr.frag(OFF_QX, s, q) + lane * 16, fv = nr.frag(OFF_VX, s, q) + lane * 16;
#pragma unroll
      for (int i = 0; i < 8; ++i) KX[i >> 2][i & 3] = ld16(rs, fk + i * 1024);
#pragma unroll
      for (int i = 0; i < 4; ++i) { QX[i] = ld16(rs, fq + qt * 4096 + i * 1024); VX[i] = ld16(rs, fv + (4 * eh + i) * 1024); }
    };
    auto finish = [&](int s) {
      const int f = 4 * ks + (w & 3);
      wait_lines(ctrl, W_SP + (set * 32 + ct) * LN, 8 * LN, 4, (unsigned)(s + 1), lane, 0x72u, dead);
      CFENCE();
      f32x4 sum = {0.f, 0.f, 0.f, 0.f};
#pragma unroll
      for (int k = 0; k < 8; ++k) sum += __builtin_bit_cast(f32x4, ld16(rs, nr.frag(OFF_PS, 0, (((s & 7) * 8 + ct) * 8 + k) * 2 + (f >> 3)) + (f & 7) * 1024 + lane * 16));
      { const int tok = s * 32 + 16 * (f & 1) + r16, col = ct * 128 + 16 * (f >> 1) + 4 * g;
        mix_store(p, rs, top, tok, col, sum, u2f(ld4(rs, OFF_G + tok * 4)), *(const GAS v2u*)(p.x + (size_t)tok * DM + col)); }
    };
    unsigned prog = 0;
    if (ATT) prefetch(NBULK * 4 + set);
    for (int s = NBULK * 4 + set; s < NSEG; s += 2) {
      const int si = s - NBULK * 4;
      if (ATT) {
        const unsigned need = (unsigned)(s + 1 < NSEG - 1 ? s + 1 : NSEG - 1);
        if (prog < need) {
          for (unsigned sp = 0; !dead;) {
            prog = __builtin_amdgcn_readfirstlane(__hip_atomic_load(ctrl + W_PROG + LN * q, RLX_AGENT));
            if (prog >= need) break;
            __builtin_amdgcn_s_sleep(2);
            if (spin_fail(sp, ctrl, 0x52u)) dead = true;
          }
        }
        const unsigned qo = nr.blk(OFF_QKV + (unsigned)(s * QH + (q & (QH - 1))) * 16384u, nr.rel(q * 4));
        v4u F[4]; v2u VM[4];
        for (unsigned sp = 0;;) {
          CFENCE();
          unsigned m = 0;
#pragma unroll
          for (int u = 0; u < 4; ++u) { F[u] = ld16(rs, qo + u * 1024 + lane * 16); m = max(m, umax4(F[u])); }
#pragma unroll
          for (int e = 0; e < 4; ++e) { VM[e] = ld8(rs, qo + 4096 + (16 * (4 * eh + e) + r16) * 16 + gg * 8); m = max(m, max(VM[e].x, VM[e].y)); }
          if (__all(m != SENT) || dead) break;
          if (spin_fail(sp, ctrl, 0x53u)) { dead = true; break; }
        }
        v4u pb; v2u pm;
        seg_softmax(F, KX, QX, 16 * qt + r16, g, pb, pm);
        const unsigned ao = nr.frag(OFF_AX, s, q) + qt * 4096 + eh * 2048 + lane * 16;
#pragma unroll
        for (int v = 0; v < 2; ++v) {
          f32x4 o0 = mfma32(VX[2 * v], pb, f32x4{0.f, 0.f, 0.f, 0.f}), o1 = mfma32(VX[2 * v + 1], pb, f32x4{0.f, 0.f, 0.f, 0.f});
          o0 = mfma16(VM[2 * v], pm, o0); o1 = mfma16(VM[2 * v + 1], pm, o1);
          st16(rs, ao + v * 1024, v4u{pk2(o0[0], o0[1]), pk2(o0[2], o0[3]), pk2(o1[0], o1[1]), pk2(o1[2], o1[3])});
        }
        DRAIN_VM();
      } else {
        if (si >= 4) finish(s - 4);
        DRAIN_VM();
      }
      wgbar();
      WSTAMP();
      if (tid == 0) __hip_atomic_store(ctrl + W_SA + (set * 32 + q) * LN, (unsigned)(s + 1), RLX_AGENT);
      if (tid == 64 && si >= 2) __hip_atomic_store(ctrl + W_SP + (set * 32 + q) * LN, (unsigned)(s - 1), RLX_AGENT);
      if (w == 4) wait_lines(ctrl, W_SA + (set * 32 + ks * 8) * LN, LN, 8, (unsigned)(s + 1), lane, 0x71u, dead);
      wgbar();
      WSTAMP();
      {
        const unsigned ao = nr.frag(OFF_AX, s, 8 * ks + w) + lane * 16;
        v4u t[8];
#pragma unroll
        for (int i = 0; i < 8; ++i) t[i] = ld16(rs, ao + i * 1024);
#pragma unroll
        for (int i = 0; i < 8; ++i) *(LAS v4u*)(L + w * 8192 + i * 1024 + lane * 16) = t[i];
      }
      if (ATT && s + 2 < NSEG) prefetch(s + 2);
      wgbar();
      f32x4 a[2][2];
#pragma unroll
      for (int i = 0; i < 4; ++i) a[i >> 1][i & 1] = f32x4{0.f, 0.f, 0.f, 0.f};
#pragma unroll
      for (int k = 0; k < 16; ++k) {
        const LAS u8* fp = L + kh * 32768 + (k >> 2) * 8192 + (k & 3) * 1024 + lane * 16;
        const v4u x0 = *(const LAS v4u*)fp, x1 = *(const LAS v4u*)(fp + 4096);
#pragma unroll
        for (int n = 0; n < 2; ++n) {
          const v4u wf = k < 12 ? Wf[k][n] : *(const LAS v4u*)(Wl + ((k - 12) * 2 + n) * 1024);
          a[n][0] = mfma32(wf, x0, a[n][0]); a[n][1] = mfma32(wf, x1, a[n][1]);
        }
      }
      WSTAMP();
      const unsigned pb = nr.frag(OFF_PS, 0, (((s & 7) * 8 + ct) * 8 + 2 * ks + kh) * 2 + (np >> 1)) + 4 * (np & 1) * 1024 + lane * 16;
#pragma unroll
      for (int i = 0; i < 4; ++i) st16(rs, pb + i * 1024, __builtin_bit_cast(v4u, a[i >> 1][i & 1]));
      WSTAMP();
    }
    DRAIN_VM();
    wgbar();
    if (tid == 0) __hip_atomic_store(ctrl + W_SP + (set * 32 + q) * LN, (unsigned)(NSEG - 1 + set), RLX_AGENT);
    finish(ATT ? NSEG - 4 + set : NSEG - 2 + set);
  };
  if (w < 4) run(IC<0>{}); else run(IC<1>{});
}

__device__ __forceinline__ void worker_role(const Params& p, LAS u8* L, rsrc_t rs, int b) {
  const int w = __builtin_amdgcn_readfirstlane(threadIdx.x >> 6);
  const int x8 = 2 * (b & 3) + (b >> 7), r = (b >> 3) & 15, wk = r * 8 + x8;
  gu32* ctrl = (gu32*)p.ws;
  bool dead = false;
  int wst = 0; (void)wst;
  Near nr = near_init(p.ws);
  WSTAMP();
  {
    const int cg = x8 * 16 + r;
    for (int blk = 0; blk < 3; ++blk) {
      if (cg < 64) proj_task<64, M_K, 4>(p, L, rs, nr, dead, blk, 0, cg * 64); else proj_task<64, M_V, 4>(p, L, rs, nr, dead, blk, 1, (cg - 64) * 64);
      WSTAMP();
    }
  }
  const int ct4 = x8 * 4 + (r & 3);
  for (int rnd = 0; rnd < 2; ++rnd) {
    if (rnd == 0 || r < 4) {
      const int blk = rnd == 0 ? 3 + (r >> 2) : 15;
      if (ct4 < 16) proj_task<256, M_K>(p, L, rs, nr, dead, blk, 0, ct4 * 256); else proj_task<256, M_V>(p, L, rs, nr, dead, blk, 1, (ct4 - 16) * 256);
    } else if (r < 8) {
      mlp2_task(p, L, rs, (r - 4) * 8 + x8, dead);
      if (r < 7) mlp2_task(p, L, rs, r * 8 + x8, dead);
    }
    if (rnd == 0) rg_task(p, rs, wk);
    WSTAMP();
    const int ct = rnd ? x8 * 2 + (r & 1) : ct4;
    const int blk = rnd ? (r >> 3) * 8 + 2 * ((r >> 1) & 3) : 7 + 2 * (r >> 2);
    if (rnd || ct < 16) proj_big<M_K>(p, L, rs, nr, blk, rnd ? 2 : 0, ct * 256); else proj_big<M_V>(p, L, rs, nr, blk, 1, (ct - 16) * 256);
    WSTAMP();
    if (rnd == 0 && r < 7) mlp1_task(p, L, rs, r * 8 + x8, dead);
  }
  WSTAMP();
  if (r < 8) {
    const int blk = x8, m = r;
    for (int i = 0; i < 2; ++i) { const int t = (i * 8 + m) * 8 + w; attn_task(p, rs, nr, blk * 4 + (t >> 5), t & 31, dead); WSTAMP(); }
    wgbar();
    if (threadIdx.x == 0) __hip_atomic_store(ctrl + W_T2 + (8 * blk + m) * LN, 1u, RLX_AGENT);
    oproj_task(p, L, rs, nr, blk, m, dead);
    WSTAMP();
  } else {
    const int sid = x8 * 8 + (r - 8);
    stream_role(p, L, rs, nr, sid >> 5, sid & 31, dead);
    WSTAMP();
  }
}

__global__ void __launch_bounds__(512) rmt_init_kernel(u8* ws, const u16* mem0) {
  const int tid = fresh_tid();
  if (blockIdx.x >= 4) {
    const Near n = near_init(ws);
    arm_share(__builtin_amdgcn_make_buffer_rsrc(ws, 0, WS_END, 0x00020000), n.al, blockIdx.x - 4, tid);
    return;
  }
  if (blockIdx.x >= 2) {
    const Near n = near_init(ws);
    const int i = (blockIdx.x - 2) * 512 + tid, cb = i >> 3, row = i & 7;
    const u16* m0 = mem0 + row * DM + (cb >> 2) * 32 + (cb & 3) * 4;
    const v2u lo = *(const GAS v2u*)m0, hi = *(const GAS v2u*)(m0 + 16);
    const v4u v = {canon(__builtin_amdgcn_perm(hi.x, lo.x, 0x05040100u)), canon(__builtin_amdgcn_perm(hi.x, lo.x, 0x07060302u)),
                   canon(__builtin_amdgcn_perm(hi.y, lo.y, 0x05040100u)), canon(__builtin_amdgcn_perm(hi.y, lo.y, 0x07060302u))};
    u8* d = ws + n.al + OFF_MEM + (cb >> 6) * 16384 + (cb & 63) * 128 + row * 16;
    *(GAS v4u*)d = v; *(GAS v4u*)(d + 8192) = v;
    return;
  }
  if (blockIdx.x == 0) {
    for (int i = tid; i < (int)(CTRL_BYTES / 16); i += 512)
      if (i != W_CFG / 4) ((GAS v4u*)ws)[i] = v4u{0u, 0u, 0u, 0u};
  } else if (tid == 0) {
    const Near n = near_init(ws);
    gu32* a0 = (gu32*)(ws + n.al + OFF_CAL); gu32* a1 = a0 + 2048;
    unsigned long long b0 = ~0ull, b1 = ~0ull;
    for (int i = 0; i < 3; ++i) {
      const unsigned long long t0 = __builtin_amdgcn_s_memtime();
      __hip_atomic_store(a0, 0u, RLX_AGENT); DRAIN_VM();
      const unsigned long long t1 = __builtin_amdgcn_s_memtime();
      __hip_atomic_store(a1, 0u, RLX_AGENT); DRAIN_VM();
      const unsigned long long t2 = __builtin_amdgcn_s_memtime();
      b0 = t1 - t0 < b0 ? t1 - t0 : b0; b1 = t2 - t1 < b1 ? t2 - t1 : b1;
    }
    const unsigned nz = ((b1 < b0 ? 1u : 0u) ^ ((n.va21 + OFF_CAL) >> 21) ^ (xcc_id() >> 2)) & 1u;
    ((GAS v4u*)ws)[W_CFG / 4] = v4u{nz, 0u, 0u, 0u};
  }
}

__global__ void __launch_bounds__(NTHR, 2) rmt_mod_kernel(Params p) {
  extern __shared__ __attribute__((aligned(16))) u8 smem[];
  LAS u8* L = (LAS u8*)smem;
  const rsrc_t rs = __builtin_amdgcn_make_buffer_rsrc(p.ws, 0, WS_END, 0x00020000);
  const int b = blockIdx.x;
  if ((b & 4) == 0) chain_role(p, L, rs, b); else worker_role(p, L, rs, b);
}

}

extern "C" void kernel_launch(void* const* d_in, const int* in_sizes, int n_in, void* d_out, int out_size, void* d_ws, size_t ws_size,
                              hipStream_t stream) {
  (void)in_sizes; (void)n_in; (void)out_size; (void)ws_size;
  Params p;
  p.x = (const u16*)d_in[0]; p.mem0 = (const u16*)d_in[1]; p.Wq = (const u16*)d_in[2]; p.Wk = (const u16*)d_in[3];
  p.Wv = (const u16*)d_in[4]; p.Wo = (const u16*)d_in[5]; p.wr = (const u16*)d_in[8]; p.wg = (const u16*)d_in[9];
  p.wu = (const u16*)d_in[10]; p.wd = (const u16*)d_in[11]; p.gw = (const u16*)d_in[12];
  p.out = (u16*)d_out; p.ws = (u8*)d_ws;
  (void)hipFuncSetAttribute((const void*)rmt_mod_kernel, hipFuncAttributeMaxDynamicSharedMemorySize, LDS_BYTES);
  rmt_init_kernel<<<4 + NCHAIN, 512, 0, stream>>>(p.ws, p.mem0);
  rmt_mod_kernel<<<NWG, NTHR, LDS_BYTES, stream>>>(p);
}
```

```cpp
#include <hip/hip_runtime.h>
#include <hip/hip_bf16.h>
#include <stdint.h>

#define GAS __attribute__((address_space(1)))
#define LAS __attribute__((address_space(3)))
typedef unsigned v4u __attribute__((ext_vector_type(4)));
typedef unsigned v2u __attribute__((ext_vector_type(2)));
typedef unsigned v8u __attribute__((ext_vector_type(8)));
typedef float f32x4 __attribute__((ext_vector_type(4)));
typedef __bf16 bfx8 __attribute__((ext_vector_type(8)));
typedef __bf16 bfx16 __attribute__((ext_vector_type(16)));
typedef short s16x4 __attribute__((ext_vector_type(4)));
typedef GAS unsigned gu32;
typedef unsigned short u16;
typedef unsigned char u8;
#define RLX_AGENT __ATOMIC_RELAXED, __HIP_MEMORY_SCOPE_AGENT

#define PBF 1
#define SPLIT_XCD 2
namespace {

constexpr int SEQ = 2048, DM = 1024, NH = 32, HD = 4096, NSEG = 64, FF = 3584;
constexpr int NCHAIN = 128, NWORK = 128, NWG = 256, NTHR = 512;
constexpr unsigned SENT = 0xFFFFFFFFu;
constexpr int AUX_SC1 = 16;

constexpr unsigned CTRL_BYTES = 77824;
constexpr int LN = 32;
constexpr int W_TMO = 0, W_TOP = 3 * LN  , W_CFG = 4 * LN  ,
              W_XCC = 5 * LN, W_PROG = 8 * LN  , W_FKV = 64 * LN  , W_FQ = 128 * LN  ,
              W_RG = 160 * LN  , W_M2 = 288 * LN  , W_T2 = 344 * LN  ,
              W_SA = 408 * LN  , W_SP = 472 * LN  , W_M1 = 536 * LN  ;
constexpr unsigned OFF_MEM = 98304;
constexpr unsigned MEM_ST = 32768;
constexpr unsigned OFF_QKV = OFF_MEM + NSEG * MEM_ST;
constexpr unsigned QH = 32, PR = 128;
constexpr unsigned OFF_PART = OFF_QKV + NSEG * QH * 16384;
constexpr unsigned PART_SZ = PR * 16384;
#define NSLOT 3
constexpr unsigned OFF_ARM_END = OFF_PART + NSLOT * PART_SZ;
constexpr unsigned OFF_CAL = OFF_ARM_END;
constexpr unsigned OFF_KX = OFF_CAL + 16384 + 16384;
#define WFAR 1
constexpr unsigned FB = WFAR ? 16384 : 8192;
constexpr unsigned OFF_VX = OFF_KX + NSEG * NH * FB;
constexpr unsigned OFF_QX = OFF_VX + NSEG * NH * FB;
constexpr unsigned OFF_AX = OFF_QX + NSEG * NH * FB;
constexpr unsigned OFF_R = OFF_AX + NSEG * NH * FB + 1024 * FB + 16384;
constexpr unsigned OFF_G = OFF_R + SEQ * 4;
constexpr unsigned OFF_HID = OFF_G + SEQ * 4;
constexpr unsigned OFF_YP = OFF_HID + 2 * FF * 4;
constexpr unsigned OFF_PS = OFF_AX + NSEG * NH * FB;
constexpr unsigned WS_END = OFF_YP + 14 * 2 * DM * 4;
constexpr int NBULK = 8;
constexpr int LDS_BYTES = 160 * 1024;
constexpr unsigned SPIN_CAP = 1u << 18;

#define TAGP(...) (__VA_ARGS__)
#define BG_DELAY 16
#define ARM_LATE 0
#define TLB_WARM 0
#define NQK 4
#define FOK_AT 4
#define ROT_A 0
#define ROT_B 0
#define ROT_O 0
#define W_A 300
#define W_B 120
#define W_C 240

struct Params {
  const u16 *x, *mem0, *Wq, *Wk, *Wv, *Wo, *wr, *wg, *wu, *wd, *gw;
  u16* out;
  u8* ws;
};

__device__ __forceinline__ float u2f(unsigned u) { return __builtin_bit_cast(float, u); }
__device__ __forceinline__ unsigned f2u(float f) { return __builtin_bit_cast(unsigned, f); }
__device__ __forceinline__ float bf2f(unsigned b) { return u2f(b << 16); }
__device__ __forceinline__ float bflo(unsigned d) { return u2f(d << 16); }
__device__ __forceinline__ float bfhi(unsigned d) { return u2f(d & 0xFFFF0000u); }
__device__ __forceinline__ unsigned pk2(float lo, float hi) {
  typedef __bf16 bfx2 __attribute__((ext_vector_type(2)));
  bfx2 v; v.x = (__bf16)lo; v.y = (__bf16)hi;
  return __builtin_bit_cast(unsigned, v);
}
__device__ __forceinline__ float bfr(float f) { return (float)(__bf16)f; }
__device__ __forceinline__ unsigned canon(unsigned d) { return min(d, 0xFFFFFFFEu); }
__device__ __forceinline__ f32x4 mfma32(v4u a, v4u b, f32x4 c) {
  return __builtin_amdgcn_mfma_f32_16x16x32_bf16(__builtin_bit_cast(bfx8, a), __builtin_bit_cast(bfx8, b), c, 0, 0, 0);
}
__device__ __forceinline__ f32x4 mfma16(v2u a, v2u b, f32x4 c) { return mfma32(v4u{a.x, a.y, 0u, 0u}, v4u{b.x, b.y, 0u, 0u}, c); }
__device__ __forceinline__ f32x4 smf64(v4u a, v8u b, f32x4 c, int idx) {
  return __builtin_amdgcn_smfmac_f32_16x16x64_bf16(__builtin_bit_cast(bfx8, a), __builtin_bit_cast(bfx16, b), c, idx, 0, 0);
}
__device__ __forceinline__ v2u trread(unsigned lds_addr) {
  return __builtin_bit_cast(v2u, __builtin_amdgcn_ds_read_tr16_b64_v4i16((LAS s16x4*)(size_t)lds_addr));
}
typedef float f32x2 __attribute__((ext_vector_type(2)));
__device__ __forceinline__ f32x2 fold32(f32x4 v) {
  const auto r0 = __builtin_amdgcn_permlane32_swap(f2u(v[0]), f2u(v[2]), false, false);
  const auto r1 = __builtin_amdgcn_permlane32_swap(f2u(v[1]), f2u(v[3]), false, false);
  return f32x2{u2f(r0[0]), u2f(r1[0])} + f32x2{u2f(r0[1]), u2f(r1[1])};
}
__device__ __forceinline__ float xmax16(float a) { auto r = __builtin_amdgcn_permlane16_swap(f2u(a), f2u(a), false, false); return __builtin_amdgcn_fmed3f(u2f(r[0]), u2f(r[1]), INFINITY); }
__device__ __forceinline__ float xmax32(float a) { auto r = __builtin_amdgcn_permlane32_swap(f2u(a), f2u(a), false, false); return __builtin_amdgcn_fmed3f(u2f(r[0]), u2f(r[1]), INFINITY); }
__device__ __forceinline__ float xadd16(float a) { auto r = __builtin_amdgcn_permlane16_swap(f2u(a), f2u(a), false, false); return u2f(r[0]) + u2f(r[1]); }
__device__ __forceinline__ float xadd32(float a) { auto r = __builtin_amdgcn_permlane32_swap(f2u(a), f2u(a), false, false); return u2f(r[0]) + u2f(r[1]); }

__device__ __forceinline__ float wave_sum(float v) {
  v += __shfl_xor(v, 1); v += __shfl_xor(v, 2); v += __shfl_xor(v, 4); v += __shfl_xor(v, 8);
  return xadd32(xadd16(v));
}
__device__ __forceinline__ int fresh_tid() { int t = threadIdx.x; asm volatile("" : "+v"(t)); return t; }
#define CFENCE() asm volatile("" ::: "memory")
#define DRAIN_VM() asm volatile("s_waitcnt vmcnt(0)" ::: "memory")
#define WAIT_LGKM() asm volatile("s_waitcnt lgkmcnt(0)" ::: "memory")
__device__ __forceinline__ void wgbar() { WAIT_LGKM(); __builtin_amdgcn_s_barrier(); CFENCE(); }

typedef __amdgpu_buffer_rsrc_t rsrc_t;
__device__ __forceinline__ v4u ld16(rsrc_t rs, unsigned off) { return __builtin_amdgcn_raw_buffer_load_b128(rs, off, 0, AUX_SC1); }
__device__ __forceinline__ v2u ld8(rsrc_t rs, unsigned off) { return __builtin_amdgcn_raw_buffer_load_b64(rs, off, 0, AUX_SC1); }
__device__ __forceinline__ unsigned ld4(rsrc_t rs, unsigned off) { return __builtin_amdgcn_raw_buffer_load_b32(rs, off, 0, AUX_SC1); }
__device__ __forceinline__ void st16(rsrc_t rs, unsigned off, v4u v) { __builtin_amdgcn_raw_buffer_store_b128(v, rs, off, 0, AUX_SC1); }
__device__ __forceinline__ void st8(rsrc_t rs, unsigned off, v2u v) { __builtin_amdgcn_raw_buffer_store_b64(v, rs, off, 0, AUX_SC1); }
__device__ __forceinline__ void st4(rsrc_t rs, unsigned off, unsigned v) { __builtin_amdgcn_raw_buffer_store_b32(v, rs, off, 0, AUX_SC1); }
__device__ __forceinline__ unsigned umax4(v4u v) { return max(max(v.x, v.y), max(v.z, v.w)); }

template <int N> __device__ __forceinline__ void delay_cyc() {
#pragma unroll
  for (int i = 0; i < N / 64; ++i) asm volatile("s_nop 15");
  if ((N % 64) >= 4) asm volatile("s_nop %0" :: "i"((N % 64) / 4 - 1));
}

#define PACE 0
#define PACE_GAIN 0.3f
#define PACE_CLAMP 2.0f
#define GAP_A 6
#define DB3 5
#define EXTRA_B 0
#define EXTRA_C 0
#define GAP_C 6
#define AD_DEC 0
#define AD_INC 0
__device__ __forceinline__ void delay_rt(int n) {
  asm volatile("s_cmp_lt_i32 %0, 1\n\ts_cbranch_scc1 2f\n\ts_mov_b32 vcc_lo, %0\n1:\n\ts_nop 1\n\ts_sub_i32 vcc_lo, vcc_lo, 1\n\ts_cmp_gt_i32 vcc_lo, 0\n\ts_cbranch_scc1 1b\n2:" :: "s"(n) : "vcc", "scc");
}
__device__ __forceinline__ void adapt(int& n, int misses) { n = misses ? min(n + AD_INC, 120) : max(n - AD_DEC, 0); }

__device__ __forceinline__ bool spin_fail(unsigned& spins, gu32* ctrl, unsigned code) {
  ++spins;
  if (spins > SPIN_CAP) { __hip_atomic_store(ctrl + W_TMO, 1u, RLX_AGENT); __hip_atomic_fetch_max(ctrl + W_TMO + 1, code, RLX_AGENT); return true; }
  return (spins & 255u) == 0u && __builtin_amdgcn_readfirstlane(__hip_atomic_load(ctrl + W_TMO, RLX_AGENT)) != 0u;
}
constexpr int RETRY_CAP = 1 << 15;
__device__ __forceinline__ void wait_ge(gu32* ctrl, int word, unsigned want, unsigned code, bool& dead) {
  if (dead) return;
  for (unsigned sp = 0;;) {
    if ((unsigned)__builtin_amdgcn_readfirstlane(__hip_atomic_load(ctrl + word, RLX_AGENT)) >= want) return;
    __builtin_amdgcn_s_sleep(4);
    if (spin_fail(sp, ctrl, code)) { dead = true; return; }
  }
}
__device__ __forceinline__ void wait_flags2(gu32* ctrl, int w0, int w1, unsigned code, bool& dead) {
  if (dead) return;
  for (unsigned sp = 0;;) {
    const unsigned long long a = __hip_atomic_load((GAS unsigned long long*)(ctrl + w0), RLX_AGENT);
    const unsigned long long b = __hip_atomic_load((GAS unsigned long long*)(ctrl + w1), RLX_AGENT);
    const unsigned long long m = a & b;
    if (__builtin_amdgcn_readfirstlane((unsigned)m & (unsigned)(m >> 32)) != 0u) return;
    __builtin_amdgcn_s_sleep(4);
    if (spin_fail(sp, ctrl, code)) { dead = true; return; }
  }
}
__device__ __forceinline__ void wait_lines(gu32* ctrl, int word, int stride, int n, unsigned want, int lane, unsigned code, bool& dead) {
  if (dead) return;
  gu32* mine = ctrl + word + (lane < n ? lane : 0) * stride;
  for (unsigned sp = 0;;) {
    if (__all(__hip_atomic_load(mine, RLX_AGENT) >= want)) return;
    __builtin_amdgcn_s_sleep(2);
    if (spin_fail(sp, ctrl, code)) { dead = true; return; }
  }
}
constexpr int ARM_AHEAD = 4;
__device__ __forceinline__ void arm_share(rsrc_t rs, unsigned al, int c, int tid) {
  const v4u s = {SENT, SENT, SENT, SENT};
  const unsigned lo = (tid & 63) * 16, w = tid >> 6;
  constexpr int NB = PR / 64;
  for (int sl = 0; sl < NSLOT; ++sl)
#pragma unroll
    for (int k = 0; k < NB; ++k) if (!PBF || w < 4) st16(rs, al + OFF_PART + sl * PART_SZ + (c * NB + k) * 8192 + w * 1024 + lo, s);
  if (w < 6) {
#pragma unroll
    for (int k = 0; k < NB; ++k) st16(rs, al + OFF_QKV + (c * NB + k) * 8192 + w * 1024 + lo, s);
  } else if (w == 6) st16(rs, al + OFF_MEM + MEM_ST + c * 1024 + lo, s);
  else if (c < 32) st16(rs, al + OFF_MEM + 5 * MEM_ST + c * 1024 + lo, s);
}
struct Near {
  unsigned al, va21, nz, k;
  __device__ __forceinline__ unsigned rel(int c) const { return k >> 2; }
  __device__ __forceinline__ unsigned frag(unsigned reg, int s, int h) const { return WFAR ? blk(reg + (unsigned)(s * NH + h) * FB, (k >> 2) ^ 1u) : reg + (unsigned)(s * NH + h) * FB; }
  __device__ __forceinline__ unsigned blk(unsigned pair_off, unsigned iod) const { return al + pair_off + ((((va21 + pair_off) >> 21) ^ nz ^ iod) & 1u) * 8192u; }
};
__device__ __forceinline__ Near near_init(const u8* ws) {
  Near n; const unsigned lo = (unsigned)(size_t)ws;
  n.al = (0u - lo) & 16383u; n.va21 = lo + n.al; n.nz = 0; n.k = 0; return n;
}
__device__ __forceinline__ unsigned xcc_id() { return (unsigned)__builtin_amdgcn_s_getreg(20 | (3 << 11)) & 7u; }
__device__ __forceinline__ void near_wait(gu32* ctrl, Near& n, bool& dead) {
  wait_ge(ctrl, W_XCC, 16u, 0x11u, dead);
  n.k = __builtin_amdgcn_readfirstlane(__hip_atomic_load(ctrl + W_XCC, RLX_AGENT)) & 7u;
  n.nz = __builtin_amdgcn_readfirstlane(__hip_atomic_load(ctrl + W_CFG, RLX_AGENT)) & 1u;
}

#define CSTAMP(wv, slot)
#define PT(id)
#define CTRIES(slot, n)
#define WSTAMP()
template <bool SC1>
__device__ __forceinline__ void dma16(const void* base, unsigned voff, unsigned lds_addr) {
  unsigned keep;
  if (SC1) asm volatile("s_mov_b32 %0, m0\n\ts_mov_b32 m0, %3\n\ts_nop 0\n\tglobal_load_lds_dwordx4 %1, %2 sc1\n\ts_mov_b32 m0, %0" : "=&s"(keep) : "v"(voff), "s"(base), "s"(lds_addr) : "memory");
  else asm volatile("s_mov_b32 %0, m0\n\ts_mov_b32 m0, %3\n\ts_nop 0\n\tglobal_load_lds_dwordx4 %1, %2\n\ts_mov_b32 m0, %0" : "=&s"(keep) : "v"(voff), "s"(base), "s"(lds_addr) : "memory");
}

template <int N> __device__ __forceinline__ void wait_vm() { static_assert(N >= 0 && N < 64, "vmcnt"); asm volatile("s_waitcnt vmcnt(%0)" :: "i"(N) : "memory"); }
template <int V> struct IC { static constexpr int v = V; };
template <int N, int I = 0, class F> __device__ __forceinline__ void static_for(F&& f) { if constexpr (I < N) { f(IC<I>{}); static_for<N, I + 1>(f); } }
__device__ __forceinline__ void slice_dma(const u16* src, int ld, unsigned dst, int lane) {
  const unsigned vo = ((lane >> 2) * ld + 8 * (lane & 3)) * 2;
#pragma unroll
  for (int p = 0; p < 8; ++p) dma16<false>(src + (size_t)16 * p * ld, vo, dst + p * 1024);
}
template <bool PERM>
__device__ __forceinline__ void slice_frags(unsigned base, int lane, v8u (&f)[2][2]) {
  const int gB = lane >> 4, q = (lane & 15) >> 2, p = lane & 3;
#pragma unroll
  for (int cc = 0; cc < 2; ++cc)
#pragma unroll
    for (int rr = 0; rr < 4; ++rr) {
      const int kp = 64 * cc + 32 * (rr >> 1) + 16 * (gB >> 1) + 4 * (gB & 1) + 2 * (rr & 1) + (q & 1) + 8 * (q >> 1);
      const int row = PERM ? ((kp & ~31) + 16 * (kp & 1) + ((kp & 31) >> 1)) : kp;
#pragma unroll
      for (int tb = 0; tb < 2; ++tb) {
        const v2u v = trread(base + row * 64 + tb * 32 + p * 8);
        f[tb][cc][2 * rr] = v.x; f[tb][cc][2 * rr + 1] = v.y;
      }
    }
}

__device__ __forceinline__ void chain_role(const Params& p, LAS u8* L, rsrc_t rs, int b) {
  const int tid = threadIdx.x, lane = tid & 63, w = __builtin_amdgcn_readfirstlane(tid >> 6);
  const int c = (b & 3) * 32 + (b >> 3);
  const int h = c >> 2, j = c & 3;
  const int dbgi = (c >> 4) + 8 * (c & 15); (void)dbgi;
  const int r16 = lane & 15, g = lane >> 4;
  gu32* ctrl = (gu32*)p.ws;
  bool dead = false;
  const unsigned lbase = (unsigned)(size_t)L;
  Near nr = near_init(p.ws);
  if (b == 0 && tid == 0) __hip_atomic_store(ctrl + W_XCC, 16u | xcc_id(), RLX_AGENT);

  v8u W1[3][2][2];
  v4u am0, am1;
  v8u WO[2][2];
  {
    const unsigned mine = lbase + w * 16384;
    const size_t wcol = (size_t)h * 128 + 32 * j;
    slice_dma(p.Wk + (size_t)128 * w * HD + wcol, HD, mine, lane);
    slice_dma(p.Wv + (size_t)128 * w * HD + wcol, HD, mine + 8192, lane);
    near_wait(ctrl, nr, dead);
    const unsigned m0 = nr.blk(OFF_MEM + (w >> 2) * 16384u, nr.rel(c)) + (w & 3) * 2048 + lane * 16;
    am0 = ld16(rs, m0); am1 = ld16(rs, m0 + 1024);
    slice_frags<true>(mine, lane, W1[1]);
    slice_frags<true>(mine + 8192, lane, W1[2]);
    WAIT_LGKM();
    asm volatile("s_waitcnt vmcnt(0)" : "+v"(am0), "+v"(am1) :: "memory");
    __builtin_amdgcn_sched_barrier(0);
    slice_dma(p.Wq + (size_t)128 * w * HD + wcol, HD, mine, lane);
    slice_dma(p.Wo + (size_t)128 * h * DM + 256 * j + 32 * w, DM, mine + 8192, lane);
    asm volatile("s_waitcnt vmcnt(8)" ::: "memory");
    slice_frags<true>(mine, lane, W1[0]);
    WAIT_LGKM(); __builtin_amdgcn_sched_barrier(0);
  }
  const unsigned myrel = nr.rel(c);

  LAS float* RED = (LAS float*)(L + 131072);
  LAS u8* AHF = L + 155648;
  volatile LAS unsigned* FOK = (volatile LAS unsigned*)(L + 162816);
  LAS float* INV = (LAS float*)(L + 162048);
  LAS u8* KVL = L;
  const int sidx = r16 < 8 ? 0x4444 : 0xEEEE;
  const int gg = g & 1, hi = g >> 1, rp = 2 * gg + hi;
  int gone = dead ? 1 : 0;

  auto fetch_kv_issue = [&](int s, int lo, int hi) {
    if ((s & 3) == 0) { bool d = gone != 0; const int blk = s >> 2; wait_flags2(ctrl, W_FKV + blk * 128 + 2 * h, W_FKV + blk * 128 + 64 + 2 * h, 0x20u, d); gone = __builtin_amdgcn_readfirstlane(d ? 1 : 0); }
    CFENCE();
    const unsigned dst = lbase + (s & 1) * 32768;
#pragma unroll
    for (int i = lo; i < hi; ++i) dma16<true>(p.ws + nr.frag((i >> 3) ? OFF_VX : OFF_KX, s, h) + (i & 7) * 1024, lane * 16, dst + (i >> 3) * 16384 + (i & 7) * 1024);
  };
  auto kv_ready = [&](int s) {
    if ((s & 3) == 0) { bool d = gone != 0; const int blk = s >> 2; wait_flags2(ctrl, W_FKV + blk * 128 + 2 * h, W_FKV + blk * 128 + 64 + 2 * h, 0x20u, d); gone = __builtin_amdgcn_readfirstlane(d ? 1 : 0); CFENCE(); }
  };
  auto arm_addr = [&](int s, int slot, int q, unsigned& a0, unsigned& a1) {
    a0 = s > 0 ? nr.blk(OFF_PART + (unsigned)(slot == 0 ? 2 : slot - 1) * PART_SZ + (c & (PR - 1)) * 16384u, myrel) + q * 1024 + lane * 16 : 0xFFFFFF00u;
    const unsigned qn = nr.blk(OFF_QKV + (unsigned)((s + ARM_AHEAD) * QH + (h & (QH - 1))) * 16384u, myrel);
    a1 = q == 0 ? qn + j * 1024 + lane * 16
                : lane < 32 ? qn + 4096 + j * 512 + lane * 16
                            : (lane < 48 && s + ARM_AHEAD + 2 < NSEG) ? nr.al + OFF_MEM + (unsigned)(s + ARM_AHEAD + 2) * MEM_ST + (c >> 6) * 16384u + ((lane >> 3) & 1) * 8192u + (c & 63) * 128 + (lane & 7) * 16
                                                                      : 0xFFFFFF00u;
    a1 = s + ARM_AHEAD < NSEG ? a1 : 0xFFFFFF00u;
  };
  if (tid == 0) { FOK[0] = 0u; FOK[1] = 0u; }
  int dA = W_A / 20, dB = W_B / 20, dC = W_C / 20;
  wgbar();

  auto run = [&](auto role) {
    constexpr int ROLE = decltype(role)::v;
    LAS f32x2* redw = (LAS f32x2*)(RED + w * 768) + (rp * 16 + r16) * 2;
    const int R = 32 * j + 4 * w + (r16 >> 2);
    const unsigned rrel = nr.rel(R), part_w = (R & (PR - 1)) * 16384u, part_l = PBF ? h * 128 + rp * 32 + (r16 & 3) * 8 : h * 256 + rp * 64 + (r16 & 3) * 16;
    const unsigned pub_l = ROLE == 0 ? j * 1024 + (8 * w + 2 * g + (r16 & 1) + 16 * (r16 >> 2)) * 16 + (r16 & 2) * 4 : 4096 + (32 * j + r16) * 16 + g * 4;
    unsigned qo, a_part, mo, a_pub;
    float pinf = INFINITY, mi = g < 2 ? -INFINITY : 0.f;
    asm volatile("" : "+v"(pinf), "+v"(mi));
    const f32x4 minit = {mi, mi, mi, mi};
    const unsigned s1 = (r16 & 3) == 0 ? 0x00003F80u : (r16 & 3) == 1 ? 0x3F800000u : 0u;
    const v4u rsel = {(r16 & 4) ? 0u : s1, (r16 & 4) ? s1 : 0u, (r16 & 4) ? 0u : s1, (r16 & 4) ? s1 : 0u};
    int slot = 0, lim = gone ? 0 : RETRY_CAP;
    auto prepare = [&](int s, int sl) {
      qo = nr.blk(OFF_QKV + (unsigned)(s * QH + (h & (QH - 1))) * 16384u, myrel);
      a_part = nr.blk(OFF_PART + (unsigned)sl * PART_SZ + part_w, rrel) + part_l;
      mo = nr.blk(OFF_MEM + (unsigned)s * MEM_ST + (w >> 2) * 16384u, myrel) + (w & 3) * 2048 + lane * 16;
      a_pub = qo + pub_l;
      asm volatile("" : "+v"(a_part), "+v"(mo), "+v"(a_pub));
    };
    prepare(0, 0);
    auto step = [&](auto first, int s) -> bool {
      constexpr bool FIRST = decltype(first)::v != 0;
      const unsigned par = (unsigned)(s / 3) & 1u; (void)par;
      PT(0);
      if (!FIRST) delay_rt(dA);
      PT(1);
      f32x4 acc[6];
      {
        int tries = 0; bool again;
        do {
          CFENCE();
          const v4u a0 = FIRST ? am0 : ld16(rs, mo), a1 = FIRST ? am1 : ld16(rs, mo + 1024);
#pragma unroll
          for (int t = 0; t < 6; ++t) { acc[t] = f32x4{0.f, 0.f, 0.f, 0.f}; asm volatile("" : "+v"(acc[t])); }
          __builtin_amdgcn_sched_barrier(0);
#pragma unroll
          for (int t = 0; t < 6; ++t) acc[t] = smf64(a0, W1[t >> 1][t & 1][0], acc[t], sidx);
          __builtin_amdgcn_sched_barrier(0);
#pragma unroll
          for (int t = 0; t < 6; ++t) acc[t] = smf64(a1, W1[t >> 1][t & 1][1], acc[t], sidx);
          again = !FIRST && ((__ballot(max(umax4(a0), umax4(a1)) == SENT) != 0ull) & (tries < lim));
          ++tries;
        } while (__builtin_expect(again, 0));
        lim = tries > lim ? 0 : lim;
        if (w == 0) CTRIES(0, tries - 1);
      }
      CSTAMP(0, 0);
      PT(2);
#pragma unroll
      for (int t = 0; t < 6; ++t) redw[(t >> 1) * 128 + (t & 1)] = fold32(acc[t]);
      PT(3);
      wgbar();
      PT(4);
      CSTAMP(0, 1);
      if (ROLE <= 2) {
        const LAS f32x4* redr = (const LAS f32x4*)RED + w * 64 + lane;
        f32x4 t0 = redr[0], t1 = redr[192];
#pragma unroll
        for (int k = 2; k < 8; k += 2) { t0 += redr[k * 192]; t1 += redr[(k + 1) * 192]; }
        t0 += t1;
        if (ROLE == 0) {
          const unsigned d0 = pk2(t0[0], t0[2]), d1 = pk2(t0[1], t0[3]);
          const unsigned got = __builtin_amdgcn_mov_dpp((r16 & 1) ? d0 : d1, 0xB1, 0xF, 0xF, true);
          st8(rs, a_pub, (r16 & 1) ? v2u{canon(got), canon(d1)} : v2u{canon(d0), canon(got)});
        } else {
          st4(rs, a_pub, canon(pk2(t0[0], t0[1])));
          st4(rs, a_pub + 256, canon(pk2(t0[2], t0[3])));
        }
      }
      CSTAMP(0, 2);
      PT(5);
      if (!FIRST && s == NSEG - 1) return false;
      if (FIRST) {
        if (ROLE >= 4) fetch_kv_issue(0, 4 * (ROLE - 4), 4 * (ROLE - 4) + 4);
        DRAIN_VM();
        slice_frags<false>(lbase + w * 16384 + 8192, lane, WO);
        wgbar();
      }
      acc[0] = f32x4{0.f, 0.f, 0.f, 0.f}; acc[1] = acc[0];
      asm volatile("" : "+v"(acc[0]), "+v"(acc[1]));
      if (ROLE < NQK) {
        constexpr int NE = 8 / NQK;
        v4u KX[2][4], VX[NE], F[4]; v2u VM[NE];
        const LAS u8* kv = KVL + (s & 1) * 32768 + lane * 16;
#pragma unroll
        for (int i = 0; i < 8; ++i) KX[i >> 2][i & 3] = *(const LAS v4u*)(kv + i * 1024);
#pragma unroll
        for (int e = 0; e < NE; ++e) VX[e] = *(const LAS v4u*)(kv + 16384 + (NE * w + e) * 1024);
        const unsigned fo = qo + lane * 16, vo = qo + 4096 + (16 * NE * w + r16) * 16 + gg * 8;
        __builtin_amdgcn_sched_barrier(0);
        delay_rt((ROLE == 3 ? dB + DB3 : dB));
        PT(6);
        f32x4 sA, sB, sM;
        {
          int tries = 0; bool again;
          do {
            CFENCE();
#pragma unroll
            for (int u = 0; u < 4; ++u) F[u] = ld16(rs, fo + u * 1024);
#pragma unroll
            for (int e = 0; e < NE; ++e) VM[e] = ld8(rs, vo + e * 256);
            sA = f32x4{0.f, 0.f, 0.f, 0.f}; sB = sA; sM = minit;
#pragma unroll
            for (int u = 0; u < 4; ++u) {
              sM = mfma32(F[u], F[u], sM); sA = mfma32(KX[0][u], F[u], sA); sB = mfma32(KX[1][u], F[u], sB);
              if (u == FOK_AT) { __builtin_amdgcn_sched_barrier(0); FOK[0] = (unsigned)(s + 1); __builtin_amdgcn_sched_barrier(0); }
            }
            unsigned m = max(max(umax4(F[0]), umax4(F[1])), max(umax4(F[2]), umax4(F[3])));
#pragma unroll
            for (int e = 0; e < NE; ++e) m = max(m, max(VM[e].x, VM[e].y));
            again = (__ballot(m == SENT) != 0ull) & (tries < lim);
            ++tries;
          } while (__builtin_expect(again, 0));
          lim = tries > lim ? 0 : lim;
          if (w == 0) CTRIES(1, tries - 1);
        }
        if (FOK_AT > 3) FOK[0] = (unsigned)(s + 1);
        CSTAMP(0, 3);
        PT(7);
        const float sc2 = 0.08838834764831845f * 1.4426950408889634f;
        sA *= sc2; sB *= sc2; sM *= sc2;
        float mx = fmaxf(fmaxf(fmaxf(fmaxf(sA[0], sA[1]), sA[2]), fmaxf(fmaxf(sA[3], sB[0]), sB[1])), fmaxf(fmaxf(fmaxf(sB[2], sB[3]), sM[0]), fmaxf(fmaxf(sM[1], sM[2]), sM[3])));
        { auto r = __builtin_amdgcn_permlane16_swap(f2u(mx), f2u(mx), false, false); mx = __builtin_amdgcn_fmed3f(u2f(r[0]), u2f(r[1]), pinf); }
        { auto r = __builtin_amdgcn_permlane32_swap(f2u(mx), f2u(mx), false, false); mx = __builtin_amdgcn_fmed3f(u2f(r[0]), u2f(r[1]), pinf); }
        sA -= mx; sB -= mx; sM -= mx;
#pragma unroll
        for (int r = 0; r < 4; ++r) { sA[r] = __builtin_amdgcn_exp2f(sA[r]); sB[r] = __builtin_amdgcn_exp2f(sB[r]); sM[r] = __builtin_amdgcn_exp2f(sM[r]); }
        const v4u pb = {pk2(sA[0], sA[1]), pk2(sA[2], sA[3]), pk2(sB[0], sB[1]), pk2(sB[2], sB[3])};
        const v2u pm = {pk2(sM[0], sM[1]), pk2(sM[2], sM[3])};
        f32x4 eo[NE];
#pragma unroll
        for (int e = 0; e < NE; ++e) eo[e] = mfma32(VX[e], pb, f32x4{0.f, 0.f, 0.f, 0.f});
#pragma unroll
        for (int e = 0; e < NE; ++e) eo[e] = mfma16(VM[e], pm, eo[e]);
        if (ROLE == 3 || NQK == 2) {
          const f32x4 ss = sA + sB + sM;
          INV[lane] = __builtin_amdgcn_rcpf(xadd32(xadd16((ss[0] + ss[1]) + (ss[2] + ss[3]))));
        }
        LAS u8* ah = AHF + (r16 < 8 ? (2 * NE * w + hi) * 128 + r16 * 16 + gg * 8 : 4096 + lane * 8);
#pragma unroll
        for (int e = 0; e < NE; ++e) *(LAS v2u*)(ah + e * 256) = v2u{pk2(eo[e][0], eo[e][1]), pk2(eo[e][2], eo[e][3])};
      } else if (ROLE >= 4) {
        constexpr int Q = ROLE - 4;
        kv_ready(s + 1);
        const u8* kb = p.ws + nr.frag((Q >> 1) ? OFF_VX : OFF_KX, s + 1, h) + (Q & 1) * 4096;
        const unsigned dst = lbase + ((s + 1) & 1) * 32768 + (Q >> 1) * 16384 + (Q & 1) * 4096;
        unsigned ar0, ar1;
        arm_addr(s, slot, Q, ar0, ar1);
        asm volatile("" : "+v"(ar0), "+v"(ar1));
        __builtin_amdgcn_sched_barrier(0);
        for (unsigned sp = 0; sp < (1u << 20); ++sp)
          if ((unsigned)__builtin_amdgcn_readfirstlane(FOK[0]) == (unsigned)(s + 1)) break;
#pragma unroll
        for (int i = 0; i < 4; ++i) dma16<true>(kb + i * 1024, lane * 16, dst + i * 1024);
        st16(rs, ar0, v4u{SENT, SENT, SENT, SENT});
        if (Q < 2) st16(rs, ar1, v4u{SENT, SENT, SENT, SENT});
      }
      PT(9);
      wgbar();
      PT(10);
      CSTAMP(7, 4);
      {
        const v4u b0 = *(LAS v4u*)(AHF + lane * 16), b1 = *(LAS v4u*)(AHF + 1024 + lane * 16);
        const f32x2 iv = *(LAS f32x2*)(INV + 2 * rp);
        acc[0] = smf64(b0, WO[0][0], acc[0], sidx); acc[1] = smf64(b0, WO[1][0], acc[1], sidx);
        acc[0] = smf64(b1, WO[0][1], acc[0], sidx); acc[1] = smf64(b1, WO[1][1], acc[1], sidx);
        const f32x2 f0 = fold32(acc[0]) * iv, f1 = fold32(acc[1]) * iv;
        const v4u pv = {canon(f2u(f0[0])), canon(f2u(f1[0])), canon(f2u(f0[1])), canon(f2u(f1[1]))};
        if (PBF) st8(rs, a_part, TAGP(v2u{canon(pk2(f0[0], f1[0])), canon(pk2(f0[1], f1[1]))})); else st16(rs, a_part, pv);
      }
      __builtin_amdgcn_sched_barrier(0);
      CSTAMP(7, 5);
      PT(11);
      const int nslot = slot == NSLOT - 1 ? 0 : slot + 1;
      if (ROLE == 7) {
        unsigned po = nr.blk(OFF_PART + (unsigned)slot * PART_SZ + (c & (PR - 1)) * 16384u, myrel) + lane * 16;
        unsigned a_mem = nr.blk(OFF_MEM + (unsigned)(s + 1) * MEM_ST + (c >> 6) * 16384u, myrel) + (c & 63) * 128 + (2 * (r16 >> 2) + g) * 16 + (r16 & 3) * 4;
        a_mem = lane < 32 ? a_mem : 0xFFFFFF00u;
        prepare(s + 1, nslot);
        asm volatile("" : "+v"(a_mem), "+v"(po));
        delay_rt(dC);
        PT(12);
        f32x4 t;
        {
          int tries = 0; bool again;
          const unsigned p8 = po - lane * 8;
          do {
            CFENCE();
            v2u pq[8];
#pragma unroll
            for (int i = 0; i < 8; ++i) pq[i] = ld8(rs, p8 + i * 512);
            t = f32x4{0.f, 0.f, 0.f, 0.f};
#pragma unroll
            for (int i = 0; i < 8; i += 2) t = mfma32(rsel, v4u{pq[i].x, pq[i].y, pq[i + 1].x, pq[i + 1].y}, t);
            unsigned m = 0;
#pragma unroll
            for (int i = 0; i < 8; ++i) m = max(m, max(pq[i].x, pq[i].y));
            again = (__ballot(m == SENT) != 0ull) & (tries < lim);
            ++tries;
          } while (__builtin_expect(again, 0));
          lim = tries > lim ? 0 : lim;
          CTRIES(2, tries - 1);
        }
        CSTAMP(7, 6);
        PT(13);
        st4(rs, a_mem, canon(pk2(t[0], t[1])));
      } else {
        prepare(s + 1, nslot);
        if (ROLE == 2 && j == 0 && lane == 0) __hip_atomic_store(ctrl + W_PROG + LN * h, (unsigned)(s + 1), RLX_AGENT);
        if (ROLE >= 4 || ARM_LATE) DRAIN_VM();
      }
      slot = nslot;
      PT(14);
      wgbar();
      CSTAMP(7, 7);
      return true;
    };
    step(IC<1>{}, 0);
    for (int s = 1; step(IC<0>{}, s); ++s) {}
    if (lim == 0 && lane == 0) __hip_atomic_store(ctrl + W_TMO, 1u, RLX_AGENT);
  };
  switch (w) {
    case 0: case 1: run(IC<0>{}); break;
    case 2: run(IC<2>{}); break;
    case 3: run(IC<3>{}); break;
    case 4: run(IC<4>{}); break;
    case 5: run(IC<5>{}); break;
    case 6: run(IC<6>{}); break;
    default: run(IC<7>{}); break;
  }
}

enum { M_K = 0, M_V = 1, M_O = 2 };

template <int BN, int MODE, int WM = 2>
__device__ __forceinline__ void gemm_tile(LAS u8* L, const u16* A, int lda, const u16* W, int ldw, int nkt, f32x4 (&acc)[8 / WM][BN * WM / 128], int rot = 0, const Near* nr = nullptr) {
  constexpr int MT = 8 / WM, NT = BN * WM / 128, NCG = BN / 64, STG = 16384 + BN * 128, NB = BN / 64;
  constexpr int NSTG = LDS_BYTES / STG;
  const int tid = fresh_tid(), lane = tid & 63, w = __builtin_amdgcn_readfirstlane(tid >> 6);
  const int wm = w / (8 / WM), wn = w % (8 / WM), r16 = lane & 15, g = lane >> 4, q = r16 >> 2, pp = lane & 3;
  const int arow = 16 * w + (lane >> 3);
  const unsigned avo0 = (arow * lda + 8 * ((lane & 7) ^ ((arow >> 1) & 7))) * 2;
  const unsigned avo1 = ((arow + 8) * lda + 8 * ((lane & 7) ^ (((arow + 8) >> 1) & 7))) * 2;
  const int kr = 8 * w + (lane >> 3);
  const int krs = MODE == M_O ? ((kr & ~31) + 16 * ((kr >> 2) & 1) + 4 * ((kr >> 3) & 3) + (kr & 3)) : kr;
  const int fsw = ((kr >> 1) & 1) | (((kr >> 3) & 1) << 1);
  const unsigned wvo = (krs * ldw + 16 * (((lane & 7) >> 1) ^ fsw) + 8 * (lane & 1)) * 2;
  const unsigned lb = (unsigned)(size_t)L;
  auto piece = [&](int kn, int buf, int i) {
    const int kt = (kn + rot) & (nkt - 1);
    const unsigned sb = lb + buf * STG;
    if (i >= 2) dma16<false>(W + (size_t)kt * 64 * ldw + 64 * (i - 2), wvo, sb + 16384 + (w * NB + i - 2) * 1024);
    else if (MODE == M_O) {
      const u8* ab = (const u8*)A + (unsigned)__builtin_amdgcn_readfirstlane(nr->frag(OFF_AX, lda + (w >> 1), kt >> 1)) + (w & 1) * 4096 + (kt & 1) * 2048;
      dma16<false>(ab + i * 1024, lane * 16, sb + (2 * w + i) * 1024);
    } else dma16<false>(A + kt * 64, i ? avo1 : avo0, sb + (2 * w + i) * 1024);
  };
  const unsigned aoff = lb + (16 * MT * wm + r16) * 128 + ((g ^ ((r16 >> 1) & 7)) * 16);
  const int fr = ((q >> 1) & 1) | ((g & 1) << 1);
  const int cgw = (wn * NT) >> 2, c0 = (wn * NT) & 3;
  unsigned boff[NT];
#pragma unroll
  for (int n = 0; n < NT; ++n) boff[n] = lb + 16384 + (g * NCG + cgw) * 1024 + q * 128 + (((c0 + n) ^ fr) * 32) + pp * 8;
#pragma unroll
  for (int m = 0; m < MT; ++m)
#pragma unroll
    for (int n = 0; n < NT; ++n) acc[m][n] = f32x4{0.f, 0.f, 0.f, 0.f};

  v4u af[2][MT], bf[2][NT];
  auto frag1 = [&](int buf, int ks, int i) {
    const unsigned so = buf * STG;
    if (i < NT) {
      const v2u x0 = trread(boff[i] + so + ks * 4 * NCG * 1024), x1 = trread(boff[i] + so + ks * 4 * NCG * 1024 + 512);
      bf[ks][i] = v4u{x0.x, x0.y, x1.x, x1.y};
    } else {
      const int m = i - NT;
      af[ks][m] = MODE == M_O ? *(LAS v4u*)(size_t)(lb + so + wm * 8192 + lane * 16 + m * 2048 + ks * 1024) : *(LAS v4u*)(size_t)(((aoff + so) ^ (ks * 64)) + m * 2048);
    }
  };
  auto mma = [&](int ks, int fb, bool fill, int kt, int buf) {
    if (fb >= 0) {
#pragma unroll
      for (int i = 0; i < MT + NT; ++i) frag1(fb, ks ^ 1, i);
    }
    __builtin_amdgcn_sched_barrier(0);
#pragma unroll
    for (int i = 0; i < MT * NT; ++i) {
      const int m = i / NT, n = i % NT;
      acc[m][n] = MODE == M_V ? mfma32(af[ks][m], bf[ks][n], acc[m][n]) : mfma32(bf[ks][n], af[ks][m], acc[m][n]);
      constexpr int EV = MT * NT >= 2 * (NB + 2) ? 2 : 1;
      if (fill && i % EV == EV - 1 && i / EV < NB + 2) { __builtin_amdgcn_sched_barrier(0); piece(kt, buf, i / EV); __builtin_amdgcn_sched_barrier(0); }
    }
    __builtin_amdgcn_sched_barrier(0);
  };
  static_assert(MT * NT >= NB + 2 && MT * NT >= MT + NT, "every piece and fragment needs a product to hide behind");
  wgbar();
#pragma unroll
  for (int st = 0; st < NSTG; ++st)
#pragma unroll
    for (int i = 0; i < NB + 2; ++i) piece(st, st, i);
  wait_vm<(NSTG - 1) * (2 + NB)>();
  __builtin_amdgcn_s_barrier(); CFENCE();
#pragma unroll
  for (int i = 0; i < MT + NT; ++i) frag1(0, 0, i);
  int buf = 0;
  for (int kt = 0; kt < nkt - NSTG; ++kt) {
    const int nb = buf == NSTG - 1 ? 0 : buf + 1;
    mma(0, buf, false, 0, 0);
    wait_vm<(NSTG - 2) * (2 + NB)>();
    WAIT_LGKM();
    __builtin_amdgcn_s_barrier(); CFENCE();
    mma(1, nb, true, kt + NSTG, buf);
    buf = nb;
  }
  static_for<NSTG>([&](auto ic) {
    constexpr int R = decltype(ic)::v;
    const int nb = buf == NSTG - 1 ? 0 : buf + 1;
    mma(0, buf, false, 0, 0);
    if (R < NSTG - 1) {
      wait_vm<(NSTG - 2 - R > 0 ? NSTG - 2 - R : 0) * (2 + NB)>();
      __builtin_amdgcn_s_barrier(); CFENCE();
    }
    mma(1, R < NSTG - 1 ? nb : -1, false, 0, 0);
    buf = nb;
  });
}

template <int BN, int MODE, int WM = 2>
__device__ __forceinline__ void proj_task(const Params& p, LAS u8* L, rsrc_t rs, Near& nr, bool& dead, int blk, int mtx, int col0) {
  constexpr int MT = 8 / WM, NT = BN * WM / 128;
  const int tid = fresh_tid(), lane = tid & 63, w = __builtin_amdgcn_readfirstlane(tid >> 6), wm = w / (8 / WM), wn = w % (8 / WM);
  f32x4 acc[MT][NT];
  const u16* W = (mtx == 0 ? p.Wk : mtx == 1 ? p.Wv : p.Wq) + col0;
  gemm_tile<BN, MODE, WM>(L, p.x + (size_t)blk * 128 * DM, DM, W, HD, DM / 64, acc, BN == 256 ? ROT_A * blk + ROT_B * (col0 >> 8) : 0);
  const int cw = col0 + wn * 16 * NT, h = cw >> 7;
  if (BN == 64 && blk == 0) near_wait((gu32*)p.ws, nr, dead);
  const unsigned reg = mtx == 0 ? OFF_KX : mtx == 1 ? OFF_VX : OFF_QX;
  const int s0 = blk * 4 + (MT / 2) * wm;
  if (MODE == M_K) {
#pragma unroll
    for (int m = 0; m < MT; ++m) {
      const unsigned o = nr.frag(reg, s0 + (m >> 1), h) + lane * 16 + ((m & 1) * 4 + ((cw & 127) >> 5)) * 1024;
#pragma unroll
      for (int v = 0; v < NT / 2; ++v)
        st16(rs, o + v * 1024, v4u{pk2(acc[m][2 * v][0], acc[m][2 * v + 1][0]), pk2(acc[m][2 * v][1], acc[m][2 * v + 1][1]),
                                   pk2(acc[m][2 * v][2], acc[m][2 * v + 1][2]), pk2(acc[m][2 * v][3], acc[m][2 * v + 1][3])});
    }
  } else {
#pragma unroll
    for (int sg = 0; sg < MT / 2; ++sg)
#pragma unroll
      for (int n = 0; n < NT; ++n)
        st16(rs, nr.frag(reg, s0 + sg, h) + lane * 16 + (((cw & 127) >> 4) + n) * 1024,
             v4u{pk2(acc[2 * sg][n][0], acc[2 * sg][n][1]), pk2(acc[2 * sg][n][2], acc[2 * sg][n][3]),
                 pk2(acc[2 * sg + 1][n][0], acc[2 * sg + 1][n][1]), pk2(acc[2 * sg + 1][n][2], acc[2 * sg + 1][n][3])});
  }
  DRAIN_VM();
  wgbar();
  gu32* ctrl = (gu32*)p.ws;
  if (tid < BN / 64) {
    const int word = mtx == 2 ? W_FQ + blk * 64 + (col0 >> 6) + tid : W_FKV + blk * 128 + mtx * 64 + (col0 >> 6) + tid;
    __hip_atomic_store(ctrl + word, 1u, RLX_AGENT);
  }
}

template <int MODE>
__device__ __forceinline__ void proj_big(const Params& p, LAS u8* L, rsrc_t rs, const Near& nr, int blk, int mtx, int col0) {
  constexpr int NS = 5, STG = 32768, NK = DM / 32;
  const int tid = fresh_tid();
  const int lane = tid & 63, w = __builtin_amdgcn_readfirstlane(tid >> 6), wm = w >> 2, wn = w & 3, r16 = lane & 15, g = lane >> 4, q = r16 >> 2;
  const u16* A = p.x + (size_t)blk * 128 * DM;
  const u16* W = (mtx == 0 ? p.Wk : mtx == 1 ? p.Wv : p.Wq) + col0;
  const unsigned lb = (unsigned)(size_t)L;
  const int ar = lane >> 2;
  const unsigned avo = ((32 * w + ar) * DM + 8 * ((lane & 3) ^ ((4 - (ar >> 2)) & 3))) * 2;
  const int kr = 8 * (w >> 1) + (lane >> 3);
  const unsigned wvo = (kr * HD + 128 * (w & 1) + 16 * (((lane & 7) >> 1) ^ (((kr >> 1) & 1) | (((kr >> 3) & 1) << 1))) + 8 * (lane & 1)) * 2;
  auto piece = [&](int kt, int buf, int i) {
    const unsigned sb = lb + buf * STG;
    if (i < 2) dma16<false>(A + kt * 32 + i * 16 * DM, avo, sb + (2 * w + i) * 1024);
    else dma16<false>(W + (size_t)kt * 32 * HD + 64 * (i - 2), wvo, sb + 16384 + (2 * w + i - 2) * 1024);
  };
  const unsigned aoff = lb + (128 * wm + r16) * 64 + ((g ^ ((4 - q) & 3)) * 16);
  const int fr = ((q >> 1) & 1) | ((g & 1) << 1);
  const unsigned boff = lb + 16384 + (g * 4 + wn) * 1024 + q * 128 + (lane & 3) * 8;
  f32x4 acc[8][4];
#pragma unroll
  for (int i = 0; i < 32; ++i) acc[i >> 2][i & 3] = f32x4{0.f, 0.f, 0.f, 0.f};
  v4u af[2][8], bf[2][4];
  auto frag1 = [&](int buf, int set, int i) {
    const unsigned so = buf * STG;
    if (i < 4) {
      const v2u x0 = trread(boff + so + ((i ^ fr) * 32)), x1 = trread(boff + so + ((i ^ fr) * 32) + 512);
      bf[set][i] = v4u{x0.x, x0.y, x1.x, x1.y};
    } else af[set][i - 4] = *(LAS v4u*)(size_t)(aoff + so + (i - 4) * 1024);
  };
  auto mma = [&](int set, int nb, bool fill, int kt, int buf) {
#pragma unroll
    for (int i = 0; i < 32; ++i) {
      const int m = i >> 2, n = i & 3;
      acc[m][n] = MODE == M_V ? mfma32(af[set][m], bf[set][n], acc[m][n]) : mfma32(bf[set][n], af[set][m], acc[m][n]);
      if ((i & 1) == 0 && i < 24 && nb >= 0) { __builtin_amdgcn_sched_barrier(0); frag1(nb, set ^ 1, i >> 1); __builtin_amdgcn_sched_barrier(0); }
      if (fill && (i & 7) == 5) { __builtin_amdgcn_sched_barrier(0); piece(kt, buf, i >> 3); __builtin_amdgcn_sched_barrier(0); }
    }
    __builtin_amdgcn_sched_barrier(0);
  };
  wgbar();
#pragma unroll
  for (int st = 0; st < NS; ++st)
#pragma unroll
    for (int i = 0; i < 4; ++i) piece(st, st, i);
  wait_vm<(NS - 1) * 4>();
  __builtin_amdgcn_s_barrier(); CFENCE();
#pragma unroll
  for (int i = 0; i < 12; ++i) frag1(0, 0, i);
  int buf = 0;
  auto step = [&](int kt, int set, auto inflight, bool fill) {
    const int nb = buf == NS - 1 ? 0 : buf + 1;
    wait_vm<decltype(inflight)::v * 4>();
    WAIT_LGKM();
    __builtin_amdgcn_s_barrier(); CFENCE();
    mma(set, nb, fill, kt + NS, buf);
    buf = nb;
  };
  for (int kt = 0; kt < NK - NS - 1; kt += 2) { step(kt, 0, IC<NS - 2>{}, true); step(kt + 1, 1, IC<NS - 2>{}, true); }
  static_assert((NK - NS - 1) % 2 == 0 && NS == 5, "tail written out for these sizes");
  step(NK - 6, 0, IC<3>{}, true);
  step(NK - 5, 1, IC<3>{}, false);
  step(NK - 4, 0, IC<2>{}, false);
  step(NK - 3, 1, IC<1>{}, false);
  step(NK - 2, 0, IC<0>{}, false);
  mma(1, -1, false, 0, 0);

  const int cw = col0 + wn * 64, h = cw >> 7, s0 = blk * 4 + 4 * wm;
  const unsigned reg = mtx == 0 ? OFF_KX : mtx == 1 ? OFF_VX : OFF_QX;
#pragma unroll
  for (int sg = 0; sg < 4; ++sg) {
    const unsigned o = nr.frag(reg, s0 + sg, h) + lane * 16;
    if (MODE == M_K) {
#pragma unroll
      for (int i = 0; i < 4; ++i) {
        const int m = 2 * sg + (i >> 1), v = i & 1;
        st16(rs, o + ((i >> 1) * 4 + ((cw & 127) >> 5) + v) * 1024,
             v4u{pk2(acc[m][2 * v][0], acc[m][2 * v + 1][0]), pk2(acc[m][2 * v][1], acc[m][2 * v + 1][1]),
                 pk2(acc[m][2 * v][2], acc[m][2 * v + 1][2]), pk2(acc[m][2 * v][3], acc[m][2 * v + 1][3])});
      }
    } else {
#pragma unroll
      for (int n = 0; n < 4; ++n)
        st16(rs, o + (((cw & 127) >> 4) + n) * 1024,
             v4u{pk2(acc[2 * sg][n][0], acc[2 * sg][n][1]), pk2(acc[2 * sg][n][2], acc[2 * sg][n][3]),
                 pk2(acc[2 * sg + 1][n][0], acc[2 * sg + 1][n][1]), pk2(acc[2 * sg + 1][n][2], acc[2 * sg + 1][n][3])});
    }
  }
  DRAIN_VM();
  wgbar();
  if (tid < 8) {
    const int b2 = blk + (tid >> 2);
    const int word = (mtx == 2 ? W_FQ + b2 * 64 : W_FKV + b2 * 128 + mtx * 64) + (col0 >> 6) + (tid & 3);
    __hip_atomic_store((gu32*)p.ws + word, 1u, RLX_AGENT);
  }
}

__device__ __forceinline__ float sigmoidf_(float v) { return 1.0f / (1.0f + __expf(-v)); }

__device__ __forceinline__ void rg_task(const Params& p, rsrc_t rs, int wk) {
  const int tid = fresh_tid(), lane = tid & 63, w = __builtin_amdgcn_readfirstlane(tid >> 6);
  v4u wr[2], gw[2];
#pragma unroll
  for (int i = 0; i < 2; ++i) { wr[i] = *(const GAS v4u*)(p.wr + i * 512 + lane * 8); gw[i] = *(const GAS v4u*)(p.gw + i * 512 + lane * 8); }
#pragma unroll
  for (int t = 0; t < 2; ++t) {
    const int tok = wk * 16 + 2 * w + t;
    float r = 0.f, gs = 0.f;
#pragma unroll
    for (int i = 0; i < 2; ++i) {
      const v4u xv = *(const GAS v4u*)(p.x + (size_t)tok * DM + i * 512 + lane * 8);
#pragma unroll
      for (int d = 0; d < 4; ++d) {
        r = fmaf(bflo(xv[d]), bflo(wr[i][d]), r); r = fmaf(bfhi(xv[d]), bfhi(wr[i][d]), r);
        gs += bfr(bflo(xv[d]) * bflo(gw[i][d])); gs += bfr(bfhi(xv[d]) * bfhi(gw[i][d]));
      }
    }
    r = bfr(wave_sum(r)); gs = bfr(wave_sum(gs));
    if (lane == 0) { st4(rs, OFF_R + tok * 4, f2u(r)); st4(rs, OFF_G + tok * 4, f2u(bfr(sigmoidf_(gs)))); }
  }
  DRAIN_VM();
  wgbar();
  if (tid == 0) __hip_atomic_store((gu32*)p.ws + W_RG + wk * LN, 1u, RLX_AGENT);
}

__device__ __forceinline__ unsigned long long top_key(unsigned bits, int idx) {
  const unsigned o = (bits & 0x7FFFFFFFu) > 0x7F800000u ? 0u : ((bits & 0x80000000u) ? ~bits : (bits | 0x80000000u));
  return ((unsigned long long)o << 32) | (unsigned)(0x7FFFFFFF - idx);
}
__device__ __forceinline__ void top_ins(unsigned long long k, unsigned long long& a, unsigned long long& b) {
  const unsigned long long lo = k < a ? k : a;
  a = k < a ? a : k;
  b = lo > b ? lo : b;
}
__device__ __forceinline__ void top2(rsrc_t rs, LAS u8* L, int& i0, int& i1) {
  const int tid = fresh_tid(), lane = tid & 63, w = tid >> 6;
  const v4u rv = ld16(rs, OFF_R + tid * 16);
  unsigned long long a = 0ull, b = 0ull;
#pragma unroll
  for (int d = 0; d < 4; ++d) top_ins(top_key(rv[d], tid * 4 + d), a, b);
#pragma unroll
  for (int o = 1; o < 64; o <<= 1) {
    const unsigned long long c0 = __shfl_xor(a, o), c1 = __shfl_xor(b, o);
    top_ins(c0, a, b); top_ins(c1, a, b);
  }
  LAS unsigned long long* tv = (LAS unsigned long long*)L;
  wgbar();
  if (lane == 0) { tv[2 * w] = a; tv[2 * w + 1] = b; }
  wgbar();
  a = b = 0ull;
#pragma unroll
  for (int k = 0; k < 16; ++k) top_ins(tv[k], a, b);
  wgbar();
  i0 = (0x7FFFFFFF - (int)(unsigned)a) & 2047; i1 = (0x7FFFFFFF - (int)(unsigned)b) & 2047;
}

__device__ __forceinline__ void mlp1_task(const Params& p, LAS u8* L, rsrc_t rs, int t, bool& dead) {
  const int tid = fresh_tid();
  gu32* ctrl = (gu32*)p.ws;
  {
    const int rg = tid >> 3, cq = tid & 7;
    v4u gv[16], uv[16];
#pragma unroll
    for (int i = 0; i < 16; ++i) {
      gv[i] = *(const GAS v4u*)(p.wg + (size_t)(rg + 64 * i) * FF + 64 * t + 8 * cq);
      uv[i] = *(const GAS v4u*)(p.wu + (size_t)(rg + 64 * i) * FF + 64 * t + 8 * cq);
    }
    __builtin_amdgcn_sched_barrier(0);
    if (tid < 128) wait_lines(ctrl, W_RG + (tid >> 6) * 64 * LN, LN, 64, 1u, tid & 63, 0x40u, dead);
    wgbar();
    int i0, i1;
    top2(rs, L, i0, i1);
    if (t == 0 && tid == 0) {
      const float r0 = u2f(ld4(rs, OFF_R + i0 * 4)), r1 = u2f(ld4(rs, OFF_R + i1 * 4));
      st16(rs, W_TOP * 4, v4u{(unsigned)i0, (unsigned)i1, f2u(bfr(sigmoidf_(r0))), f2u(bfr(sigmoidf_(r1)))});
    }
    LAS float* X = (LAS float*)(L + 69632);
#pragma unroll
    for (int k = 0; k < 4; ++k) X[tid + 512 * k] = bf2f(p.x[(size_t)(k < 2 ? i0 : i1) * DM + tid + 512 * (k & 1)]);
    wgbar();
    float ag[2][8], au[2][8];
#pragma unroll
    for (int e = 0; e < 8; ++e) { ag[0][e] = ag[1][e] = au[0][e] = au[1][e] = 0.f; }
#pragma unroll
    for (int i = 0; i < 16; ++i) {
      const float x0 = X[rg + 64 * i], x1 = X[1024 + rg + 64 * i];
#pragma unroll
      for (int d = 0; d < 4; ++d) {
        ag[0][2 * d] = fmaf(x0, bflo(gv[i][d]), ag[0][2 * d]); ag[0][2 * d + 1] = fmaf(x0, bfhi(gv[i][d]), ag[0][2 * d + 1]);
        ag[1][2 * d] = fmaf(x1, bflo(gv[i][d]), ag[1][2 * d]); ag[1][2 * d + 1] = fmaf(x1, bfhi(gv[i][d]), ag[1][2 * d + 1]);
        au[0][2 * d] = fmaf(x0, bflo(uv[i][d]), au[0][2 * d]); au[0][2 * d + 1] = fmaf(x0, bfhi(uv[i][d]), au[0][2 * d + 1]);
        au[1][2 * d] = fmaf(x1, bflo(uv[i][d]), au[1][2 * d]); au[1][2 * d + 1] = fmaf(x1, bfhi(uv[i][d]), au[1][2 * d + 1]);
      }
    }
    LAS float* R = (LAS float*)L;
#pragma unroll
    for (int e = 0; e < 8; ++e) {
      R[(0 * 64 + rg) * 65 + 8 * cq + e] = ag[0][e]; R[(1 * 64 + rg) * 65 + 8 * cq + e] = ag[1][e];
      R[(2 * 64 + rg) * 65 + 8 * cq + e] = au[0][e]; R[(3 * 64 + rg) * 65 + 8 * cq + e] = au[1][e];
    }
  }
  wgbar();
  if (tid < 128) {
    const LAS float* R = (const LAS float*)L;
    const int tok = tid >> 6, col = tid & 63;
    float gs = 0.f, us = 0.f;
#pragma unroll 16
    for (int r = 0; r < 64; ++r) { gs += R[(tok * 64 + r) * 65 + col]; us += R[((2 + tok) * 64 + r) * 65 + col]; }
    gs = bfr(gs); us = bfr(us);
    const float act = bfr(gs * sigmoidf_(gs));
    st4(rs, OFF_HID + (tok * FF + 64 * t + col) * 4, f2u(bfr(act * us)));
  }
  DRAIN_VM();
  wgbar();
  if (tid == 0) __hip_atomic_store(ctrl + W_M1 + t * LN, 1u, RLX_AGENT);
}
__device__ __forceinline__ void mlp2_task(const Params& p, LAS u8* L, rsrc_t rs, int t, bool& dead) {
  const int tid = fresh_tid();
  gu32* ctrl = (gu32*)p.ws;
  const int kc = t >> 2, cb = t & 3, rg = tid >> 5, cq = tid & 31;
  v4u dv[16];
#pragma unroll
  for (int i = 0; i < 16; ++i) dv[i] = *(const GAS v4u*)(p.wd + (size_t)(256 * kc + rg + 16 * i) * DM + 256 * cb + 8 * cq);
  __builtin_amdgcn_sched_barrier(0);
  if (tid < 64) wait_lines(ctrl, W_M1 + 4 * kc * LN, LN, 4, 1u, tid, 0x41u, dead);
  wgbar();
  LAS float* Hh = (LAS float*)(L + 69632);
  Hh[tid] = u2f(ld4(rs, OFF_HID + ((tid >> 8) * FF + 256 * kc + (tid & 255)) * 4));
  wgbar();
  float a[2][8];
#pragma unroll
  for (int e = 0; e < 8; ++e) a[0][e] = a[1][e] = 0.f;
#pragma unroll
  for (int i = 0; i < 16; ++i) {
    const float h0 = Hh[rg + 16 * i], h1 = Hh[256 + rg + 16 * i];
#pragma unroll
    for (int d = 0; d < 4; ++d) {
      a[0][2 * d] = fmaf(h0, bflo(dv[i][d]), a[0][2 * d]); a[0][2 * d + 1] = fmaf(h0, bfhi(dv[i][d]), a[0][2 * d + 1]);
      a[1][2 * d] = fmaf(h1, bflo(dv[i][d]), a[1][2 * d]); a[1][2 * d + 1] = fmaf(h1, bfhi(dv[i][d]), a[1][2 * d + 1]);
    }
  }
  LAS float* R2 = (LAS float*)L;
#pragma unroll
  for (int e = 0; e < 8; ++e) { R2[(0 * 16 + rg) * 257 + 8 * cq + e] = a[0][e]; R2[(1 * 16 + rg) * 257 + 8 * cq + e] = a[1][e]; }
  wgbar();
  {
    const int tok = tid >> 8, col = tid & 255;
    float sacc = 0.f;
#pragma unroll
    for (int r = 0; r < 16; ++r) sacc += R2[(tok * 16 + r) * 257 + col];
    st4(rs, OFF_YP + ((kc * 2 + tok) * DM + 256 * cb + col) * 4, f2u(sacc));
  }
  DRAIN_VM();
  wgbar();
  if (tid == 0) __hip_atomic_store(ctrl + W_M2 + t * LN, 1u, RLX_AGENT);
}

__device__ __forceinline__ void seg_softmax(const v4u (&F)[4], const v4u (&KX)[2][4], const v4u (&QX)[4], int tq, int g, v4u& pb, v2u& pm) {
  const float scale = 0.08838834764831845f, l2e = 1.4426950408889634f, neg = bfr(-1000000000.0f);
  f32x4 sA = {0.f, 0.f, 0.f, 0.f}, sB = sA, sM = sA;
#pragma unroll
  for (int u = 0; u < 4; ++u) { sM = mfma32(F[u], QX[u], sM); sA = mfma32(KX[0][u], QX[u], sA); sB = mfma32(KX[1][u], QX[u], sB); }
#pragma unroll
  for (int r = 0; r < 4; ++r) {
    sA[r] = 4 * g + r > tq ? neg : bfr(bfr(sA[r]) * scale);
    sB[r] = 16 + 4 * g + r > tq ? neg : bfr(bfr(sB[r]) * scale);
    sM[r] = g < 2 ? -INFINITY : bfr(bfr(sM[r]) * scale);
  }
  float mx = fmaxf(fmaxf(fmaxf(sA[0], sA[1]), fmaxf(sA[2], sA[3])), fmaxf(fmaxf(sB[0], sB[1]), fmaxf(sB[2], sB[3])));
  mx = fmaxf(mx, fmaxf(fmaxf(sM[0], sM[1]), fmaxf(sM[2], sM[3])));
  mx = xmax32(xmax16(mx));
  float sum = 0.f;
#pragma unroll
  for (int r = 0; r < 4; ++r) {
    sA[r] = __builtin_amdgcn_exp2f((sA[r] - mx) * l2e); sB[r] = __builtin_amdgcn_exp2f((sB[r] - mx) * l2e); sM[r] = __builtin_amdgcn_exp2f((sM[r] - mx) * l2e);
    sum += sA[r] + sB[r] + sM[r];
  }
  const float inv = 1.0f / xadd32(xadd16(sum));
  pb = v4u{pk2(sA[0] * inv, sA[1] * inv), pk2(sA[2] * inv, sA[3] * inv), pk2(sB[0] * inv, sB[1] * inv), pk2(sB[2] * inv, sB[3] * inv)};
  pm = v2u{pk2(sM[0] * inv, sM[1] * inv), pk2(sM[2] * inv, sM[3] * inv)};
}

__device__ __forceinline__ void attn_task(const Params& p, rsrc_t rs, const Near& nr, int s, int h, bool& dead) {
  const int lane = fresh_tid() & 63, r16 = lane & 15, g = lane >> 4, gg = g & 1;
  gu32* ctrl = (gu32*)p.ws;
  const int blk = s >> 2;
  wait_flags2(ctrl, W_FKV + blk * 128 + 2 * h, W_FKV + blk * 128 + 64 + 2 * h, 0x50u, dead);
  wait_flags2(ctrl, W_FQ + blk * 64 + 2 * h, W_FQ + blk * 64 + 2 * h, 0x51u, dead);
  CFENCE();
  const unsigned fk = nr.frag(OFF_KX, s, h) + lane * 16, fq = nr.frag(OFF_QX, s, h) + lane * 16, fv = nr.frag(OFF_VX, s, h) + lane * 16;
  v4u KX[2][4], QX[2][4], VX[8], F[4]; v2u VM[8];
#pragma unroll
  for (int i = 0; i < 8; ++i) { KX[i >> 2][i & 3] = ld16(rs, fk + i * 1024); QX[i >> 2][i & 3] = ld16(rs, fq + i * 1024); VX[i] = ld16(rs, fv + i * 1024); }
  wait_ge(ctrl, W_PROG + LN * h, (unsigned)(s + 1), 0x52u, dead);
  const unsigned qo = nr.blk(OFF_QKV + (unsigned)(s * QH + (h & (QH - 1))) * 16384u, nr.rel(h * 4));
  for (unsigned sp = 0;;) {
    CFENCE();
    unsigned m = 0;
#pragma unroll
    for (int u = 0; u < 4; ++u) { F[u] = ld16(rs, qo + u * 1024 + lane * 16); m = max(m, umax4(F[u])); }
#pragma unroll
    for (int e = 0; e < 8; ++e) { VM[e] = ld8(rs, qo + 4096 + (16 * e + r16) * 16 + gg * 8); m = max(m, max(VM[e].x, VM[e].y)); }
    if (__all(m != SENT) || dead) break;
    if (spin_fail(sp, ctrl, 0x53u)) { dead = true; break; }
  }
#pragma unroll
  for (int qt = 0; qt < 2; ++qt) {
    v4u pb; v2u pm;
    seg_softmax(F, KX, QX[qt], 16 * qt + r16, g, pb, pm);
    const unsigned ao = nr.frag(OFF_AX, s, h) + lane * 16 + qt * 4096;
#pragma unroll
    for (int v = 0; v < 4; ++v) {
      f32x4 o0 = mfma32(VX[2 * v], pb, f32x4{0.f, 0.f, 0.f, 0.f}), o1 = mfma32(VX[2 * v + 1], pb, f32x4{0.f, 0.f, 0.f, 0.f});
      o0 = mfma16(VM[2 * v], pm, o0); o1 = mfma16(VM[2 * v + 1], pm, o1);
      st16(rs, ao + v * 1024, v4u{pk2(o0[0], o0[1]), pk2(o0[2], o0[3]), pk2(o1[0], o1[1]), pk2(o1[2], o1[3])});
    }
  }
  DRAIN_VM();
}

__device__ __forceinline__ void mix_store(const Params& p, rsrc_t rs, const v4u& top, int tok, int col, f32x4 a, float gt, v2u xv) {
  const float og = bfr(1.0f - gt);
  const int sel = tok == (int)top.x ? 0 : tok == (int)top.y ? 1 : -1;
  float bv[4] = {bflo(xv.x), bfhi(xv.x), bflo(xv.y), bfhi(xv.y)};
  if (sel >= 0) {
    f32x4 y = {0.f, 0.f, 0.f, 0.f};
    for (int kc = 0; kc < 14; ++kc) y += __builtin_bit_cast(f32x4, ld16(rs, OFF_YP + ((kc * 2 + sel) * DM + col) * 4));
    const float rsel = u2f(sel == 0 ? top.z : top.w);
#pragma unroll
    for (int r = 0; r < 4; ++r) bv[r] = bfr(bv[r] + bfr(bfr(y[r]) * rsel));
  }
  float o[4];
#pragma unroll
  for (int r = 0; r < 4; ++r) o[r] = bfr(gt * bfr(a[r])) + bfr(og * bv[r]);
  *(GAS v2u*)(p.out + (size_t)tok * DM + col) = v2u{pk2(o[0], o[1]), pk2(o[2], o[3])};
}

__device__ __forceinline__ void oproj_task(const Params& p, LAS u8* L, rsrc_t rs, const Near& nr, int blk, int ct, bool& dead) {
  const int tid = fresh_tid(), lane = tid & 63, w = __builtin_amdgcn_readfirstlane(tid >> 6), wm = w >> 2, wn = w & 3, r16 = lane & 15, g = lane >> 4;
  gu32* ctrl = (gu32*)p.ws;
  if (w == 0) {
    wait_lines(ctrl, W_M2, LN, 56, 1u, lane, 0x60u, dead);
    wait_lines(ctrl, W_T2 + 8 * blk * LN, LN, 8, 1u, lane, 0x61u, dead);
    __builtin_amdgcn_fence(__ATOMIC_ACQUIRE, "agent");
    DRAIN_VM();
  }
  f32x4 acc[4][2];
  gemm_tile<128, M_O>(L, (const u16*)p.ws, blk * 4, p.Wo + ct * 128, DM, HD / 64, acc, ROT_O * ct, &nr);
  const v4u top = ld16(rs, W_TOP * 4);
#pragma unroll
  for (int m = 0; m < 4; ++m)
#pragma unroll
    for (int n = 0; n < 2; ++n) {
      const int tok = blk * 128 + 64 * wm + 16 * m + r16, col = ct * 128 + 32 * wn + 16 * n + 4 * g;
      mix_store(p, rs, top, tok, col, acc[m][n], u2f(ld4(rs, OFF_G + tok * 4)), *(const GAS v2u*)(p.x + (size_t)tok * DM + col));
    }
}

__device__ __forceinline__ void stream_role(const Params& p, LAS u8* L, rsrc_t rs, const Near& nr, int set, int q, bool& dead) {
  int wst = 100; const int wk = set * 32 + q; (void)wst; (void)wk;
  const int tid = fresh_tid(), lane = tid & 63, w = __builtin_amdgcn_readfirstlane(tid >> 6), r16 = lane & 15, g = lane >> 4, gg = g & 1;
  const int ks = q >> 3, ct = q & 7, np = w & 3, kh = w >> 2;
  gu32* ctrl = (gu32*)p.ws;
  const unsigned lb = (unsigned)(size_t)L;
  v4u Wf[16][2];
  LAS u8* Wl = L + 65536 + w * 8192 + lane * 16;
  {
    const int kr = 8 * w + (lane >> 3);
    const int krs = (kr & ~31) + 16 * ((kr >> 2) & 1) + 4 * ((kr >> 3) & 3) + (kr & 3);
    const int fsw = ((kr >> 1) & 1) | (((kr >> 3) & 1) << 1);
    const unsigned wvo = (krs * DM + 16 * (((lane & 7) >> 1) ^ fsw) + 8 * (lane & 1)) * 2;
    const int qq = r16 >> 2, fr = ((qq >> 1) & 1) | ((g & 1) << 1);
    const unsigned boff = lb + kh * 65536 + (g * 2 + (np >> 1)) * 1024 + qq * 128 + (lane & 3) * 8;
    const unsigned b0 = boff + (((2 * (np & 1)) ^ fr) * 32), b1 = boff + (((2 * (np & 1) + 1) ^ fr) * 32);
    static_for<2>([&](auto rc) {
      constexpr int RD = decltype(rc)::v;
      wgbar();
#pragma unroll
      for (int kt = 0; kt < 8; ++kt) {
        const u16* wb = p.Wo + (size_t)(1024 * ks + 512 * (kt >> 2) + 64 * (4 * RD + (kt & 3))) * DM + 128 * ct;
        dma16<false>(wb, wvo, lb + kt * 16384 + (w * 2) * 1024);
        dma16<false>(wb + 64, wvo, lb + kt * 16384 + (w * 2 + 1) * 1024);
      }
      DRAIN_VM();
      wgbar();
#pragma unroll
      for (int i = 0; i < 8; ++i) {
        const unsigned o = (i >> 1) * 16384 + (i & 1) * 8192;
        const v2u x0 = trread(b0 + o), x1 = trread(b0 + o + 512), y0 = trread(b1 + o), y1 = trread(b1 + o + 512);
        Wf[8 * RD + i][0] = v4u{x0.x, x0.y, x1.x, x1.y}; Wf[8 * RD + i][1] = v4u{y0.x, y0.y, y1.x, y1.y};
      }
    });
  }
  wgbar();
#pragma unroll
  for (int i = 0; i < 8; ++i) *(LAS v4u*)(Wl + i * 1024) = Wf[12 + (i >> 1)][i & 1];
  wait_lines(ctrl, W_M2, LN, 56, 1u, lane, 0x70u, dead);
  CFENCE();
  const v4u top = ld16(rs, W_TOP * 4);

  auto run = [&](auto role) {
    constexpr bool ATT = decltype(role)::v == 0;
    const int qt = w & 1, eh = w >> 1;
    v4u KX[2][4], QX[4], VX[4];
    auto prefetch = [&](int s) {
      if ((s & 3) < 2) {
        const int kind = (lane >> 1) % 3;
        gu32* f = ctrl + (kind == 2 ? W_FQ + (s >> 2) * 64 : W_FKV + (s >> 2) * 128 + kind * 64) + 2 * q + (lane & 1);
        for (unsigned sp = 0; !dead;) {
          if (__all(__hip_atomic_load(f, RLX_AGENT) != 0u)) break;
          __builtin_amdgcn_s_sleep(8);
          if (spin_fail(sp, ctrl, 0x73u)) dead = true;
        }
        CFENCE();
      }
      const unsigned fk = nr.frag(OFF_KX, s, q) + lane * 16, fq = nr.frag(OFF_QX, s, q) + lane * 16, fv = nr.frag(OFF_VX, s, q) + lane * 16;
#pragma unroll
      for (int i = 0; i < 8; ++i) KX[i >> 2][i & 3] = ld16(rs, fk + i * 1024);
#pragma unroll
      for (int i = 0; i < 4; ++i) { QX[i] = ld16(rs, fq + qt * 4096 + i * 1024); VX[i] = ld16(rs, fv + (4 * eh + i) * 1024); }
    };
    auto finish = [&](int s) {
      const int f = 4 * ks + (w & 3);
      wait_lines(ctrl, W_SP + (set * 32 + ct) * LN, 8 * LN, 4, (unsigned)(s + 1), lane, 0x72u, dead);
      CFENCE();
      f32x4 sum = {0.f, 0.f, 0.f, 0.f};
#pragma unroll
      for (int k = 0; k < 8; ++k) sum += __builtin_bit_cast(f32x4, ld16(rs, nr.frag(OFF_PS, 0, (((s & 7) * 8 + ct) * 8 + k) * 2 + (f >> 3)) + (f & 7) * 1024 + lane * 16));
      { const int tok = s * 32 + 16 * (f & 1) + r16, col = ct * 128 + 16 * (f >> 1) + 4 * g;
        mix_store(p, rs, top, tok, col, sum, u2f(ld4(rs, OFF_G + tok * 4)), *(const GAS v2u*)(p.x + (size_t)tok * DM + col)); }
    };
    unsigned prog = 0;
    if (ATT) prefetch(NBULK * 4 + set);
    for (int s = NBULK * 4 + set; s < NSEG; s += 2) {
      const int si = s - NBULK * 4;
      if (ATT) {
        const unsigned need = (unsigned)(s + 1 < NSEG - 1 ? s + 1 : NSEG - 1);
        if (prog < need) {
          for (unsigned sp = 0; !dead;) {
            prog = __builtin_amdgcn_readfirstlane(__hip_atomic_load(ctrl + W_PROG + LN * q, RLX_AGENT));
            if (prog >= need) break;
            __builtin_amdgcn_s_sleep(2);
            if (spin_fail(sp, ctrl, 0x52u)) dead = true;
          }
        }
        const unsigned qo = nr.blk(OFF_QKV + (unsigned)(s * QH + (q & (QH - 1))) * 16384u, nr.rel(q * 4));
        v4u F[4]; v2u VM[4];
        for (unsigned sp = 0;;) {
          CFENCE();
          unsigned m = 0;
#pragma unroll
          for (int u = 0; u < 4; ++u) { F[u] = ld16(rs, qo + u * 1024 + lane * 16); m = max(m, umax4(F[u])); }
#pragma unroll
          for (int e = 0; e < 4; ++e) { VM[e] = ld8(rs, qo + 4096 + (16 * (4 * eh + e) + r16) * 16 + gg * 8); m = max(m, max(VM[e].x, VM[e].y)); }
          if (__all(m != SENT) || dead) break;
          if (spin_fail(sp, ctrl, 0x53u)) { dead = true; break; }
        }
        v4u pb; v2u pm;
        seg_softmax(F, KX, QX, 16 * qt + r16, g, pb, pm);
        const unsigned ao = nr.frag(OFF_AX, s, q) + qt * 4096 + eh * 2048 + lane * 16;
#pragma unroll
        for (int v = 0; v < 2; ++v) {
          f32x4 o0 = mfma32(VX[2 * v], pb, f32x4{0.f, 0.f, 0.f, 0.f}), o1 = mfma32(VX[2 * v + 1], pb, f32x4{0.f, 0.f, 0.f, 0.f});
          o0 = mfma16(VM[2 * v], pm, o0); o1 = mfma16(VM[2 * v + 1], pm, o1);
          st16(rs, ao + v * 1024, v4u{pk2(o0[0], o0[1]), pk2(o0[2], o0[3]), pk2(o1[0], o1[1]), pk2(o1[2], o1[3])});
        }
        DRAIN_VM();
      } else {
        if (si >= 4) finish(s - 4);
        DRAIN_VM();
      }
      wgbar();
      WSTAMP();
      if (tid == 0) __hip_atomic_store(ctrl + W_SA + (set * 32 + q) * LN, (unsigned)(s + 1), RLX_AGENT);
      if (tid == 64 && si >= 2) __hip_atomic_store(ctrl + W_SP + (set * 32 + q) * LN, (unsigned)(s - 1), RLX_AGENT);
      if (w == 4) wait_lines(ctrl, W_SA + (set * 32 + ks * 8) * LN, LN, 8, (unsigned)(s + 1), lane, 0x71u, dead);
      wgbar();
      WSTAMP();
      {
        const unsigned ao = nr.frag(OFF_AX, s, 8 * ks + w) + lane * 16;
        v4u t[8];
#pragma unroll
        for (int i = 0; i < 8; ++i) t[i] = ld16(rs, ao + i * 1024);
#pragma unroll
        for (int i = 0; i < 8; ++i) *(LAS v4u*)(L + w * 8192 + i * 1024 + lane * 16) = t[i];
      }
      if (ATT && s + 2 < NSEG) prefetch(s + 2);
      wgbar();
      f32x4 a[2][2];
#pragma unroll
      for (int i = 0; i < 4; ++i) a[i >> 1][i & 1] = f32x4{0.f, 0.f, 0.f, 0.f};
#pragma unroll
      for (int k = 0; k < 16; ++k) {
        const LAS u8* fp = L + kh * 32768 + (k >> 2) * 8192 + (k & 3) * 1024 + lane * 16;
        const v4u x0 = *(const LAS v4u*)fp, x1 = *(const LAS v4u*)(fp + 4096);
#pragma unroll
        for (int n = 0; n < 2; ++n) {
          const v4u wf = k < 12 ? Wf[k][n] : *(const LAS v4u*)(Wl + ((k - 12) * 2 + n) * 1024);
          a[n][0] = mfma32(wf, x0, a[n][0]); a[n][1] = mfma32(wf, x1, a[n][1]);
        }
      }
      WSTAMP();
      const unsigned pb = nr.frag(OFF_PS, 0, (((s & 7) * 8 + ct) * 8 + 2 * ks + kh) * 2 + (np >> 1)) + 4 * (np & 1) * 1024 + lane * 16;
#pragma unroll
      for (int i = 0; i < 4; ++i) st16(rs, pb + i * 1024, __builtin_bit_cast(v4u, a[i >> 1][i & 1]));
      WSTAMP();
    }
    DRAIN_VM();
    wgbar();
    if (tid == 0) __hip_atomic_store(ctrl + W_SP + (set * 32 + q) * LN, (unsigned)(NSEG - 1 + set), RLX_AGENT);
    finish(ATT ? NSEG - 4 + set : NSEG - 2 + set);
  };
  if (w < 4) run(IC<0>{}); else run(IC<1>{});
}

__device__ __forceinline__ void worker_role(const Params& p, LAS u8* L, rsrc_t rs, int b) {
  const int w = __builtin_amdgcn_readfirstlane(threadIdx.x >> 6);
  const int x8 = 2 * (b & 3) + (b >> 7), r = (b >> 3) & 15, wk = r * 8 + x8;
  gu32* ctrl = (gu32*)p.ws;
  bool dead = false;
  int wst = 0; (void)wst;
  Near nr = near_init(p.ws);
  WSTAMP();
  {
    const int cg = x8 * 16 + r;
    for (int blk = 0; blk < 3; ++blk) {
      if (cg < 64) proj_task<64, M_K, 4>(p, L, rs, nr, dead, blk, 0, cg * 64); else proj_task<64, M_V, 4>(p, L, rs, nr, dead, blk, 1, (cg - 64) * 64);
      WSTAMP();
    }
  }
  const int ct4 = x8 * 4 + (r & 3);
  for (int rnd = 0; rnd < 2; ++rnd) {
    if (rnd == 0 || r < 4) {
      const int blk = rnd == 0 ? 3 + (r >> 2) : 15;
      if (ct4 < 16) proj_task<256, M_K>(p, L, rs, nr, dead, blk, 0, ct4 * 256); else proj_task<256, M_V>(p, L, rs, nr, dead, blk, 1, (ct4 - 16) * 256);
    } else if (r < 8) {
      mlp2_task(p, L, rs, (r - 4) * 8 + x8, dead);
      if (r < 7) mlp2_task(p, L, rs, r * 8 + x8, dead);
    }
    if (rnd == 0) rg_task(p, rs, wk);
    WSTAMP();
    const int ct = rnd ? x8 * 2 + (r & 1) : ct4;
    const int blk = rnd ? (r >> 3) * 8 + 2 * ((r >> 1) & 3) : 7 + 2 * (r >> 2);
    if (rnd || ct < 16) proj_big<M_K>(p, L, rs, nr, blk, rnd ? 2 : 0, ct * 256); else proj_big<M_V>(p, L, rs, nr, blk, 1, (ct - 16) * 256);
    WSTAMP();
    if (rnd == 0 && r < 7) mlp1_task(p, L, rs, r * 8 + x8, dead);
  }
  WSTAMP();
  if (r < 8) {
    const int blk = x8, m = r;
    for (int i = 0; i < 2; ++i) { const int t = (i * 8 + m) * 8 + w; attn_task(p, rs, nr, blk * 4 + (t >> 5), t & 31, dead); WSTAMP(); }
    wgbar();
    if (threadIdx.x == 0) __hip_atomic_store(ctrl + W_T2 + (8 * blk + m) * LN, 1u, RLX_AGENT);
    oproj_task(p, L, rs, nr, blk, m, dead);
    WSTAMP();
  } else {
    const int sid = x8 * 8 + (r - 8);
    stream_role(p, L, rs, nr, sid >> 5, sid & 31, dead);
    WSTAMP();
  }
}

__global__ void __launch_bounds__(512) rmt_init_kernel(u8* ws, const u16* mem0) {
  const int tid = fresh_tid();
  if (blockIdx.x >= 4) {
    const Near n = near_init(ws);
    arm_share(__builtin_amdgcn_make_buffer_rsrc(ws, 0, WS_END, 0x00020000), n.al, blockIdx.x - 4, tid);
    return;
  }
  if (blockIdx.x >= 2) {
    const Near n = near_init(ws);
    const int i = (blockIdx.x - 2) * 512 + tid, cb = i >> 3, row = i & 7;
    const u16* m0 = mem0 + row * DM + (cb >> 2) * 32 + (cb & 3) * 4;
    const v2u lo = *(const GAS v2u*)m0, hi = *(const GAS v2u*)(m0 + 16);
    const v4u v = {canon(__builtin_amdgcn_perm(hi.x, lo.x, 0x05040100u)), canon(__builtin_amdgcn_perm(hi.x, lo.x, 0x07060302u)),
                   canon(__builtin_amdgcn_perm(hi.y, lo.y, 0x05040100u)), canon(__builtin_amdgcn_perm(hi.y, lo.y, 0x07060302u))};
    u8* d = ws + n.al + OFF_MEM + (cb >> 6) * 16384 + (cb & 63) * 128 + row * 16;
    *(GAS v4u*)d = v; *(GAS v4u*)(d + 8192) = v;
    return;
  }
  if (blockIdx.x == 0) {
    for (int i = tid; i < (int)(CTRL_BYTES / 16); i += 512)
      if (i != W_CFG / 4) ((GAS v4u*)ws)[i] = v4u{0u, 0u, 0u, 0u};
  } else if (tid == 0) {
    const Near n = near_init(ws);
    gu32* a0 = (gu32*)(ws + n.al + OFF_CAL); gu32* a1 = a0 + 2048;
    unsigned long long b0 = ~0ull, b1 = ~0ull;
    for (int i = 0; i < 3; ++i) {
      const unsigned long long t0 = __builtin_amdgcn_s_memtime();
      __hip_atomic_store(a0, 0u, RLX_AGENT); DRAIN_VM();
      const unsigned long long t1 = __builtin_amdgcn_s_memtime();
      __hip_atomic_store(a1, 0u, RLX_AGENT); DRAIN_VM();
      const unsigned long long t2 = __builtin_amdgcn_s_memtime();
      b0 = t1 - t0 < b0 ? t1 - t0 : b0; b1 = t2 - t1 < b1 ? t2 - t1 : b1;
    }
    const unsigned nz = ((b1 < b0 ? 1u : 0u) ^ ((n.va21 + OFF_CAL) >> 21) ^ (xcc_id() >> 2)) & 1u;
    ((GAS v4u*)ws)[W_CFG / 4] = v4u{nz, 0u, 0u, 0u};
  }
}

__global__ void __launch_bounds__(NTHR, 2) rmt_mod_kernel(Params p) {
  extern __shared__ __attribute__((aligned(16))) u8 smem[];
  LAS u8* L = (LAS u8*)smem;
  const rsrc_t rs = __builtin_amdgcn_make_buffer_rsrc(p.ws, 0, WS_END, 0x00020000);
  const int b = blockIdx.x;
  if ((b & 4) == 0) chain_role(p, L, rs, b); else worker_role(p, L, rs, b);
}

}

extern "C" void kernel_launch(void* const* d_in, const int* in_sizes, int n_in, void* d_out, int out_size, void* d_ws, size_t ws_size,
                              hipStream_t stream) {
  (void)in_sizes; (void)n_in; (void)out_size; (void)ws_size;
  Params p;
  p.x = (const u16*)d_in[0]; p.mem0 = (const u16*)d_in[1]; p.Wq = (const u16*)d_in[2]; p.Wk = (const u16*)d_in[3];
  p.Wv = (const u16*)d_in[4]; p.Wo = (const u16*)d_in[5]; p.wr = (const u16*)d_in[8]; p.wg = (const u16*)d_in[9];
  p.wu = (const u16*)d_in[10]; p.wd = (const u16*)d_in[11]; p.gw = (const u16*)d_in[12];
  p.out = (u16*)d_out; p.ws = (u8*)d_ws;
  (void)hipFuncSetAttribute((const void*)rmt_mod_kernel, hipFuncAttributeMaxDynamicSharedMemorySize, LDS_BYTES);
  rmt_init_kernel<<<4 + NCHAIN, 512, 0, stream>>>(p.ws, p.mem0);
  rmt_mod_kernel<<<NWG, NTHR, LDS_BYTES, stream>>>(p);
}
```

```cpp
#include <hip/hip_runtime.h>
#include <hip/hip_bf16.h>
#include <stdint.h>

#define GAS __attribute__((address_space(1)))
#define LAS __attribute__((address_space(3)))
typedef unsigned v4u __attribute__((ext_vector_type(4)));
typedef unsigned v2u __attribute__((ext_vector_type(2)));
typedef unsigned v8u __attribute__((ext_vector_type(8)));
typedef float f32x4 __attribute__((ext_vector_type(4)));
typedef __bf16 bfx8 __attribute__((ext_vector_type(8)));
typedef __bf16 bfx16 __attribute__((ext_vector_type(16)));
typedef short s16x4 __attribute__((ext_vector_type(4)));
typedef GAS unsigned gu32;
typedef unsigned short u16;
typedef unsigned char u8;
#define RLX_AGENT __ATOMIC_RELAXED, __HIP_MEMORY_SCOPE_AGENT

namespace {

constexpr int SEQ = 2048, DM = 1024, NH = 32, HD = 4096, NSEG = 64, FF = 3584;
constexpr int NCHAIN = 128, NWORK = 128, NWG = 256, NTHR = 512;
constexpr unsigned SENT = 0xFFFFFFFFu;
constexpr int AUX_SC1 = 16;

constexpr unsigned CTRL_BYTES = 77824;
constexpr int LN = 32;
constexpr int W_TMO = 0, W_TOP = 3 * LN  , W_CFG = 4 * LN  ,
              W_XCC = 5 * LN, W_PROG = 8 * LN  , W_FKV = 64 * LN  , W_FQ = 128 * LN  ,
              W_RG = 160 * LN  , W_M2 = 288 * LN  , W_T2 = 344 * LN  ,
              W_SA = 408 * LN  , W_SP = 472 * LN  , W_M1 = 536 * LN  ;
constexpr unsigned OFF_MEM = 98304;
constexpr unsigned MEM_ST = 32768;
constexpr unsigned OFF_QKV = OFF_MEM + NSEG * MEM_ST;
constexpr unsigned OFF_PART = OFF_QKV + NSEG * NH * 16384;
constexpr unsigned PART_SZ = NCHAIN * 16384;
constexpr unsigned OFF_CAL = OFF_PART + 3 * PART_SZ;
constexpr unsigned OFF_KX = OFF_CAL + 32768;
constexpr unsigned OFF_VX = OFF_KX + NSEG * NH * 16384;
constexpr unsigned OFF_QX = OFF_VX + NSEG * NH * 16384;
constexpr unsigned OFF_AX = OFF_QX + NSEG * NH * 16384;
constexpr unsigned OFF_PS = OFF_AX + NSEG * NH * 16384;
constexpr unsigned OFF_R = OFF_PS + 1024 * 16384 + 16384;
constexpr unsigned OFF_G = OFF_R + SEQ * 4;
constexpr unsigned OFF_HID = OFF_G + SEQ * 4;
constexpr unsigned OFF_YP = OFF_HID + 2 * FF * 4;
constexpr unsigned WS_END = OFF_YP + 14 * 2 * DM * 4;
constexpr unsigned OOB = 0xFFFFFF00u;
constexpr int NBULK = 8;
constexpr int LDS_BYTES = 160 * 1024;
constexpr unsigned SPIN_CAP = 1u << 18;
constexpr int DLY_MEM = 15, DLY_QKV = 8, DLY_QKV3 = 5, DLY_PART = 12;

struct Params {
  const u16 *x, *mem0, *Wq, *Wk, *Wv, *Wo, *wr, *wg, *wu, *wd, *gw;
  u16* out;
  u8* ws;
};

__device__ __forceinline__ float u2f(unsigned u) { return __builtin_bit_cast(float, u); }
__device__ __forceinline__ unsigned f2u(float f) { return __builtin_bit_cast(unsigned, f); }
__device__ __forceinline__ float bf2f(unsigned b) { return u2f(b << 16); }
__device__ __forceinline__ float bflo(unsigned d) { return u2f(d << 16); }
__device__ __forceinline__ float bfhi(unsigned d) { return u2f(d & 0xFFFF0000u); }
__device__ __forceinline__ unsigned pk2(float lo, float hi) {
  typedef __bf16 bfx2 __attribute__((ext_vector_type(2)));
  bfx2 v; v.x = (__bf16)lo; v.y = (__bf16)hi;
  return __builtin_bit_cast(unsigned, v);
}
__device__ __forceinline__ float bfr(float f) { return (float)(__bf16)f; }
__device__ __forceinline__ unsigned canon(unsigned d) { return min(d, 0xFFFFFFFEu); }
__device__ __forceinline__ f32x4 mfma32(v4u a, v4u b, f32x4 c) {
  return __builtin_amdgcn_mfma_f32_16x16x32_bf16(__builtin_bit_cast(bfx8, a), __builtin_bit_cast(bfx8, b), c, 0, 0, 0);
}
__device__ __forceinline__ f32x4 mfma16(v2u a, v2u b, f32x4 c) { return mfma32(v4u{a.x, a.y, 0u, 0u}, v4u{b.x, b.y, 0u, 0u}, c); }
__device__ __forceinline__ f32x4 smf64(v4u a, v8u b, f32x4 c, int idx) {
  return __builtin_amdgcn_smfmac_f32_16x16x64_bf16(__builtin_bit_cast(bfx8, a), __builtin_bit_cast(bfx16, b), c, idx, 0, 0);
}
__device__ __forceinline__ v2u trread(unsigned lds_addr) {
  return __builtin_bit_cast(v2u, __builtin_amdgcn_ds_read_tr16_b64_v4i16((LAS s16x4*)(size_t)lds_addr));
}
typedef float f32x2 __attribute__((ext_vector_type(2)));
__device__ __forceinline__ f32x2 fold32(f32x4 v) {
  const auto r0 = __builtin_amdgcn_permlane32_swap(f2u(v[0]), f2u(v[2]), false, false);
  const auto r1 = __builtin_amdgcn_permlane32_swap(f2u(v[1]), f2u(v[3]), false, false);
  return f32x2{u2f(r0[0]), u2f(r1[0])} + f32x2{u2f(r0[1]), u2f(r1[1])};
}
__device__ __forceinline__ float xmax16(float a) { auto r = __builtin_amdgcn_permlane16_swap(f2u(a), f2u(a), false, false); return __builtin_amdgcn_fmed3f(u2f(r[0]), u2f(r[1]), INFINITY); }
__device__ __forceinline__ float xmax32(float a) { auto r = __builtin_amdgcn_permlane32_swap(f2u(a), f2u(a), false, false); return __builtin_amdgcn_fmed3f(u2f(r[0]), u2f(r[1]), INFINITY); }
__device__ __forceinline__ float xadd16(float a) { auto r = __builtin_amdgcn_permlane16_swap(f2u(a), f2u(a), false, false); return u2f(r[0]) + u2f(r[1]); }
__device__ __forceinline__ float xadd32(float a) { auto r = __builtin_amdgcn_permlane32_swap(f2u(a), f2u(a), false, false); return u2f(r[0]) + u2f(r[1]); }

__device__ __forceinline__ float wave_sum(float v) {
  v += __shfl_xor(v, 1); v += __shfl_xor(v, 2); v += __shfl_xor(v, 4); v += __shfl_xor(v, 8);
  return xadd32(xadd16(v));
}
__device__ __forceinline__ int fresh_tid() { int t = threadIdx.x; asm volatile("" : "+v"(t)); return t; }
#define CFENCE() asm volatile("" ::: "memory")
#define DRAIN_VM() asm volatile("s_waitcnt vmcnt(0)" ::: "memory")
#define WAIT_LGKM() asm volatile("s_waitcnt lgkmcnt(0)" ::: "memory")
__device__ __forceinline__ void wgbar() { WAIT_LGKM(); __builtin_amdgcn_s_barrier(); CFENCE(); }

typedef __amdgpu_buffer_rsrc_t rsrc_t;
__device__ __forceinline__ v4u ld16(rsrc_t rs, unsigned off) { return __builtin_amdgcn_raw_buffer_load_b128(rs, off, 0, AUX_SC1); }
__device__ __forceinline__ v2u ld8(rsrc_t rs, unsigned off) { return __builtin_amdgcn_raw_buffer_load_b64(rs, off, 0, AUX_SC1); }
__device__ __forceinline__ unsigned ld4(rsrc_t rs, unsigned off) { return __builtin_amdgcn_raw_buffer_load_b32(rs, off, 0, AUX_SC1); }
__device__ __forceinline__ void st16(rsrc_t rs, unsigned off, v4u v) { __builtin_amdgcn_raw_buffer_store_b128(v, rs, off, 0, AUX_SC1); }
__device__ __forceinline__ void st8(rsrc_t rs, unsigned off, v2u v) { __builtin_amdgcn_raw_buffer_store_b64(v, rs, off, 0, AUX_SC1); }
__device__ __forceinline__ void st4(rsrc_t rs, unsigned off, unsigned v) { __builtin_amdgcn_raw_buffer_store_b32(v, rs, off, 0, AUX_SC1); }
__device__ __forceinline__ unsigned umax4(v4u v) { return max(max(v.x, v.y), max(v.z, v.w)); }

__device__ __forceinline__ void delay_rt(int n) {
  asm volatile("s_cmp_lt_i32 %0, 1\n\ts_cbranch_scc1 2f\n\ts_mov_b32 vcc_lo, %0\n\t.p2align 5\n1:\n\ts_nop 1\n\ts_sub_i32 vcc_lo, vcc_lo, 1\n\ts_cmp_gt_i32 vcc_lo, 0\n\ts_cbranch_scc1 1b\n2:" :: "s"(n) : "vcc", "scc");
}

__device__ __forceinline__ bool spin_fail(unsigned& spins, gu32* ctrl, unsigned code) {
  ++spins;
  if (spins > SPIN_CAP) { __hip_atomic_store(ctrl + W_TMO, 1u, RLX_AGENT); __hip_atomic_fetch_max(ctrl + W_TMO + 1, code, RLX_AGENT); return true; }
  return (spins & 255u) == 0u && __builtin_amdgcn_readfirstlane(__hip_atomic_load(ctrl + W_TMO, RLX_AGENT)) != 0u;
}
constexpr int RETRY_CAP = 1 << 15;
__device__ __forceinline__ void wait_ge(gu32* ctrl, int word, unsigned want, unsigned code, bool& dead) {
  if (dead) return;
  for (unsigned sp = 0;;) {
    if ((unsigned)__builtin_amdgcn_readfirstlane(__hip_atomic_load(ctrl + word, RLX_AGENT)) >= want) return;
    __builtin_amdgcn_s_sleep(4);
    if (spin_fail(sp, ctrl, code)) { dead = true; return; }
  }
}
__device__ __forceinline__ void wait_flags2(gu32* ctrl, int w0, int w1, unsigned code, bool& dead) {
  if (dead) return;
  for (unsigned sp = 0;;) {
    const unsigned long long a = __hip_atomic_load((GAS unsigned long long*)(ctrl + w0), RLX_AGENT);
    const unsigned long long b = __hip_atomic_load((GAS unsigned long long*)(ctrl + w1), RLX_AGENT);
    const unsigned long long m = a & b;
    if (__builtin_amdgcn_readfirstlane((unsigned)m & (unsigned)(m >> 32)) != 0u) return;
    __builtin_amdgcn_s_sleep(4);
    if (spin_fail(sp, ctrl, code)) { dead = true; return; }
  }
}
__device__ __forceinline__ void wait_lines(gu32* ctrl, int word, int stride, int n, unsigned want, int lane, unsigned code, bool& dead) {
  if (dead) return;
  gu32* mine = ctrl + word + (lane < n ? lane : 0) * stride;
  for (unsigned sp = 0;;) {
    if (__all(__hip_atomic_load(mine, RLX_AGENT) >= want)) return;
    __builtin_amdgcn_s_sleep(2);
    if (spin_fail(sp, ctrl, code)) { dead = true; return; }
  }
}
constexpr int ARM_AHEAD = 4;
__device__ __forceinline__ void arm_share(rsrc_t rs, unsigned al, int c, int tid) {
  const v4u s = {SENT, SENT, SENT, SENT};
  const unsigned lo = (tid & 63) * 16, w = tid >> 6;
#pragma unroll
  for (int sl = 0; sl < 3; ++sl) st16(rs, al + OFF_PART + sl * PART_SZ + c * 16384 + (w >> 2) * 8192 + (w & 3) * 1024 + lo, s);
  if (w < 6) { st16(rs, al + OFF_QKV + c * 16384 + w * 1024 + lo, s); st16(rs, al + OFF_QKV + c * 16384 + 8192 + w * 1024 + lo, s); }
  else if (w == 6) st16(rs, al + OFF_MEM + MEM_ST + c * 1024 + lo, s);
  else if (c < 32) st16(rs, al + OFF_MEM + 5 * MEM_ST + c * 1024 + lo, s);
}
struct Near {
  unsigned al, va21, nz, cd;
  __device__ __forceinline__ unsigned blk(unsigned pair_off, unsigned iod) const { return al + pair_off + ((((va21 + pair_off) >> 21) ^ nz ^ iod) & 1u) * 8192u; }
  __device__ __forceinline__ unsigned hand(unsigned pair_off) const { return blk(pair_off, cd); }
  __device__ __forceinline__ unsigned frag(unsigned reg, int s, int h) const { return blk(reg + (unsigned)(s * NH + h) * 16384u, cd ^ 1u); }
};
__device__ __forceinline__ Near near_init(const u8* ws) {
  Near n; const unsigned lo = (unsigned)(size_t)ws;
  n.al = (0u - lo) & 16383u; n.va21 = lo + n.al; n.nz = 0; n.cd = 0; return n;
}
__device__ __forceinline__ unsigned xcc_id() { return (unsigned)__builtin_amdgcn_s_getreg(20 | (3 << 11)) & 7u; }
__device__ __forceinline__ void near_wait(gu32* ctrl, Near& n, bool& dead) {
  const v4u c0 = *(const GAS v4u*)(ctrl + W_CFG), c1 = *(const GAS v4u*)(ctrl + W_CFG + 4);
  unsigned x = 0;
  for (unsigned sp = 0; !dead;) {
    x = __builtin_amdgcn_readfirstlane(__hip_atomic_load(ctrl + W_XCC, RLX_AGENT));
    if (x >= 16u) break;
    __builtin_amdgcn_s_sleep(4);
    if (spin_fail(sp, ctrl, 0x11u)) dead = true;
  }
  n.cd = (x & 7u) >> 2;
  const int d = (int)(c0.x + c0.y + c0.z + c0.w + c1.x + c1.y + c1.z + c1.w);
  n.nz = __builtin_amdgcn_readfirstlane(((d > 0 ? 1u : 0u) ^ ((n.va21 + OFF_CAL) >> 21)) & 1u);
}

template <bool SC1>
__device__ __forceinline__ void dma16(const void* base, unsigned voff, unsigned lds_addr) {
  unsigned keep;
  if (SC1) asm volatile("s_mov_b32 %0, m0\n\ts_mov_b32 m0, %3\n\ts_nop 0\n\tglobal_load_lds_dwordx4 %1, %2 sc1\n\ts_mov_b32 m0, %0" : "=&s"(keep) : "v"(voff), "s"(base), "s"(lds_addr) : "memory");
  else asm volatile("s_mov_b32 %0, m0\n\ts_mov_b32 m0, %3\n\ts_nop 0\n\tglobal_load_lds_dwordx4 %1, %2\n\ts_mov_b32 m0, %0" : "=&s"(keep) : "v"(voff), "s"(base), "s"(lds_addr) : "memory");
}

template <int N> __device__ __forceinline__ void wait_vm() { static_assert(N >= 0 && N < 64, "vmcnt"); asm volatile("s_waitcnt vmcnt(%0)" :: "i"(N) : "memory"); }
template <int V> struct IC { static constexpr int v = V; };
template <int N, int I = 0, class F> __device__ __forceinline__ void static_for(F&& f) { if constexpr (I < N) { f(IC<I>{}); static_for<N, I + 1>(f); } }
__device__ __forceinline__ void slice_dma(const u16* src, int ld, unsigned dst, int lane) {
  const unsigned vo = ((lane >> 2) * ld + 8 * (lane & 3)) * 2;
#pragma unroll
  for (int p = 0; p < 8; ++p) dma16<false>(src + (size_t)16 * p * ld, vo, dst + p * 1024);
}
template <bool PERM>
__device__ __forceinline__ void slice_frags(unsigned base, int lane, v8u (&f)[2][2]) {
  const int gB = lane >> 4, q = (lane & 15) >> 2, p = lane & 3;
#pragma unroll
  for (int cc = 0; cc < 2; ++cc)
#pragma unroll
    for (int rr = 0; rr < 4; ++rr) {
      const int kp = 64 * cc + 32 * (rr >> 1) + 16 * (gB >> 1) + 4 * (gB & 1) + 2 * (rr & 1) + (q & 1) + 8 * (q >> 1);
      const int row = PERM ? ((kp & ~31) + 16 * (kp & 1) + ((kp & 31) >> 1)) : kp;
#pragma unroll
      for (int tb = 0; tb < 2; ++tb) {
        const v2u v = trread(base + row * 64 + tb * 32 + p * 8);
        f[tb][cc][2 * rr] = v.x; f[tb][cc][2 * rr + 1] = v.y;
      }
    }
}

__device__ __forceinline__ void chain_role(const Params& p, LAS u8* L, rsrc_t rs, int b) {
  const int tid = threadIdx.x, lane = tid & 63, w = __builtin_amdgcn_readfirstlane(tid >> 6);
  const int c = (b & 3) * 32 + (b >> 3);
  const int h = c >> 2, j = c & 3;
  const int r16 = lane & 15, g = lane >> 4;
  gu32* ctrl = (gu32*)p.ws;
  bool dead = false;
  const unsigned lbase = (unsigned)(size_t)L;
  Near nr = near_init(p.ws);
  if (b == 0 && tid == 0) __hip_atomic_store(ctrl + W_XCC, 16u | xcc_id(), RLX_AGENT);

  v8u W1[3][2][2];
  v4u am0, am1;
  v8u WO[2][2];
  {
    const unsigned mine = lbase + w * 16384;
    const size_t wcol = (size_t)h * 128 + 32 * j;
    slice_dma(p.Wk + (size_t)128 * w * HD + wcol, HD, mine, lane);
    slice_dma(p.Wv + (size_t)128 * w * HD + wcol, HD, mine + 8192, lane);
    near_wait(ctrl, nr, dead);
    DRAIN_VM();
    const unsigned m0 = nr.hand(OFF_MEM + (w >> 2) * 16384u) + (w & 3) * 2048 + lane * 16;
    am0 = ld16(rs, m0); am1 = ld16(rs, m0 + 1024);
    slice_frags<true>(mine, lane, W1[1]);
    slice_frags<true>(mine + 8192, lane, W1[2]);
    WAIT_LGKM();
    asm volatile("s_waitcnt vmcnt(0)" : "+v"(am0), "+v"(am1) :: "memory");
    __builtin_amdgcn_sched_barrier(0);
    slice_dma(p.Wq + (size_t)128 * w * HD + wcol, HD, mine, lane);
    slice_dma(p.Wo + (size_t)128 * h * DM + 256 * j + 32 * w, DM, mine + 8192, lane);
    asm volatile("s_waitcnt vmcnt(8)" ::: "memory");
    slice_frags<true>(mine, lane, W1[0]);
    WAIT_LGKM(); __builtin_amdgcn_sched_barrier(0);
  }

  LAS float* RED = (LAS float*)(L + 131072);
  LAS u8* AHF = L + 155648;
  volatile LAS unsigned* FOK = (volatile LAS unsigned*)(L + 162816);
  LAS float* INV = (LAS float*)(L + 162048);
  LAS u8* KVL = L;
  const int sidx = r16 < 8 ? 0x4444 : 0xEEEE;
  const int gg = g & 1, hi = g >> 1, rp = 2 * gg + hi;
  int gone = dead ? 1 : 0;

  auto kv_ready = [&](int s) {
    if ((s & 3) == 0) { bool d = gone != 0; const int blk = s >> 2; wait_flags2(ctrl, W_FKV + blk * 128 + 2 * h, W_FKV + blk * 128 + 64 + 2 * h, 0x20u, d); gone = __builtin_amdgcn_readfirstlane(d ? 1 : 0); CFENCE(); }
  };
  auto arm_addr = [&](int s, int slot, int q, unsigned& a0, unsigned& a1) {
    a0 = s > 0 ? nr.hand(OFF_PART + (unsigned)(slot == 0 ? 2 : slot - 1) * PART_SZ + c * 16384u) + q * 1024 + lane * 16 : OOB;
    const unsigned qn = nr.hand(OFF_QKV + (unsigned)((s + ARM_AHEAD) * NH + h) * 16384u);
    a1 = q == 0 ? qn + j * 1024 + lane * 16
                : lane < 32 ? qn + 4096 + j * 512 + lane * 16
                            : (lane < 48 && s + ARM_AHEAD + 2 < NSEG) ? nr.al + OFF_MEM + (unsigned)(s + ARM_AHEAD + 2) * MEM_ST + (c >> 6) * 16384u + ((lane >> 3) & 1) * 8192u + (c & 63) * 128 + (lane & 7) * 16
                                                                      : OOB;
    a1 = s + ARM_AHEAD < NSEG ? a1 : OOB;
  };
  if (tid == 0) FOK[0] = 0u;
  wgbar();

  auto run = [&](auto role) {
    constexpr int ROLE = decltype(role)::v;
    LAS f32x2* redw = (LAS f32x2*)(RED + w * 768) + (rp * 16 + r16) * 2;
    const int R = 32 * j + 4 * w + (r16 >> 2);
    const unsigned part_w = R * 16384u, part_l = h * 128 + rp * 32 + (r16 & 3) * 8;
    const unsigned pub_l = ROLE == 0 ? j * 1024 + (8 * w + 2 * g + (r16 & 1) + 16 * (r16 >> 2)) * 16 + (r16 & 2) * 4 : 4096 + (32 * j + r16) * 16 + g * 4;
    unsigned qo, a_part, mo, a_pub;
    float pinf = INFINITY, mi = g < 2 ? -INFINITY : 0.f;
    asm volatile("" : "+v"(pinf), "+v"(mi));
    const f32x4 minit = {mi, mi, mi, mi};
    const unsigned s1 = (r16 & 3) == 0 ? 0x00003F80u : (r16 & 3) == 1 ? 0x3F800000u : 0u;
    const v4u rsel = {(r16 & 4) ? 0u : s1, (r16 & 4) ? s1 : 0u, (r16 & 4) ? 0u : s1, (r16 & 4) ? s1 : 0u};
    int slot = 0, lim = gone ? 0 : RETRY_CAP;
    auto prepare = [&](int s, int sl) {
      qo = nr.hand(OFF_QKV + (unsigned)(s * NH + h) * 16384u);
      a_part = nr.hand(OFF_PART + (unsigned)sl * PART_SZ + part_w) + part_l;
      mo = nr.hand(OFF_MEM + (unsigned)s * MEM_ST + (w >> 2) * 16384u) + (w & 3) * 2048 + lane * 16;
      a_pub = qo + pub_l;
      asm volatile("" : "+v"(a_part), "+v"(mo), "+v"(a_pub));
    };
    prepare(0, 0);
    auto step = [&](auto first, int s) -> bool {
      constexpr bool FIRST = decltype(first)::v != 0;
      if (!FIRST) delay_rt(DLY_MEM);
      f32x4 acc[6];
      {
        int tries = 0; bool again;
        do {
          CFENCE();
          const v4u a0 = FIRST ? am0 : ld16(rs, mo), a1 = FIRST ? am1 : ld16(rs, mo + 1024);
#pragma unroll
          for (int t = 0; t < 6; ++t) { acc[t] = f32x4{0.f, 0.f, 0.f, 0.f}; asm volatile("" : "+v"(acc[t])); }
          __builtin_amdgcn_sched_barrier(0);
#pragma unroll
          for (int t = 0; t < 6; ++t) acc[t] = smf64(a0, W1[t >> 1][t & 1][0], acc[t], sidx);
          __builtin_amdgcn_sched_barrier(0);
#pragma unroll
          for (int t = 0; t < 6; ++t) acc[t] = smf64(a1, W1[t >> 1][t & 1][1], acc[t], sidx);
          again = !FIRST && ((__ballot(max(umax4(a0), umax4(a1)) == SENT) != 0ull) & (tries < lim));
          ++tries;
        } while (__builtin_expect(again, 0));
        lim = tries > lim ? 0 : lim;
      }
#pragma unroll
      for (int t = 0; t < 6; ++t) redw[(t >> 1) * 128 + (t & 1)] = fold32(acc[t]);
      wgbar();
      if (ROLE <= 2) {
        const LAS f32x4* redr = (const LAS f32x4*)RED + w * 64 + lane;
        f32x4 t0 = redr[0], t1 = redr[192];
#pragma unroll
        for (int k = 2; k < 8; k += 2) { t0 += redr[k * 192]; t1 += redr[(k + 1) * 192]; }
        t0 += t1;
        if (ROLE == 0) {
          const unsigned d0 = pk2(t0[0], t0[2]), d1 = pk2(t0[1], t0[3]);
          const unsigned got = __builtin_amdgcn_mov_dpp((r16 & 1) ? d0 : d1, 0xB1, 0xF, 0xF, true);
          st8(rs, a_pub, (r16 & 1) ? v2u{canon(got), canon(d1)} : v2u{canon(d0), canon(got)});
        } else {
          st4(rs, a_pub, canon(pk2(t0[0], t0[1])));
          st4(rs, a_pub + 256, canon(pk2(t0[2], t0[3])));
        }
      }
      if (!FIRST && s == NSEG - 1) return false;
      if (FIRST) {
        if (ROLE >= 4) {
          kv_ready(0);
#pragma unroll
          for (int i = 0; i < 4; ++i)
            dma16<true>(p.ws + nr.frag(ROLE >= 6 ? OFF_VX : OFF_KX, 0, h) + (ROLE & 1) * 4096 + i * 1024, lane * 16, lbase + (ROLE >= 6 ? 16384 : 0) + (ROLE & 1) * 4096 + i * 1024);
        }
        DRAIN_VM();
        slice_frags<false>(lbase + w * 16384 + 8192, lane, WO);
        wgbar();
      }
      acc[0] = f32x4{0.f, 0.f, 0.f, 0.f}; acc[1] = acc[0];
      asm volatile("" : "+v"(acc[0]), "+v"(acc[1]));
      if (ROLE <= 3) {
        constexpr int NE = 2;
        v4u KX[2][4], VX[NE], F[4]; v2u VM[NE];
        const LAS u8* kv = KVL + (s & 1) * 32768 + lane * 16;
#pragma unroll
        for (int i = 0; i < 8; ++i) KX[i >> 2][i & 3] = *(const LAS v4u*)(kv + i * 1024);
#pragma unroll
        for (int e = 0; e < NE; ++e) VX[e] = *(const LAS v4u*)(kv + 16384 + (NE * w + e) * 1024);
        const unsigned fo = qo + lane * 16, vo = qo + 4096 + (16 * NE * w + r16) * 16 + gg * 8;
        __builtin_amdgcn_sched_barrier(0);
        delay_rt(ROLE == 3 ? DLY_QKV + DLY_QKV3 : DLY_QKV);
        f32x4 sA, sB, sM;
        {
          int tries = 0; bool again;
          do {
            CFENCE();
#pragma unroll
            for (int u = 0; u < 4; ++u) F[u] = ld16(rs, fo + u * 1024);
#pragma unroll
            for (int e = 0; e < NE; ++e) VM[e] = ld8(rs, vo + e * 256);
            sA = f32x4{0.f, 0.f, 0.f, 0.f}; sB = sA; sM = minit;
#pragma unroll
            for (int u = 0; u < 4; ++u) {
              sM = mfma32(F[u], F[u], sM); sA = mfma32(KX[0][u], F[u], sA); sB = mfma32(KX[1][u], F[u], sB);
            }
            unsigned m = max(max(umax4(F[0]), umax4(F[1])), max(umax4(F[2]), umax4(F[3])));
#pragma unroll
            for (int e = 0; e < NE; ++e) m = max(m, max(VM[e].x, VM[e].y));
            again = (__ballot(m == SENT) != 0ull) & (tries < lim);
            ++tries;
          } while (__builtin_expect(again, 0));
          lim = tries > lim ? 0 : lim;
        }
        FOK[0] = (unsigned)(s + 1);
        const float sc2 = 0.08838834764831845f * 1.4426950408889634f;
        sA *= sc2; sB *= sc2; sM *= sc2;
        float mx = fmaxf(fmaxf(fmaxf(fmaxf(sA[0], sA[1]), sA[2]), fmaxf(fmaxf(sA[3], sB[0]), sB[1])), fmaxf(fmaxf(fmaxf(sB[2], sB[3]), sM[0]), fmaxf(fmaxf(sM[1], sM[2]), sM[3])));
        { auto r = __builtin_amdgcn_permlane16_swap(f2u(mx), f2u(mx), false, false); mx = __builtin_amdgcn_fmed3f(u2f(r[0]), u2f(r[1]), pinf); }
        { auto r = __builtin_amdgcn_permlane32_swap(f2u(mx), f2u(mx), false, false); mx = __builtin_amdgcn_fmed3f(u2f(r[0]), u2f(r[1]), pinf); }
        sA -= mx; sB -= mx; sM -= mx;
#pragma unroll
        for (int r = 0; r < 4; ++r) { sA[r] = __builtin_amdgcn_exp2f(sA[r]); sB[r] = __builtin_amdgcn_exp2f(sB[r]); sM[r] = __builtin_amdgcn_exp2f(sM[r]); }
        const v4u pb = {pk2(sA[0], sA[1]), pk2(sA[2], sA[3]), pk2(sB[0], sB[1]), pk2(sB[2], sB[3])};
        const v2u pm = {pk2(sM[0], sM[1]), pk2(sM[2], sM[3])};
        f32x4 eo[NE];
#pragma unroll
        for (int e = 0; e < NE; ++e) eo[e] = mfma32(VX[e], pb, f32x4{0.f, 0.f, 0.f, 0.f});
#pragma unroll
        for (int e = 0; e < NE; ++e) eo[e] = mfma16(VM[e], pm, eo[e]);
        if (ROLE == 3) {
          const f32x4 ss = sA + sB + sM;
          INV[lane] = __builtin_amdgcn_rcpf(xadd32(xadd16((ss[0] + ss[1]) + (ss[2] + ss[3]))));
        }
        LAS u8* ah = AHF + (r16 < 8 ? (2 * NE * w + hi) * 128 + r16 * 16 + gg * 8 : 4096 + lane * 8);
#pragma unroll
        for (int e = 0; e < NE; ++e) *(LAS v2u*)(ah + e * 256) = v2u{pk2(eo[e][0], eo[e][1]), pk2(eo[e][2], eo[e][3])};
      } else if (ROLE >= 4) {
        constexpr int Q = ROLE - 4;
        kv_ready(s + 1);
        const u8* kb = p.ws + nr.frag((Q >> 1) ? OFF_VX : OFF_KX, s + 1, h) + (Q & 1) * 4096;
        const unsigned dst = lbase + ((s + 1) & 1) * 32768 + (Q >> 1) * 16384 + (Q & 1) * 4096;
        unsigned ar0, ar1;
        arm_addr(s, slot, Q, ar0, ar1);
        asm volatile("" : "+v"(ar0), "+v"(ar1));
        __builtin_amdgcn_sched_barrier(0);
        for (unsigned sp = 0; sp < (1u << 20); ++sp)
          if ((unsigned)__builtin_amdgcn_readfirstlane(FOK[0]) == (unsigned)(s + 1)) break;
#pragma unroll
        for (int i = 0; i < 4; ++i) dma16<true>(kb + i * 1024, lane * 16, dst + i * 1024);
        st16(rs, ar0, v4u{SENT, SENT, SENT, SENT});
        if (Q < 2) st16(rs, ar1, v4u{SENT, SENT, SENT, SENT});
      }
      wgbar();
      {
        const v4u b0 = *(LAS v4u*)(AHF + lane * 16), b1 = *(LAS v4u*)(AHF + 1024 + lane * 16);
        const f32x2 iv = *(LAS f32x2*)(INV + 2 * rp);
        acc[0] = smf64(b0, WO[0][0], acc[0], sidx); acc[1] = smf64(b0, WO[1][0], acc[1], sidx);
        acc[0] = smf64(b1, WO[0][1], acc[0], sidx); acc[1] = smf64(b1, WO[1][1], acc[1], sidx);
        const f32x2 f0 = fold32(acc[0]) * iv, f1 = fold32(acc[1]) * iv;
        st8(rs, a_part, v2u{canon(pk2(f0[0], f1[0])), canon(pk2(f0[1], f1[1]))});
      }
      __builtin_amdgcn_sched_barrier(0);
      const int nslot = slot == 2 ? 0 : slot + 1;
      if (ROLE == 7) {
        unsigned po = nr.hand(OFF_PART + (unsigned)slot * PART_SZ + c * 16384u) + lane * 16;
        unsigned a_mem = nr.hand(OFF_MEM + (unsigned)(s + 1) * MEM_ST + (c >> 6) * 16384u) + (c & 63) * 128 + (2 * (r16 >> 2) + g) * 16 + (r16 & 3) * 4;
        a_mem = lane < 32 ? a_mem : OOB;
        prepare(s + 1, nslot);
        asm volatile("" : "+v"(a_mem), "+v"(po));
        delay_rt(DLY_PART);
        f32x4 t;
        {
          int tries = 0; bool again;
          const unsigned p8 = po - lane * 8;
          do {
            CFENCE();
            v2u pq[8];
#pragma unroll
            for (int i = 0; i < 8; ++i) pq[i] = ld8(rs, p8 + i * 512);
            t = f32x4{0.f, 0.f, 0.f, 0.f};
#pragma unroll
            for (int i = 0; i < 8; i += 2) t = mfma32(rsel, v4u{pq[i].x, pq[i].y, pq[i + 1].x, pq[i + 1].y}, t);
            unsigned m = 0;
#pragma unroll
            for (int i = 0; i < 8; ++i) m = max(m, max(pq[i].x, pq[i].y));
            again = (__ballot(m == SENT) != 0ull) & (tries < lim);
            ++tries;
          } while (__builtin_expect(again, 0));
          lim = tries > lim ? 0 : lim;
        }
        st4(rs, a_mem, canon(pk2(t[0], t[1])));
      } else {
        prepare(s + 1, nslot);
        if (ROLE == 2 && j == 0 && lane == 0) __hip_atomic_store(ctrl + W_PROG + LN * h, (unsigned)(s + 1), RLX_AGENT);
        if (ROLE >= 4) DRAIN_VM();
      }
      slot = nslot;
      wgbar();
      return true;
    };
    step(IC<1>{}, 0);
    for (int s = 1; step(IC<0>{}, s); ++s) {}
    if (lim == 0 && lane == 0) __hip_atomic_store(ctrl + W_TMO, 1u, RLX_AGENT);
  };
  switch (w) {
    case 0: case 1: run(IC<0>{}); break;
    case 2: run(IC<2>{}); break;
    case 3: run(IC<3>{}); break;
    case 4: run(IC<4>{}); break;
    case 5: run(IC<5>{}); break;
    case 6: run(IC<6>{}); break;
    default: run(IC<7>{}); break;
  }
}

enum { M_K = 0, M_V = 1, M_O = 2 };

template <int BN, int MODE, int WM = 2>
__device__ __forceinline__ void gemm_tile(LAS u8* L, const u16* A, int lda, const u16* W, int ldw, int nkt, f32x4 (&acc)[8 / WM][BN * WM / 128], const Near* nr = nullptr) {
  constexpr int MT = 8 / WM, NT = BN * WM / 128, NCG = BN / 64, STG = 16384 + BN * 128, NB = BN / 64;
  constexpr int NSTG = LDS_BYTES / STG;
  const int tid = fresh_tid(), lane = tid & 63, w = __builtin_amdgcn_readfirstlane(tid >> 6);
  const int wm = w / (8 / WM), wn = w % (8 / WM), r16 = lane & 15, g = lane >> 4, q = r16 >> 2, pp = lane & 3;
  const int arow = 16 * w + (lane >> 3);
  const unsigned avo0 = (arow * lda + 8 * ((lane & 7) ^ ((arow >> 1) & 7))) * 2;
  const unsigned avo1 = ((arow + 8) * lda + 8 * ((lane & 7) ^ (((arow + 8) >> 1) & 7))) * 2;
  const int kr = 8 * w + (lane >> 3);
  const int krs = MODE == M_O ? ((kr & ~31) + 16 * ((kr >> 2) & 1) + 4 * ((kr >> 3) & 3) + (kr & 3)) : kr;
  const int fsw = ((kr >> 1) & 1) | (((kr >> 3) & 1) << 1);
  const unsigned wvo = (krs * ldw + 16 * (((lane & 7) >> 1) ^ fsw) + 8 * (lane & 1)) * 2;
  const unsigned lb = (unsigned)(size_t)L;
  auto piece = [&](int kt, int buf, int i) {
    const unsigned sb = lb + buf * STG;
    if (i >= 2) dma16<false>(W + (size_t)kt * 64 * ldw + 64 * (i - 2), wvo, sb + 16384 + (w * NB + i - 2) * 1024);
    else if (MODE == M_O) {
      const u8* ab = (const u8*)A + (unsigned)__builtin_amdgcn_readfirstlane(nr->frag(OFF_AX, lda + (w >> 1), kt >> 1)) + (w & 1) * 4096 + (kt & 1) * 2048;
      dma16<false>(ab + i * 1024, lane * 16, sb + (2 * w + i) * 1024);
    } else dma16<false>(A + kt * 64, i ? avo1 : avo0, sb + (2 * w + i) * 1024);
  };
  const unsigned aoff = lb + (16 * MT * wm + r16) * 128 + ((g ^ ((r16 >> 1) & 7)) * 16);
  const int fr = ((q >> 1) & 1) | ((g & 1) << 1);
  const int cgw = (wn * NT) >> 2, c0 = (wn * NT) & 3;
  unsigned boff[NT];
#pragma unroll
  for (int n = 0; n < NT; ++n) boff[n] = lb + 16384 + (g * NCG + cgw) * 1024 + q * 128 + (((c0 + n) ^ fr) * 32) + pp * 8;
#pragma unroll
  for (int m = 0; m < MT; ++m)
#pragma unroll
    for (int n = 0; n < NT; ++n) acc[m][n] = f32x4{0.f, 0.f, 0.f, 0.f};

  v4u af[2][MT], bf[2][NT];
  auto frag1 = [&](int buf, int ks, int i) {
    const unsigned so = buf * STG;
    if (i < NT) {
      const v2u x0 = trread(boff[i] + so + ks * 4 * NCG * 1024), x1 = trread(boff[i] + so + ks * 4 * NCG * 1024 + 512);
      bf[ks][i] = v4u{x0.x, x0.y, x1.x, x1.y};
    } else {
      const int m = i - NT;
      af[ks][m] = MODE == M_O ? *(LAS v4u*)(size_t)(lb + so + wm * 8192 + lane * 16 + m * 2048 + ks * 1024) : *(LAS v4u*)(size_t)(((aoff + so) ^ (ks * 64)) + m * 2048);
    }
  };
  auto mma = [&](int ks, int fb, bool fill, int kt, int buf) {
    if (fb >= 0) {
#pragma unroll
      for (int i = 0; i < MT + NT; ++i) frag1(fb, ks ^ 1, i);
    }
    __builtin_amdgcn_sched_barrier(0);
#pragma unroll
    for (int i = 0; i < MT * NT; ++i) {
      const int m = i / NT, n = i % NT;
      acc[m][n] = MODE == M_V ? mfma32(af[ks][m], bf[ks][n], acc[m][n]) : mfma32(bf[ks][n], af[ks][m], acc[m][n]);
      constexpr int EV = MT * NT >= 2 * (NB + 2) ? 2 : 1;
      if (fill && i % EV == EV - 1 && i / EV < NB + 2) { __builtin_amdgcn_sched_barrier(0); piece(kt, buf, i / EV); __builtin_amdgcn_sched_barrier(0); }
    }
    __builtin_amdgcn_sched_barrier(0);
  };
  static_assert(MT * NT >= NB + 2 && MT * NT >= MT + NT, "every piece and fragment needs a product to hide behind");
  wgbar();
#pragma unroll
  for (int st = 0; st < NSTG; ++st)
#pragma unroll
    for (int i = 0; i < NB + 2; ++i) piece(st, st, i);
  wait_vm<(NSTG - 1) * (2 + NB)>();
  __builtin_amdgcn_s_barrier(); CFENCE();
#pragma unroll
  for (int i = 0; i < MT + NT; ++i) frag1(0, 0, i);
  int buf = 0;
  for (int kt = 0; kt < nkt - NSTG; ++kt) {
    const int nb = buf == NSTG - 1 ? 0 : buf + 1;
    mma(0, buf, false, 0, 0);
    wait_vm<(NSTG - 2) * (2 + NB)>();
    WAIT_LGKM();
    __builtin_amdgcn_s_barrier(); CFENCE();
    mma(1, nb, true, kt + NSTG, buf);
    buf = nb;
  }
  static_for<NSTG>([&](auto ic) {
    constexpr int R = decltype(ic)::v;
    const int nb = buf == NSTG - 1 ? 0 : buf + 1;
    mma(0, buf, false, 0, 0);
    if (R < NSTG - 1) {
      wait_vm<(NSTG - 2 - R > 0 ? NSTG - 2 - R : 0) * (2 + NB)>();
      __builtin_amdgcn_s_barrier(); CFENCE();
    }
    mma(1, R < NSTG - 1 ? nb : -1, false, 0, 0);
    buf = nb;
  });
}

template <int BN, int MODE, int WM = 2>
__device__ __forceinline__ void proj_task(const Params& p, LAS u8* L, rsrc_t rs, Near& nr, bool& dead, int blk, int mtx, int col0) {
  constexpr int MT = 8 / WM, NT = BN * WM / 128;
  const int tid = fresh_tid(), lane = tid & 63, w = __builtin_amdgcn_readfirstlane(tid >> 6), wm = w / (8 / WM), wn = w % (8 / WM);
  f32x4 acc[MT][NT];
  const u16* W = (mtx == 0 ? p.Wk : mtx == 1 ? p.Wv : p.Wq) + col0;
  gemm_tile<BN, MODE, WM>(L, p.x + (size_t)blk * 128 * DM, DM, W, HD, DM / 64, acc);
  const int cw = col0 + wn * 16 * NT, h = cw >> 7;
  if (BN == 64 && blk == 0) near_wait((gu32*)p.ws, nr, dead);
  const unsigned reg = mtx == 0 ? OFF_KX : mtx == 1 ? OFF_VX : OFF_QX;
  const int s0 = blk * 4 + (MT / 2) * wm;
  if (MODE == M_K) {
#pragma unroll
    for (int m = 0; m < MT; ++m) {
      const unsigned o = nr.frag(reg, s0 + (m >> 1), h) + lane * 16 + ((m & 1) * 4 + ((cw & 127) >> 5)) * 1024;
#pragma unroll
      for (int v = 0; v < NT / 2; ++v)
        st16(rs, o + v * 1024, v4u{pk2(acc[m][2 * v][0], acc[m][2 * v + 1][0]), pk2(acc[m][2 * v][1], acc[m][2 * v + 1][1]),
                                   pk2(acc[m][2 * v][2], acc[m][2 * v + 1][2]), pk2(acc[m][2 * v][3], acc[m][2 * v + 1][3])});
    }
  } else {
#pragma unroll
    for (int sg = 0; sg < MT / 2; ++sg)
#pragma unroll
      for (int n = 0; n < NT; ++n)
        st16(rs, nr.frag(reg, s0 + sg, h) + lane * 16 + (((cw & 127) >> 4) + n) * 1024,
             v4u{pk2(acc[2 * sg][n][0], acc[2 * sg][n][1]), pk2(acc[2 * sg][n][2], acc[2 * sg][n][3]),
                 pk2(acc[2 * sg + 1][n][0], acc[2 * sg + 1][n][1]), pk2(acc[2 * sg + 1][n][2], acc[2 * sg + 1][n][3])});
  }
  DRAIN_VM();
  wgbar();
  gu32* ctrl = (gu32*)p.ws;
  if (tid < BN / 64) {
    const int word = mtx == 2 ? W_FQ + blk * 64 + (col0 >> 6) + tid : W_FKV + blk * 128 + mtx * 64 + (col0 >> 6) + tid;
    __hip_atomic_store(ctrl + word, 1u, RLX_AGENT);
  }
}

template <int MODE>
__device__ __forceinline__ void proj_big(const Params& p, LAS u8* L, rsrc_t rs, const Near& nr, int blk, int mtx, int col0) {
  constexpr int NS = 5, STG = 32768, NK = DM / 32;
  const int tid = fresh_tid();
  const int lane = tid & 63, w = __builtin_amdgcn_readfirstlane(tid >> 6), wm = w >> 2, wn = w & 3, r16 = lane & 15, g = lane >> 4, q = r16 >> 2;
  const u16* A = p.x + (size_t)blk * 128 * DM;
  const u16* W = (mtx == 0 ? p.Wk : mtx == 1 ? p.Wv : p.Wq) + col0;
  const unsigned lb = (unsigned)(size_t)L;
  const int ar = lane >> 2;
  const unsigned avo = ((32 * w + ar) * DM + 8 * ((lane & 3) ^ ((4 - (ar >> 2)) & 3))) * 2;
  const int kr = 8 * (w >> 1) + (lane >> 3);
  const unsigned wvo = (kr * HD + 128 * (w & 1) + 16 * (((lane & 7) >> 1) ^ (((kr >> 1) & 1) | (((kr >> 3) & 1) << 1))) + 8 * (lane & 1)) * 2;
  auto piece = [&](int kt, int buf, int i) {
    const unsigned sb = lb + buf * STG;
    if (i < 2) dma16<false>(A + kt * 32 + i * 16 * DM, avo, sb + (2 * w + i) * 1024);
    else dma16<false>(W + (size_t)kt * 32 * HD + 64 * (i - 2), wvo, sb + 16384 + (2 * w + i - 2) * 1024);
  };
  const unsigned aoff = lb + (128 * wm + r16) * 64 + ((g ^ ((4 - q) & 3)) * 16);
  const int fr = ((q >> 1) & 1) | ((g & 1) << 1);
  const unsigned boff = lb + 16384 + (g * 4 + wn) * 1024 + q * 128 + (lane & 3) * 8;
  f32x4 acc[8][4];
#pragma unroll
  for (int i = 0; i < 32; ++i) acc[i >> 2][i & 3] = f32x4{0.f, 0.f, 0.f, 0.f};
  v4u af[2][8], bf[2][4];
  auto frag1 = [&](int buf, int set, int i) {
    const unsigned so = buf * STG;
    if (i < 4) {
      const v2u x0 = trread(boff + so + ((i ^ fr) * 32)), x1 = trread(boff + so + ((i ^ fr) * 32) + 512);
      bf[set][i] = v4u{x0.x, x0.y, x1.x, x1.y};
    } else af[set][i - 4] = *(LAS v4u*)(size_t)(aoff + so + (i - 4) * 1024);
  };
  auto mma = [&](int set, int nb, bool fill, int kt, int buf) {
#pragma unroll
    for (int i = 0; i < 32; ++i) {
      const int m = i >> 2, n = i & 3;
      acc[m][n] = MODE == M_V ? mfma32(af[set][m], bf[set][n], acc[m][n]) : mfma32(bf[set][n], af[set][m], acc[m][n]);
      if ((i & 1) == 0 && i < 24 && nb >= 0) { __builtin_amdgcn_sched_barrier(0); frag1(nb, set ^ 1, i >> 1); __builtin_amdgcn_sched_barrier(0); }
      if (fill && (i & 7) == 5) { __builtin_amdgcn_sched_barrier(0); piece(kt, buf, i >> 3); __builtin_amdgcn_sched_barrier(0); }
    }
    __builtin_amdgcn_sched_barrier(0);
  };
  wgbar();
#pragma unroll
  for (int st = 0; st < NS; ++st)
#pragma unroll
    for (int i = 0; i < 4; ++i) piece(st, st, i);
  wait_vm<(NS - 1) * 4>();
  __builtin_amdgcn_s_barrier(); CFENCE();
#pragma unroll
  for (int i = 0; i < 12; ++i) frag1(0, 0, i);
  int buf = 0;
  auto step = [&](int kt, int set, auto inflight, bool fill) {
    const int nb = buf == NS - 1 ? 0 : buf + 1;
    wait_vm<decltype(inflight)::v * 4>();
    WAIT_LGKM();
    __builtin_amdgcn_s_barrier(); CFENCE();
    mma(set, nb, fill, kt + NS, buf);
    buf = nb;
  };
  for (int kt = 0; kt < NK - NS - 1; kt += 2) { step(kt, 0, IC<NS - 2>{}, true); step(kt + 1, 1, IC<NS - 2>{}, true); }
  static_assert((NK - NS - 1) % 2 == 0 && NS == 5, "tail written out for these sizes");
  step(NK - 6, 0, IC<3>{}, true);
  step(NK - 5, 1, IC<3>{}, false);
  step(NK - 4, 0, IC<2>{}, false);
  step(NK - 3, 1, IC<1>{}, false);
  step(NK - 2, 0, IC<0>{}, false);
  mma(1, -1, false, 0, 0);

  const int cw = col0 + wn * 64, h = cw >> 7, s0 = blk * 4 + 4 * wm;
  const unsigned reg = mtx == 0 ? OFF_KX : mtx == 1 ? OFF_VX : OFF_QX;
#pragma unroll
  for (int sg = 0; sg < 4; ++sg) {
    const unsigned o = nr.frag(reg, s0 + sg, h) + lane * 16;
    if (MODE == M_K) {
#pragma unroll
      for (int i = 0; i < 4; ++i) {
        const int m = 2 * sg + (i >> 1), v = i & 1;
        st16(rs, o + ((i >> 1) * 4 + ((cw & 127) >> 5) + v) * 1024,
             v4u{pk2(acc[m][2 * v][0], acc[m][2 * v + 1][0]), pk2(acc[m][2 * v][1], acc[m][2 * v + 1][1]),
                 pk2(acc[m][2 * v][2], acc[m][2 * v + 1][2]), pk2(acc[m][2 * v][3], acc[m][2 * v + 1][3])});
      }
    } else {
#pragma unroll
      for (int n = 0; n < 4; ++n)
        st16(rs, o + (((cw & 127) >> 4) + n) * 1024,
             v4u{pk2(acc[2 * sg][n][0], acc[2 * sg][n][1]), pk2(acc[2 * sg][n][2], acc[2 * sg][n][3]),
                 pk2(acc[2 * sg + 1][n][0], acc[2 * sg + 1][n][1]), pk2(acc[2 * sg + 1][n][2], acc[2 * sg + 1][n][3])});
    }
  }
  DRAIN_VM();
  wgbar();
  if (tid < 8) {
    const int b2 = blk + (tid >> 2);
    const int word = (mtx == 2 ? W_FQ + b2 * 64 : W_FKV + b2 * 128 + mtx * 64) + (col0 >> 6) + (tid & 3);
    __hip_atomic_store((gu32*)p.ws + word, 1u, RLX_AGENT);
  }
}

__device__ __forceinline__ float sigmoidf_(float v) { return 1.0f / (1.0f + __expf(-v)); }

__device__ __forceinline__ void rg_task(const Params& p, rsrc_t rs, int wk) {
  const int tid = fresh_tid(), lane = tid & 63, w = __builtin_amdgcn_readfirstlane(tid >> 6);
  v4u wr[2], gw[2];
#pragma unroll
  for (int i = 0; i < 2; ++i) { wr[i] = *(const GAS v4u*)(p.wr + i * 512 + lane * 8); gw[i] = *(const GAS v4u*)(p.gw + i * 512 + lane * 8); }
#pragma unroll
  for (int t = 0; t < 2; ++t) {
    const int tok = wk * 16 + 2 * w + t;
    float r = 0.f, gs = 0.f;
#pragma unroll
    for (int i = 0; i < 2; ++i) {
      const v4u xv = *(const GAS v4u*)(p.x + (size_t)tok * DM + i * 512 + lane * 8);
#pragma unroll
      for (int d = 0; d < 4; ++d) {
        r = fmaf(bflo(xv[d]), bflo(wr[i][d]), r); r = fmaf(bfhi(xv[d]), bfhi(wr[i][d]), r);
        gs += bfr(bflo(xv[d]) * bflo(gw[i][d])); gs += bfr(bfhi(xv[d]) * bfhi(gw[i][d]));
      }
    }
    r = bfr(wave_sum(r)); gs = bfr(wave_sum(gs));
    if (lane == 0) { st4(rs, OFF_R + tok * 4, f2u(r)); st4(rs, OFF_G + tok * 4, f2u(bfr(sigmoidf_(gs)))); }
  }
  DRAIN_VM();
  wgbar();
  if (tid == 0) __hip_atomic_store((gu32*)p.ws + W_RG + wk * LN, 1u, RLX_AGENT);
}

__device__ __forceinline__ unsigned long long top_key(unsigned bits, int idx) {
  const unsigned o = (bits & 0x7FFFFFFFu) > 0x7F800000u ? 0u : ((bits & 0x80000000u) ? ~bits : (bits | 0x80000000u));
  return ((unsigned long long)o << 32) | (unsigned)(0x7FFFFFFF - idx);
}
__device__ __forceinline__ void top_ins(unsigned long long k, unsigned long long& a, unsigned long long& b) {
  const unsigned long long lo = k < a ? k : a;
  a = k < a ? a : k;
  b = lo > b ? lo : b;
}
__device__ __forceinline__ void top2(rsrc_t rs, LAS u8* L, int& i0, int& i1) {
  const int tid = fresh_tid(), lane = tid & 63, w = tid >> 6;
  const v4u rv = ld16(rs, OFF_R + tid * 16);
  unsigned long long a = 0ull, b = 0ull;
#pragma unroll
  for (int d = 0; d < 4; ++d) top_ins(top_key(rv[d], tid * 4 + d), a, b);
#pragma unroll
  for (int o = 1; o < 64; o <<= 1) {
    const unsigned long long c0 = __shfl_xor(a, o), c1 = __shfl_xor(b, o);
    top_ins(c0, a, b); top_ins(c1, a, b);
  }
  LAS unsigned long long* tv = (LAS unsigned long long*)L;
  wgbar();
  if (lane == 0) { tv[2 * w] = a; tv[2 * w + 1] = b; }
  wgbar();
  a = b = 0ull;
#pragma unroll
  for (int k = 0; k < 16; ++k) top_ins(tv[k], a, b);
  wgbar();
  i0 = (0x7FFFFFFF - (int)(unsigned)a) & 2047; i1 = (0x7FFFFFFF - (int)(unsigned)b) & 2047;
}

__device__ __forceinline__ void mlp1_task(const Params& p, LAS u8* L, rsrc_t rs, int t, bool& dead) {
  const int tid = fresh_tid();
  gu32* ctrl = (gu32*)p.ws;
  {
    const int rg = tid >> 3, cq = tid & 7;
    v4u gv[16], uv[16];
#pragma unroll
    for (int i = 0; i < 16; ++i) {
      gv[i] = *(const GAS v4u*)(p.wg + (size_t)(rg + 64 * i) * FF + 64 * t + 8 * cq);
      uv[i] = *(const GAS v4u*)(p.wu + (size_t)(rg + 64 * i) * FF + 64 * t + 8 * cq);
    }
    __builtin_amdgcn_sched_barrier(0);
    if (tid < 128) wait_lines(ctrl, W_RG + (tid >> 6) * 64 * LN, LN, 64, 1u, tid & 63, 0x40u, dead);
    wgbar();
    int i0, i1;
    top2(rs, L, i0, i1);
    if (t == 0 && tid == 0) {
      const float r0 = u2f(ld4(rs, OFF_R + i0 * 4)), r1 = u2f(ld4(rs, OFF_R + i1 * 4));
      st16(rs, W_TOP * 4, v4u{(unsigned)i0, (unsigned)i1, f2u(bfr(sigmoidf_(r0))), f2u(bfr(sigmoidf_(r1)))});
    }
    LAS float* X = (LAS float*)(L + 69632);
#pragma unroll
    for (int k = 0; k < 4; ++k) X[tid + 512 * k] = bf2f(p.x[(size_t)(k < 2 ? i0 : i1) * DM + tid + 512 * (k & 1)]);
    wgbar();
    float ag[2][8], au[2][8];
#pragma unroll
    for (int e = 0; e < 8; ++e) { ag[0][e] = ag[1][e] = au[0][e] = au[1][e] = 0.f; }
#pragma unroll
    for (int i = 0; i < 16; ++i) {
      const float x0 = X[rg + 64 * i], x1 = X[1024 + rg + 64 * i];
#pragma unroll
      for (int d = 0; d < 4; ++d) {
        ag[0][2 * d] = fmaf(x0, bflo(gv[i][d]), ag[0][2 * d]); ag[0][2 * d + 1] = fmaf(x0, bfhi(gv[i][d]), ag[0][2 * d + 1]);
        ag[1][2 * d] = fmaf(x1, bflo(gv[i][d]), ag[1][2 * d]); ag[1][2 * d + 1] = fmaf(x1, bfhi(gv[i][d]), ag[1][2 * d + 1]);
        au[0][2 * d] = fmaf(x0, bflo(uv[i][d]), au[0][2 * d]); au[0][2 * d + 1] = fmaf(x0, bfhi(uv[i][d]), au[0][2 * d + 1]);
        au[1][2 * d] = fmaf(x1, bflo(uv[i][d]), au[1][2 * d]); au[1][2 * d + 1] = fmaf(x1, bfhi(uv[i][d]), au[1][2 * d + 1]);
      }
    }
    LAS float* R = (LAS float*)L;
#pragma unroll
    for (int e = 0; e < 8; ++e) {
      R[(0 * 64 + rg) * 65 + 8 * cq + e] = ag[0][e]; R[(1 * 64 + rg) * 65 + 8 * cq + e] = ag[1][e];
      R[(2 * 64 + rg) * 65 + 8 * cq + e] = au[0][e]; R[(3 * 64 + rg) * 65 + 8 * cq + e] = au[1][e];
    }
  }
  wgbar();
  if (tid < 128) {
    const LAS float* R = (const LAS float*)L;
    const int tok = tid >> 6, col = tid & 63;
    float gs = 0.f, us = 0.f;
#pragma unroll 16
    for (int r = 0; r < 64; ++r) { gs += R[(tok * 64 + r) * 65 + col]; us += R[((2 + tok) * 64 + r) * 65 + col]; }
    gs = bfr(gs); us = bfr(us);
    const float act = bfr(gs * sigmoidf_(gs));
    st4(rs, OFF_HID + (tok * FF + 64 * t + col) * 4, f2u(bfr(act * us)));
  }
  DRAIN_VM();
  wgbar();
  if (tid == 0) __hip_atomic_store(ctrl + W_M1 + t * LN, 1u, RLX_AGENT);
}
__device__ __forceinline__ void mlp2_task(const Params& p, LAS u8* L, rsrc_t rs, int t, bool& dead) {
  const int tid = fresh_tid();
  gu32* ctrl = (gu32*)p.ws;
  const int kc = t >> 2, cb = t & 3, rg = tid >> 5, cq = tid & 31;
  v4u dv[16];
#pragma unroll
  for (int i = 0; i < 16; ++i) {
    dv[i] = *(const GAS v4u*)(p.wd + (size_t)(256 * kc + rg + 16 * i) * DM + 256 * cb + 8 * cq);
  }
  __builtin_amdgcn_sched_barrier(0);
  if (tid < 64) wait_lines(ctrl, W_M1 + 4 * kc * LN, LN, 4, 1u, tid, 0x41u, dead);
  wgbar();
  LAS float* Hh = (LAS float*)(L + 69632);
  Hh[tid] = u2f(ld4(rs, OFF_HID + ((tid >> 8) * FF + 256 * kc + (tid & 255)) * 4));
  wgbar();
  float a[2][8];
#pragma unroll
  for (int e = 0; e < 8; ++e) a[0][e] = a[1][e] = 0.f;
#pragma unroll
  for (int i = 0; i < 16; ++i) {
    const float h0 = Hh[rg + 16 * i], h1 = Hh[256 + rg + 16 * i];
#pragma unroll
    for (int d = 0; d < 4; ++d) {
      a[0][2 * d] = fmaf(h0, bflo(dv[i][d]), a[0][2 * d]); a[0][2 * d + 1] = fmaf(h0, bfhi(dv[i][d]), a[0][2 * d + 1]);
      a[1][2 * d] = fmaf(h1, bflo(dv[i][d]), a[1][2 * d]); a[1][2 * d + 1] = fmaf(h1, bfhi(dv[i][d]), a[1][2 * d + 1]);
    }
  }
  LAS float* R2 = (LAS float*)L;
#pragma unroll
  for (int e = 0; e < 8; ++e) { R2[(0 * 16 + rg) * 257 + 8 * cq + e] = a[0][e]; R2[(1 * 16 + rg) * 257 + 8 * cq + e] = a[1][e]; }
  wgbar();
  {
    const int tok = tid >> 8, col = tid & 255;
    float sacc = 0.f;
#pragma unroll
    for (int r = 0; r < 16; ++r) sacc += R2[(tok * 16 + r) * 257 + col];
    st4(rs, OFF_YP + ((kc * 2 + tok) * DM + 256 * cb + col) * 4, f2u(sacc));
  }
  DRAIN_VM();
  wgbar();
  if (tid == 0) __hip_atomic_store(ctrl + W_M2 + t * LN, 1u, RLX_AGENT);
}

__device__ __forceinline__ void seg_softmax(const v4u (&F)[4], const v4u (&KX)[2][4], const v4u (&QX)[4], int tq, int g, v4u& pb, v2u& pm) {
  const float scale = 0.08838834764831845f, l2e = 1.4426950408889634f, neg = bfr(-1000000000.0f);
  f32x4 sA = {0.f, 0.f, 0.f, 0.f}, sB = sA, sM = sA;
#pragma unroll
  for (int u = 0; u < 4; ++u) { sM = mfma32(F[u], QX[u], sM); sA = mfma32(KX[0][u], QX[u], sA); sB = mfma32(KX[1][u], QX[u], sB); }
#pragma unroll
  for (int r = 0; r < 4; ++r) {
    sA[r] = 4 * g + r > tq ? neg : bfr(bfr(sA[r]) * scale);
    sB[r] = 16 + 4 * g + r > tq ? neg : bfr(bfr(sB[r]) * scale);
    sM[r] = g < 2 ? -INFINITY : bfr(bfr(sM[r]) * scale);
  }
  float mx = fmaxf(fmaxf(fmaxf(sA[0], sA[1]), fmaxf(sA[2], sA[3])), fmaxf(fmaxf(sB[0], sB[1]), fmaxf(sB[2], sB[3])));
  mx = fmaxf(mx, fmaxf(fmaxf(sM[0], sM[1]), fmaxf(sM[2], sM[3])));
  mx = xmax32(xmax16(mx));
  float sum = 0.f;
#pragma unroll
  for (int r = 0; r < 4; ++r) {
    sA[r] = __builtin_amdgcn_exp2f((sA[r] - mx) * l2e); sB[r] = __builtin_amdgcn_exp2f((sB[r] - mx) * l2e); sM[r] = __builtin_amdgcn_exp2f((sM[r] - mx) * l2e);
    sum += sA[r] + sB[r] + sM[r];
  }
  const float inv = 1.0f / xadd32(xadd16(sum));
  pb = v4u{pk2(sA[0] * inv, sA[1] * inv), pk2(sA[2] * inv, sA[3] * inv), pk2(sB[0] * inv, sB[1] * inv), pk2(sB[2] * inv, sB[3] * inv)};
  pm = v2u{pk2(sM[0] * inv, sM[1] * inv), pk2(sM[2] * inv, sM[3] * inv)};
}

__device__ __forceinline__ void attn_task(const Params& p, rsrc_t rs, const Near& nr, int s, int h, bool& dead) {
  const int lane = fresh_tid() & 63, r16 = lane & 15, g = lane >> 4, gg = g & 1;
  gu32* ctrl = (gu32*)p.ws;
  const int blk = s >> 2;
  wait_flags2(ctrl, W_FKV + blk * 128 + 2 * h, W_FKV + blk * 128 + 64 + 2 * h, 0x50u, dead);
  wait_flags2(ctrl, W_FQ + blk * 64 + 2 * h, W_FQ + blk * 64 + 2 * h, 0x51u, dead);
  CFENCE();
  const unsigned fk = nr.frag(OFF_KX, s, h) + lane * 16, fq = nr.frag(OFF_QX, s, h) + lane * 16, fv = nr.frag(OFF_VX, s, h) + lane * 16;
  v4u KX[2][4], QX[2][4], VX[8], F[4]; v2u VM[8];
#pragma unroll
  for (int i = 0; i < 8; ++i) { KX[i >> 2][i & 3] = ld16(rs, fk + i * 1024); QX[i >> 2][i & 3] = ld16(rs, fq + i * 1024); VX[i] = ld16(rs, fv + i * 1024); }
  wait_ge(ctrl, W_PROG + LN * h, (unsigned)(s + 1), 0x52u, dead);
  const unsigned qo = nr.hand(OFF_QKV + (unsigned)(s * NH + h) * 16384u);
  for (unsigned sp = 0;;) {
    CFENCE();
    unsigned m = 0;
#pragma unroll
    for (int u = 0; u < 4; ++u) { F[u] = ld16(rs, qo + u * 1024 + lane * 16); m = max(m, umax4(F[u])); }
#pragma unroll
    for (int e = 0; e < 8; ++e) { VM[e] = ld8(rs, qo + 4096 + (16 * e + r16) * 16 + gg * 8); m = max(m, max(VM[e].x, VM[e].y)); }
    if (__all(m != SENT) || dead) break;
    if (spin_fail(sp, ctrl, 0x53u)) { dead = true; break; }
  }
#pragma unroll
  for (int qt = 0; qt < 2; ++qt) {
    v4u pb; v2u pm;
    seg_softmax(F, KX, QX[qt], 16 * qt + r16, g, pb, pm);
    const unsigned ao = nr.frag(OFF_AX, s, h) + lane * 16 + qt * 4096;
#pragma unroll
    for (int v = 0; v < 4; ++v) {
      f32x4 o0 = mfma32(VX[2 * v], pb, f32x4{0.f, 0.f, 0.f, 0.f}), o1 = mfma32(VX[2 * v + 1], pb, f32x4{0.f, 0.f, 0.f, 0.f});
      o0 = mfma16(VM[2 * v], pm, o0); o1 = mfma16(VM[2 * v + 1], pm, o1);
      st16(rs, ao + v * 1024, v4u{pk2(o0[0], o0[1]), pk2(o0[2], o0[3]), pk2(o1[0], o1[1]), pk2(o1[2], o1[3])});
    }
  }
  DRAIN_VM();
}

__device__ __forceinline__ void mix_store(const Params& p, rsrc_t rs, const v4u& top, int tok, int col, f32x4 a, float gt, v2u xv) {
  const float og = bfr(1.0f - gt);
  const int sel = tok == (int)top.x ? 0 : tok == (int)top.y ? 1 : -1;
  float bv[4] = {bflo(xv.x), bfhi(xv.x), bflo(xv.y), bfhi(xv.y)};
  if (sel >= 0) {
    f32x4 y = {0.f, 0.f, 0.f, 0.f};
    for (int kc = 0; kc < 14; ++kc) y += __builtin_bit_cast(f32x4, ld16(rs, OFF_YP + ((kc * 2 + sel) * DM + col) * 4));
    const float rsel = u2f(sel == 0 ? top.z : top.w);
#pragma unroll
    for (int r = 0; r < 4; ++r) bv[r] = bfr(bv[r] + bfr(bfr(y[r]) * rsel));
  }
  float o[4];
#pragma unroll
  for (int r = 0; r < 4; ++r) o[r] = bfr(gt * bfr(a[r])) + bfr(og * bv[r]);
  *(GAS v2u*)(p.out + (size_t)tok * DM + col) = v2u{pk2(o[0], o[1]), pk2(o[2], o[3])};
}

__device__ __forceinline__ void oproj_task(const Params& p, LAS u8* L, rsrc_t rs, const Near& nr, int blk, int ct, bool& dead) {
  const int tid = fresh_tid(), lane = tid & 63, w = __builtin_amdgcn_readfirstlane(tid >> 6), wm = w >> 2, wn = w & 3, r16 = lane & 15, g = lane >> 4;
  gu32* ctrl = (gu32*)p.ws;
  if (w == 0) {
    wait_lines(ctrl, W_M2, LN, 56, 1u, lane, 0x60u, dead);
    wait_lines(ctrl, W_T2 + 8 * blk * LN, LN, 8, 1u, lane, 0x61u, dead);
    __builtin_amdgcn_fence(__ATOMIC_ACQUIRE, "agent");
    DRAIN_VM();
  }
  f32x4 acc[4][2];
  gemm_tile<128, M_O>(L, (const u16*)p.ws, blk * 4, p.Wo + ct * 128, DM, HD / 64, acc, &nr);
  const v4u top = ld16(rs, W_TOP * 4);
#pragma unroll
  for (int m = 0; m < 4; ++m)
#pragma unroll
    for (int n = 0; n < 2; ++n) {
      const int tok = blk * 128 + 64 * wm + 16 * m + r16, col = ct * 128 + 32 * wn + 16 * n + 4 * g;
      mix_store(p, rs, top, tok, col, acc[m][n], u2f(ld4(rs, OFF_G + tok * 4)), *(const GAS v2u*)(p.x + (size_t)tok * DM + col));
    }
}

__device__ __forceinline__ void stream_role(const Params& p, LAS u8* L, rsrc_t rs, const Near& nr, int set, int q, bool& dead) {
  const int tid = fresh_tid(), lane = tid & 63, w = __builtin_amdgcn_readfirstlane(tid >> 6), r16 = lane & 15, g = lane >> 4, gg = g & 1;
  const int ks = q >> 3, ct = q & 7, np = w & 3, kh = w >> 2;
  gu32* ctrl = (gu32*)p.ws;
  const unsigned lb = (unsigned)(size_t)L;
  v4u Wf[16][2];
  LAS u8* Wl = L + 65536 + w * 8192 + lane * 16;
  {
    const int kr = 8 * w + (lane >> 3);
    const int krs = (kr & ~31) + 16 * ((kr >> 2) & 1) + 4 * ((kr >> 3) & 3) + (kr & 3);
    const int fsw = ((kr >> 1) & 1) | (((kr >> 3) & 1) << 1);
    const unsigned wvo = (krs * DM + 16 * (((lane & 7) >> 1) ^ fsw) + 8 * (lane & 1)) * 2;
    const int qq = r16 >> 2, fr = ((qq >> 1) & 1) | ((g & 1) << 1);
    const unsigned boff = lb + kh * 65536 + (g * 2 + (np >> 1)) * 1024 + qq * 128 + (lane & 3) * 8;
    const unsigned b0 = boff + (((2 * (np & 1)) ^ fr) * 32), b1 = boff + (((2 * (np & 1) + 1) ^ fr) * 32);
    static_for<2>([&](auto rc) {
      constexpr int RD = decltype(rc)::v;
      wgbar();
#pragma unroll
      for (int kt = 0; kt < 8; ++kt) {
        const u16* wb = p.Wo + (size_t)(1024 * ks + 512 * (kt >> 2) + 64 * (4 * RD + (kt & 3))) * DM + 128 * ct;
        dma16<false>(wb, wvo, lb + kt * 16384 + (w * 2) * 1024);
        dma16<false>(wb + 64, wvo, lb + kt * 16384 + (w * 2 + 1) * 1024);
      }
      DRAIN_VM();
      wgbar();
#pragma unroll
      for (int i = 0; i < 8; ++i) {
        const unsigned o = (i >> 1) * 16384 + (i & 1) * 8192;
        const v2u x0 = trread(b0 + o), x1 = trread(b0 + o + 512), y0 = trread(b1 + o), y1 = trread(b1 + o + 512);
        Wf[8 * RD + i][0] = v4u{x0.x, x0.y, x1.x, x1.y}; Wf[8 * RD + i][1] = v4u{y0.x, y0.y, y1.x, y1.y};
      }
    });
  }
  wgbar();
#pragma unroll
  for (int i = 0; i < 8; ++i) *(LAS v4u*)(Wl + i * 1024) = Wf[12 + (i >> 1)][i & 1];
  wait_lines(ctrl, W_M2, LN, 56, 1u, lane, 0x70u, dead);
  CFENCE();
  const v4u top = ld16(rs, W_TOP * 4);

  auto run = [&](auto role) {
    constexpr bool ATT = decltype(role)::v == 0;
    const int qt = w & 1, eh = w >> 1;
    v4u KX[2][4], QX[4], VX[4];
    auto prefetch = [&](int s) {
      if ((s & 3) < 2) {
        const int kind = (lane >> 1) % 3;
        gu32* f = ctrl + (kind == 2 ? W_FQ + (s >> 2) * 64 : W_FKV + (s >> 2) * 128 + kind * 64) + 2 * q + (lane & 1);
        for (unsigned sp = 0; !dead;) {
          if (__all(__hip_atomic_load(f, RLX_AGENT) != 0u)) break;
          __builtin_amdgcn_s_sleep(8);
          if (spin_fail(sp, ctrl, 0x73u)) dead = true;
        }
        CFENCE();
      }
      const unsigned fk = nr.frag(OFF_KX, s, q) + lane * 16, fq = nr.frag(OFF_QX, s, q) + lane * 16, fv = nr.frag(OFF_VX, s, q) + lane * 16;
#pragma unroll
      for (int i = 0; i < 8; ++i) KX[i >> 2][i & 3] = ld16(rs, fk + i * 1024);
#pragma unroll
      for (int i = 0; i < 4; ++i) { QX[i] = ld16(rs, fq + qt * 4096 + i * 1024); VX[i] = ld16(rs, fv + (4 * eh + i) * 1024); }
    };
    auto finish = [&](int s) {
      const int f = 4 * ks + (w & 3);
      wait_lines(ctrl, W_SP + (set * 32 + ct) * LN, 8 * LN, 4, (unsigned)(s + 1), lane, 0x72u, dead);
      CFENCE();
      f32x4 sum = {0.f, 0.f, 0.f, 0.f};
#pragma unroll
      for (int k = 0; k < 8; ++k) sum += __builtin_bit_cast(f32x4, ld16(rs, nr.frag(OFF_PS, 0, (((s & 7) * 8 + ct) * 8 + k) * 2 + (f >> 3)) + (f & 7) * 1024 + lane * 16));
      { const int tok = s * 32 + 16 * (f & 1) + r16, col = ct * 128 + 16 * (f >> 1) + 4 * g;
        mix_store(p, rs, top, tok, col, sum, u2f(ld4(rs, OFF_G + tok * 4)), *(const GAS v2u*)(p.x + (size_t)tok * DM + col)); }
    };
    unsigned prog = 0;
    if (ATT) prefetch(NBULK * 4 + set);
    for (int s = NBULK * 4 + set; s < NSEG; s += 2) {
      const int si = s - NBULK * 4;
      if (ATT) {
        const unsigned need = (unsigned)(s + 1 < NSEG - 1 ? s + 1 : NSEG - 1);
        if (prog < need) {
          for (unsigned sp = 0; !dead;) {
            prog = __builtin_amdgcn_readfirstlane(__hip_atomic_load(ctrl + W_PROG + LN * q, RLX_AGENT));
            if (prog >= need) break;
            __builtin_amdgcn_s_sleep(2);
            if (spin_fail(sp, ctrl, 0x52u)) dead = true;
          }
        }
        const unsigned qo = nr.hand(OFF_QKV + (unsigned)(s * NH + q) * 16384u);
        v4u F[4]; v2u VM[4];
        for (unsigned sp = 0;;) {
          CFENCE();
          unsigned m = 0;
#pragma unroll
          for (int u = 0; u < 4; ++u) { F[u] = ld16(rs, qo + u * 1024 + lane * 16); m = max(m, umax4(F[u])); }
#pragma unroll
          for (int e = 0; e < 4; ++e) { VM[e] = ld8(rs, qo + 4096 + (16 * (4 * eh + e) + r16) * 16 + gg * 8); m = max(m, max(VM[e].x, VM[e].y)); }
          if (__all(m != SENT) || dead) break;
          if (spin_fail(sp, ctrl, 0x53u)) { dead = true; break; }
        }
        v4u pb; v2u pm;
        seg_softmax(F, KX, QX, 16 * qt + r16, g, pb, pm);
        const unsigned ao = nr.frag(OFF_AX, s, q) + qt * 4096 + eh * 2048 + lane * 16;
#pragma unroll
        for (int v = 0; v < 2; ++v) {
          f32x4 o0 = mfma32(VX[2 * v], pb, f32x4{0.f, 0.f, 0.f, 0.f}), o1 = mfma32(VX[2 * v + 1], pb, f32x4{0.f, 0.f, 0.f, 0.f});
          o0 = mfma16(VM[2 * v], pm, o0); o1 = mfma16(VM[2 * v + 1], pm, o1);
          st16(rs, ao + v * 1024, v4u{pk2(o0[0], o0[1]), pk2(o0[2], o0[3]), pk2(o1[0], o1[1]), pk2(o1[2], o1[3])});
        }
        DRAIN_VM();
      } else {
        if (si >= 4) finish(s - 4);
        DRAIN_VM();
      }
      wgbar();
      if (tid == 0) __hip_atomic_store(ctrl + W_SA + (set * 32 + q) * LN, (unsigned)(s + 1), RLX_AGENT);
      if (tid == 64 && si >= 2) __hip_atomic_store(ctrl + W_SP + (set * 32 + q) * LN, (unsigned)(s - 1), RLX_AGENT);
      if (w == 4) wait_lines(ctrl, W_SA + (set * 32 + ks * 8) * LN, LN, 8, (unsigned)(s + 1), lane, 0x71u, dead);
      wgbar();
      {
        const unsigned ao = nr.frag(OFF_AX, s, 8 * ks + w) + lane * 16;
        v4u t[8];
#pragma unroll
        for (int i = 0; i < 8; ++i) t[i] = ld16(rs, ao + i * 1024);
#pragma unroll
        for (int i = 0; i < 8; ++i) *(LAS v4u*)(L + w * 8192 + i * 1024 + lane * 16) = t[i];
      }
      if (ATT && s + 2 < NSEG) prefetch(s + 2);
      wgbar();
      f32x4 a[2][2];
#pragma unroll
      for (int i = 0; i < 4; ++i) a[i >> 1][i & 1] = f32x4{0.f, 0.f, 0.f, 0.f};
#pragma unroll
      for (int k = 0; k < 16; ++k) {
        const LAS u8* fp = L + kh * 32768 + (k >> 2) * 8192 + (k & 3) * 1024 + lane * 16;
        const v4u x0 = *(const LAS v4u*)fp, x1 = *(const LAS v4u*)(fp + 4096);
#pragma unroll
        for (int n = 0; n < 2; ++n) {
          const v4u wf = k < 12 ? Wf[k][n] : *(const LAS v4u*)(Wl + ((k - 12) * 2 + n) * 1024);
          a[n][0] = mfma32(wf, x0, a[n][0]); a[n][1] = mfma32(wf, x1, a[n][1]);
        }
      }
      const unsigned pb = nr.frag(OFF_PS, 0, (((s & 7) * 8 + ct) * 8 + 2 * ks + kh) * 2 + (np >> 1)) + 4 * (np & 1) * 1024 + lane * 16;
#pragma unroll
      for (int i = 0; i < 4; ++i) st16(rs, pb + i * 1024, __builtin_bit_cast(v4u, a[i >> 1][i & 1]));
    }
    DRAIN_VM();
    wgbar();
    if (tid == 0) __hip_atomic_store(ctrl + W_SP + (set * 32 + q) * LN, (unsigned)(NSEG - 1 + set), RLX_AGENT);
    finish(ATT ? NSEG - 4 + set : NSEG - 2 + set);
  };
  if (w < 4) run(IC<0>{}); else run(IC<1>{});
}

__device__ __forceinline__ void worker_role(const Params& p, LAS u8* L, rsrc_t rs, int b) {
  const int w = __builtin_amdgcn_readfirstlane(threadIdx.x >> 6);
  const int x8 = 2 * (b & 3) + (b >> 7), r = (b >> 3) & 15, wk = r * 8 + x8;
  gu32* ctrl = (gu32*)p.ws;
  bool dead = false;
  Near nr = near_init(p.ws);
  {
    const int cg = x8 * 16 + r;
    for (int blk = 0; blk < 4; ++blk) {
      if (cg < 64) proj_task<64, M_K, 4>(p, L, rs, nr, dead, blk, 0, cg * 64); else proj_task<64, M_V, 4>(p, L, rs, nr, dead, blk, 1, (cg - 64) * 64);
    }
  }
  const int ct4 = x8 * 4 + (r & 3);
  if (ct4 < 16) proj_task<256, M_K>(p, L, rs, nr, dead, 4 + (r >> 2), 0, ct4 * 256); else proj_task<256, M_V>(p, L, rs, nr, dead, 4 + (r >> 2), 1, (ct4 - 16) * 256);
  rg_task(p, rs, wk);
  for (int rnd = 0; rnd < 2; ++rnd) {
    const int ct = rnd ? x8 * 2 + (r & 1) : ct4;
    const int blk = rnd ? (r >> 3) * 8 + 2 * ((r >> 1) & 3) : 8 + 2 * (r >> 2);
    if (rnd || ct < 16) proj_big<M_K>(p, L, rs, nr, blk, rnd ? 2 : 0, ct * 256); else proj_big<M_V>(p, L, rs, nr, blk, 1, (ct - 16) * 256);
    if (rnd == 0 && r < 7) { mlp1_task(p, L, rs, r * 8 + x8, dead); mlp2_task(p, L, rs, r * 8 + x8, dead); }
  }
  if (r < 8) {
    const int blk = x8, m = r;
    for (int i = 0; i < 2; ++i) { const int t = (i * 8 + m) * 8 + w; attn_task(p, rs, nr, blk * 4 + (t >> 5), t & 31, dead); }
    wgbar();
    if (threadIdx.x == 0) __hip_atomic_store(ctrl + W_T2 + (8 * blk + m) * LN, 1u, RLX_AGENT);
    oproj_task(p, L, rs, nr, blk, m, dead);
  } else {
    const int sid = x8 * 8 + (r - 8);
    stream_role(p, L, rs, nr, sid >> 5, sid & 31, dead);
  }
}

__global__ void __launch_bounds__(512) rmt_init_kernel(u8* ws, const u16* mem0) {
  const int tid = threadIdx.x, b = blockIdx.x;
  const Near n = near_init(ws);
  if (b >= 11) {
    arm_share(__builtin_amdgcn_make_buffer_rsrc(ws, 0, WS_END, 0x00020000), n.al, b - 11, tid);
  } else if (b >= 9) {
    const int i = (b - 9) * 512 + tid, cb = i >> 3, row = i & 7;
    const u16* m0 = mem0 + row * DM + (cb >> 2) * 32 + (cb & 3) * 4;
    const v2u lo = *(const GAS v2u*)m0, hi = *(const GAS v2u*)(m0 + 16);
    const v4u v = {canon(__builtin_amdgcn_perm(hi.x, lo.x, 0x05040100u)), canon(__builtin_amdgcn_perm(hi.x, lo.x, 0x07060302u)),
                   canon(__builtin_amdgcn_perm(hi.y, lo.y, 0x05040100u)), canon(__builtin_amdgcn_perm(hi.y, lo.y, 0x07060302u))};
    u8* d = ws + n.al + OFF_MEM + (cb >> 6) * 16384 + (cb & 63) * 128 + row * 16;
    *(GAS v4u*)d = v; *(GAS v4u*)(d + 8192) = v;
  } else if (b == 8) {
    for (int i = tid; i < (int)(CTRL_BYTES / 16); i += 512)
      if ((i >> 3) != W_CFG / LN) ((GAS v4u*)ws)[i] = v4u{0u, 0u, 0u, 0u};
  } else {
    __shared__ int best[8];
    if ((tid & 63) == 0) {
      gu32* a = (gu32*)(ws + n.al + OFF_CAL) + ((tid >> 6) & 1) * 2048 + b * 256 + (tid >> 7) * 32;
      unsigned long long m = ~0ull;
      for (int i = 0; i < 3; ++i) {
        const unsigned long long t0 = __builtin_amdgcn_s_memtime();
        __hip_atomic_store(a, 0u, RLX_AGENT); DRAIN_VM();
        const unsigned long long t1 = __builtin_amdgcn_s_memtime();
        m = t1 - t0 < m ? t1 - t0 : m;
      }
      best[tid >> 6] = (int)(m < 100000ull ? m : 100000ull);
    }
    __syncthreads();
    if (tid == 0) {
      const int d = min(min(best[0], best[2]), min(best[4], best[6])) - min(min(best[1], best[3]), min(best[5], best[7]));
      ((GAS int*)ws)[W_CFG + b] = (xcc_id() >> 2) ? -d : d;
    }
  }
}

__global__ void __launch_bounds__(NTHR, 2) rmt_mod_kernel(Params p) {
  extern __shared__ __attribute__((aligned(16))) u8 smem[];
  LAS u8* L = (LAS u8*)smem;
  const rsrc_t rs = __builtin_amdgcn_make_buffer_rsrc(p.ws, 0, WS_END, 0x00020000);
  const int b = blockIdx.x;
  if ((b & 4) == 0) chain_role(p, L, rs, b); else worker_role(p, L, rs, b);
}

}

extern "C" void kernel_launch(void* const* d_in, const int* in_sizes, int n_in, void* d_out, int out_size, void* d_ws, size_t ws_size,
                              hipStream_t stream) {
  (void)in_sizes; (void)n_in; (void)out_size; (void)ws_size;
  Params p;
  p.x = (const u16*)d_in[0]; p.mem0 = (const u16*)d_in[1]; p.Wq = (const u16*)d_in[2]; p.Wk = (const u16*)d_in[3];
  p.Wv = (const u16*)d_in[4]; p.Wo = (const u16*)d_in[5]; p.wr = (const u16*)d_in[8]; p.wg = (const u16*)d_in[9];
  p.wu = (const u16*)d_in[10]; p.wd = (const u16*)d_in[11]; p.gw = (const u16*)d_in[12];
  p.out = (u16*)d_out; p.ws = (u8*)d_ws;
  (void)hipFuncSetAttribute((const void*)rmt_mod_kernel, hipFuncAttributeMaxDynamicSharedMemorySize, LDS_BYTES);
  rmt_init_kernel<<<11 + NCHAIN, 512, 0, stream>>>(p.ws, p.mem0);
  rmt_mod_kernel<<<NWG, NTHR, LDS_BYTES, stream>>>(p);
}
```
